```python
import torch

def _softmax(x, axis=-1):
    return torch.softmax(x, dim=axis)
def _gelu(x):
    return torch.nn.functional.gelu(x, approximate="tanh")
def _silu(x):
    return torch.nn.functional.silu(x)
def _sigmoid(x):
    return torch.sigmoid(x)
def _layernorm(x, axis=-1, eps=1e-5):
    m = x.mean(dim=axis, keepdim=True)
    v = x.var(dim=axis, keepdim=True, unbiased=False)
    return (x - m) / torch.sqrt(v + eps)
def _scatter_add(tgt, idx, src, axis=0):
    return tgt.clone().index_add_(axis, idx, src)
def _segment_sum(src, seg, n):
    out = torch.zeros((n,) + src.shape[1:], dtype=src.dtype)
    return out.index_add_(0, seg, src)
def _pad(x, pw):
    flat = tuple(v for pair in reversed(pw) for v in pair)
    return torch.nn.functional.pad(x, flat)

kernel_name = 'parallel_sum__compressed_kv_cla__dense_activation_squared_relu__decode'

def setup_inputs():

    _w = (lambda a: (lambda t: t.to(torch.bfloat16) if t.is_floating_point() else t)(torch.as_tensor(a)))
    def _A_setup():
        B, H, Sq, Sk, D, M = 4, 32, 1, 4096, 128, 512
        Dc = 4 * D
        return {
            "x": _w((lambda *s: torch.randn(*s, dtype=torch.bfloat16) * 0.05)(B, Sq, M)),
            "c_kv": _w((lambda *s: torch.randn(*s, dtype=torch.bfloat16) * 3.2)(B, Sk, Dc)),
            "w_q": _w((lambda *s: torch.randn(*s, dtype=torch.bfloat16) * 1.6)(M, H, D) * (M ** -0.5)),
            "w_uk": _w((lambda *s: torch.randn(*s, dtype=torch.bfloat16) * 1.6)(Dc, H, D) * (Dc ** -0.5)),
            "w_uv": _w((lambda *s: torch.randn(*s, dtype=torch.bfloat16) * 3.2)(Dc, H, D) * (Dc ** -0.5)),
            "w_o": _w((lambda *s: torch.randn(*s, dtype=torch.bfloat16) * 3.2)(H, D, M) * ((H * D) ** -0.5)),
        }
    _A_in = _A_setup()

    def _B_setup():
        B, S, D, F = 4, 1, 512, 1792
        return {
            "x": _w((lambda *s: torch.randn(*s, dtype=torch.bfloat16) * 0.05)(B, S, D)),
            "w1": _w((lambda *s: torch.randn(*s, dtype=torch.bfloat16) * 0.1)(D, F)),
            "b1": _w((lambda *s: torch.randn(*s, dtype=torch.bfloat16) * 0.8)(F)),
            "w2": _w((lambda *s: torch.randn(*s, dtype=torch.bfloat16) * 0.1)(F, D)),
            "b2": _w((lambda *s: torch.randn(*s, dtype=torch.bfloat16) * 12.8)(D)),
        }
    _B_in = _B_setup()

    return {
        "A_x": _A_in["x"],
        "A_c_kv": _A_in["c_kv"],
        "A_w_q": _A_in["w_q"],
        "A_w_uk": _A_in["w_uk"],
        "A_w_uv": _A_in["w_uv"],
        "A_w_o": _A_in["w_o"],
        "B_x": _B_in["x"],
        "B_w1": _B_in["w1"],
        "B_b1": _B_in["b1"],
        "B_w2": _B_in["w2"],
        "B_b2": _B_in["b2"],
    }

def reference(A_x, A_c_kv, A_w_q, A_w_uk, A_w_uv, A_w_o, B_x, B_w1, B_b1, B_w2, B_b2):
    def _A_ref(x, c_kv, w_q, w_uk, w_uv, w_o):
        B, Sq, M = x.shape[0], x.shape[1], x.shape[2]
        Sk = c_kv.shape[1]
        H, D = w_q.shape[1], w_q.shape[2]
        q = torch.einsum("bqm,mhd->bhqd", x, w_q)
        k = torch.einsum("bkc,chd->bhkd", c_kv, w_uk)
        v = torch.einsum("bkc,chd->bhkd", c_kv, w_uv)
        scores = torch.einsum("bhqd,bhkd->bhqk", q, k) * (D ** -0.5)
        off = Sk - Sq
        qi = torch.arange(Sq)[:, None]
        kj = torch.arange(Sk)[None, :]
        cmask = kj <= (qi + off)
        scores = torch.where(cmask[None, None], scores, -1e9)
        p = _softmax(scores, axis=-1)
        ctx = torch.einsum("bhqk,bhkd->bhqd", p, v)
        return torch.einsum("bhqd,hdm->bqm", ctx, w_o)

    def _B_ref(x, w1, b1, w2, b2):
        h = torch.einsum("bsd,df->bsf", x, w1) + b1
        zero = torch.zeros_like(h)
        r = torch.where(h > zero, h, zero)
        h = r * r
        out = torch.einsum("bsf,fd->bsd", h, w2) + b2
        return out

    x = A_x
    xn = _layernorm(x, axis=-1)
    return x + _A_ref(xn, A_c_kv, A_w_q, A_w_uk, A_w_uv, A_w_o) + _B_ref(xn, B_w1, B_b1, B_w2, B_b2)
```

```cpp
#include <hip/hip_runtime.h>
#include <hip/hip_bf16.h>
#define MIMIC 1
typedef unsigned short u16;
__device__ __forceinline__ float bf2f(u16 b){ return __uint_as_float(((unsigned)b)<<16); }
__device__ __forceinline__ u16 f2bf(float f){ unsigned u=__float_as_uint(f); u += 0x7fffu + ((u>>16)&1u); return (u16)(u>>16); }
__device__ __forceinline__ float rbf(float f){ return bf2f(f2bf(f)); }
constexpr int B=4,H=32,SK=4096,D=128,M=512,DC=512,F=1792;

__device__ void ln_all(const u16* x, float* xn  , float* red){
  int w=threadIdx.x>>6, l=threadIdx.x&63;
  float v[8]; float s=0;
  for(int j=0;j<8;j++){ v[j]=bf2f(x[w*M + l*8+j]); s+=v[j]; }
  for(int o=32;o>=1;o>>=1) s+=__shfl_xor(s,o);
  float mean=s/M; float q=0;
  for(int j=0;j<8;j++){ float d=v[j]-mean; q+=d*d; }
  for(int o=32;o>=1;o>>=1) q+=__shfl_xor(q,o);
  float var=q/M;
  float mb=rbf(mean), vb=rbf(var);
  float sd=rbf(sqrtf(rbf(vb+1e-5f)));
  for(int j=0;j<8;j++){ xn[w*M+l*8+j]=rbf(rbf(v[j]-mb)/sd); }
  __syncthreads();
}
__global__ void k1(const u16* x,const u16* wq,const u16* w1,const u16* b1,float* q,float* hs){
  __shared__ float xn[B*M]; __shared__ float red[4];
  ln_all(x,xn,red);
  int n=blockIdx.x*256+threadIdx.x;
  if(n<H*D){
    float a[4]={0,0,0,0};
    for(int m=0;m<M;m++){ float w=bf2f(wq[(size_t)m*H*D+n]); for(int b=0;b<4;b++) a[b]+=xn[b*M+m]*w; }
    for(int b=0;b<4;b++) q[b*H*D+n]=rbf(a[b]);
  } else if(n<H*D+F){
    int f=n-H*D; float a[4]={0,0,0,0};
    for(int m=0;m<M;m++){ float w=bf2f(w1[(size_t)m*F+f]); for(int b=0;b<4;b++) a[b]+=xn[b*M+m]*w; }
    for(int b=0;b<4;b++){ float h=rbf(rbf(a[b])+bf2f(b1[f])); float r=h>0?h:0; hs[b*F+f]=rbf(r*r); }
  }
}
__global__ void k2(const float* q,const u16* wuk,float* qabs){
  int gw=(blockIdx.x*256+threadIdx.x)>>6, l=threadIdx.x&63;
  int c=gw/H, h=gw%H;
  const u16* row=wuk+((size_t)c*H+h)*D;
  float w0=bf2f(row[2*l]), w1=bf2f(row[2*l+1]);
  for(int b=0;b<4;b++){
    float s=q[b*H*D+h*D+2*l]*w0+q[b*H*D+h*D+2*l+1]*w1;
    for(int o=32;o>=1;o>>=1) s+=__shfl_xor(s,o);
    if(l==0) qabs[(b*H+h)*DC+c]=rbf(s);
  }
}
__global__ void k3(const float* qabs,const u16* ckv,float* sc){
  int gw=(blockIdx.x*256+threadIdx.x)>>6, l=threadIdx.x&63;
  int b=gw/SK,k=gw%SK;
  float v[8]; for(int j=0;j<8;j++) v[j]=bf2f(ckv[((size_t)b*SK+k)*DC+l*8+j]);
  for(int h=0;h<H;h++){
    float s=0; for(int j=0;j<8;j++) s+=v[j]*qabs[(b*H+h)*DC+l*8+j];
    for(int o=32;o>=1;o>>=1) s+=__shfl_xor(s,o);
    s=rbf(rbf(s)*0.08838834764831845f);
    if(l==0) sc[(b*H+h)*SK+k]=s;
  }
}
__global__ void k4(float* sc){
  __shared__ float red[4];
  float* r=sc+(size_t)blockIdx.x*SK; int t=threadIdx.x;
  float m=-1e30f; for(int k=t;k<SK;k+=256) m=fmaxf(m,r[k]);
  for(int o=32;o>=1;o>>=1) m=fmaxf(m,__shfl_xor(m,o));
  if((t&63)==0) red[t>>6]=m; __syncthreads();
  m=fmaxf(fmaxf(red[0],red[1]),fmaxf(red[2],red[3])); __syncthreads();
  float s=0; for(int k=t;k<SK;k+=256){ float e=__expf(r[k]-m); r[k]=e; s+=e; }
  for(int o=32;o>=1;o>>=1) s+=__shfl_xor(s,o);
  if((t&63)==0) red[t>>6]=s; __syncthreads();
  s=red[0]+red[1]+red[2]+red[3];
  float inv=1.f/s;
  for(int k=t;k<SK;k+=256) r[k]=rbf(r[k]*inv);
}
__global__ void k5(const float* p,const u16* ckv,float* part){
  int b=blockIdx.x/64, ch=blockIdx.x%64, t=threadIdx.x;
  __shared__ float ps[H*64];
  for(int i=t;i<H*64;i+=256){ int h=i/64,k=i%64; ps[i]=p[(b*H+h)*SK+ch*64+k]; }
  __syncthreads();
  float a0[H],a1[H];
  #pragma unroll
  for(int h=0;h<H;h++){a0[h]=0;a1[h]=0;}
  for(int k=0;k<64;k++){
    const u16* row=ckv+((size_t)b*SK+ch*64+k)*DC;
    float v0=bf2f(row[2*t]),v1=bf2f(row[2*t+1]);
    #pragma unroll
    for(int h=0;h<H;h++){ float pp=ps[h*64+k]; a0[h]+=pp*v0; a1[h]+=pp*v1; }
  }
  #pragma unroll
  for(int h=0;h<H;h++){ float* o=part+(((size_t)b*64+ch)*H+h)*DC; o[2*t]=a0[h]; o[2*t+1]=a1[h]; }
}
__global__ void k6(const float* part,const u16* wuv,float* ctx){
  int b=blockIdx.x/H,h=blockIdx.x%H,t=threadIdx.x;
  __shared__ float cl[DC]; __shared__ float r2[256];
  for(int c=t;c<DC;c+=256){ float s=0; for(int ch=0;ch<64;ch++) s+=part[(((size_t)b*64+ch)*H+h)*DC+c]; cl[c]=s; }
  __syncthreads();
  int d=t&127, half=t>>7; float s=0;
  for(int c=half*256;c<half*256+256;c++) s+=cl[c]*bf2f(wuv[((size_t)c*H+h)*D+d]);
  r2[t]=s; __syncthreads();
  if(t<128) ctx[(b*H+h)*D+t]=rbf(r2[t]+r2[t+128]);
}
__global__ void k7(const u16* x,const float* ctx,const u16* wo,const float* hs,const u16* w2,const u16* b2,u16* out){
  int b=blockIdx.x/8, m=(blockIdx.x%8)*64+(threadIdx.x&63), w=threadIdx.x>>6;
  __shared__ float ra[256], rb_[256];
  float s=0; for(int k=w*1024;k<w*1024+1024;k++) s+=ctx[b*H*D+k]*bf2f(wo[(size_t)k*M+m]);
  float s2=0; for(int f=w*448;f<w*448+448;f++) s2+=hs[b*F+f]*bf2f(w2[(size_t)f*M+m]);
  ra[threadIdx.x]=s; rb_[threadIdx.x]=s2; __syncthreads();
  if(w==0){ int l=threadIdx.x; float a=rbf(ra[l]+ra[l+64]+ra[l+128]+ra[l+192]); float mm=rbf(rbf(rb_[l]+rb_[l+64]+rb_[l+128]+rb_[l+192])+bf2f(b2[m]));
    out[b*M+m]=f2bf(rbf(bf2f(x[b*M+m])+a)+mm); }
}
extern "C" void kernel_launch(void* const* d_in, const int* in_sizes, int n_in, void* d_out, int out_size, void* d_ws, size_t ws_size, hipStream_t stream){
  const u16* x=(const u16*)d_in[0]; const u16* ckv=(const u16*)d_in[1]; const u16* wq=(const u16*)d_in[2]; const u16* wuk=(const u16*)d_in[3];
  const u16* wuv=(const u16*)d_in[4]; const u16* wo=(const u16*)d_in[5]; const u16* w1=(const u16*)d_in[7]; const u16* b1=(const u16*)d_in[8];
  const u16* w2=(const u16*)d_in[9]; const u16* b2=(const u16*)d_in[10];
  float* ws=(float*)d_ws;
  float* q=ws; float* hs=q+B*H*D; float* qabs=hs+B*2048; float* sc=qabs+B*H*DC; float* ctx=sc+B*H*SK; float* part=ctx+B*H*D;
  k1<<<(H*D+F+255)/256,256,0,stream>>>(x,wq,w1,b1,q,hs);
  k2<<<DC*H/4,256,0,stream>>>(q,wuk,qabs);
  k3<<<B*SK/4,256,0,stream>>>(qabs,ckv,sc);
  k4<<<B*H,256,0,stream>>>(sc);
  k5<<<B*64,256,0,stream>>>(sc,ckv,part);
  k6<<<B*H,256,0,stream>>>(part,wuv,ctx);
  k7<<<B*8,256,0,stream>>>(x,ctx,wo,hs,w2,b2,(u16*)d_out);
}
```

```cpp
#include <hip/hip_runtime.h>
#include <cstring>
typedef unsigned short u16; typedef unsigned u32; typedef unsigned long long u64;
#define GAS __attribute__((address_space(1)))
#define LAS __attribute__((address_space(3)))
typedef u32 v4u __attribute__((ext_vector_type(4)));
typedef u32 v2u __attribute__((ext_vector_type(2)));
typedef float f32x2 __attribute__((ext_vector_type(2)));
typedef float f32x4 __attribute__((ext_vector_type(4)));
typedef float f32x16 __attribute__((ext_vector_type(16)));
typedef __bf16 bf16x8 __attribute__((ext_vector_type(8)));
typedef __bf16 bf16x2 __attribute__((ext_vector_type(2)));
typedef short s16x4 __attribute__((ext_vector_type(4)));
typedef GAS u64 gu64;
typedef LAS unsigned char lds8;
#define RLX_AGENT __ATOMIC_RELAXED, __HIP_MEMORY_SCOPE_AGENT
#define DI __device__ __forceinline__

constexpr int NB=4, NH=32, SK=4096, HD=128, DM=512, DC=512, FF=1792;
constexpr int WS_EPOCH=0, WS_FLAG1=8192, WS_FLAG2=16384, WS_Q1=32768, WS_CX=WS_Q1+262144, WS_HM=WS_CX+262144, WS_QA=WS_HM+262144, WS_LSE=WS_QA+131072, WS_PO=WS_LSE+32768,
              WS_OUT=WS_PO+(4<<20), WS_END=WS_OUT+(2<<20), WS_STAMP=WS_END;
constexpr int L_CKV=0, L_QAS=131072, L_P=L_QAS, L_MX=L_QAS+8192, L_LS=L_QAS+8704;
constexpr int L_WQ=0, L_WUK=32768, L_WUV=65536, L_WO=98304, L_W2=131072, L_W1=L_WQ, L_XN=147456, L_QS=152064, L_HP=153088, L_CL=155136, L_H2=153088, L_RED=157184, L_EP=157696, L_STF=158720, L_QL=159232, L_WG=L_QL, L_GO=L_QAS+16384;
constexpr int LDS_BYTES=163840;
constexpr u32 SENT=0xFFFFFFFFu;
constexpr int SPIN_CAP=1<<20;
constexpr int AUX_NT=2, AUX_SC1=16;
#define CKV_INFL 16
#define SLP 8
#define GUN_MASK 3
#define D_E3F 40
#define D_E3A 70
#define D_E4 60
#define XOFF ((w&3)*1024+16*lane)
#define SLP_E1 2
#define D_E1 6
#define TW_INFL 6
#define D_E3B 0
#define CL_INFL 1
#define T_CLR 390
#define R_GO 31
#define T_GUN 600
#define SLP_GUN 4
#define SLP_QA 2
#define SLP_E4 0
#define T_WUK 60
#define T_CKV 120
#define T_TAIL 450
#define T_FLAG1 230
#define T_GATHER 330
#define STAMP(k) do{}while(0)

DI u64 rt(){ return __builtin_amdgcn_s_memrealtime(); }
DI u64 ct(){ u64 t; asm volatile("s_memtime %0\n\ts_waitcnt lgkmcnt(0)" : "=s"(t) :: "memory"); return t; }
DI __amdgpu_buffer_rsrc_t mkrs(const void* p, int bytes){ return __builtin_amdgcn_make_buffer_rsrc((void*)p,0,bytes,0x00020000); }
template<int AUX> DI v4u ld128(__amdgpu_buffer_rsrc_t rs,int off){ return __builtin_amdgcn_raw_buffer_load_b128(rs,off,0,AUX); }
template<int AUX> DI u32 ld32(__amdgpu_buffer_rsrc_t rs,int off){ return __builtin_amdgcn_raw_buffer_load_b32(rs,off,0,AUX); }
DI void st128(__amdgpu_buffer_rsrc_t rs,int off,v4u v){ __builtin_amdgcn_raw_buffer_store_b128(v,rs,off,0,AUX_SC1); }
DI void st32(__amdgpu_buffer_rsrc_t rs,int off,u32 v){ __builtin_amdgcn_raw_buffer_store_b32(v,rs,off,0,AUX_SC1); }
DI f32x4 mfma16(v4u a,v4u b,f32x4 c){ return __builtin_amdgcn_mfma_f32_16x16x32_bf16(__builtin_bit_cast(bf16x8,a),__builtin_bit_cast(bf16x8,b),c,0,0,0); }
DI f32x16 mfma32(v4u a,v4u b,f32x16 c){ return __builtin_amdgcn_mfma_f32_32x32x16_bf16(__builtin_bit_cast(bf16x8,a),__builtin_bit_cast(bf16x8,b),c,0,0,0); }
DI v2u trd(lds8* p){ return __builtin_bit_cast(v2u,__builtin_amdgcn_ds_read_tr16_b64_v4i16((LAS s16x4*)p)); }
DI v4u trd2(lds8* p0,lds8* p1){ v2u a=trd(p0), b=trd(p1); return v4u{a.x,a.y,b.x,b.y}; }
DI v4u lrd128(lds8* p){ return *(LAS v4u*)p; }
DI u32 pk2(float lo,float hi){ f32x2 f={lo,hi}; return __builtin_bit_cast(u32,__builtin_convertvector(f,bf16x2)); }
DI float rbf(float x){ return __uint_as_float(pk2(x,0.f)<<16); }
DI float blo(u32 w){ return __uint_as_float(w<<16); }
DI float bhi(u32 w){ return __uint_as_float(w&0xFFFF0000u); }
DI u32 nos(u32 w){ return w<0xFFFFFFFEu? w:0xFFFFFFFEu; }
DI v4u nos4(v4u v){ return v4u{nos(v.x),nos(v.y),nos(v.z),nos(v.w)}; }
DI bool ok4(v4u v){ return (v.x!=SENT)&(v.y!=SENT)&(v.z!=SENT)&(v.w!=SENT); }
#define BAR() asm volatile("s_waitcnt lgkmcnt(0)\n\ts_barrier" ::: "memory")
#define VMW(n) asm volatile("s_waitcnt vmcnt(%0)" :: "i"(n) : "memory")
#define FENCE() asm volatile("" ::: "memory")
#define SB() __builtin_amdgcn_sched_barrier(0)
template<int CTRL> DI float dpp(float x){ return __builtin_bit_cast(float,__builtin_amdgcn_mov_dpp(__builtin_bit_cast(int,x),CTRL,0xf,0xf,true)); }
constexpr int DXOR1=0xB1, DXOR2=0x4E, DHMIR=0x141, DROR8=0x128, DROR4=0x124;
DI float sw32add(float a,float b){ auto s=__builtin_amdgcn_permlane32_swap(__float_as_uint(a),__float_as_uint(b),false,false); return __uint_as_float(s[0])+__uint_as_float(s[1]); }
DI float sw16add(float a,float b){ auto s=__builtin_amdgcn_permlane16_swap(__float_as_uint(a),__float_as_uint(b),false,false); return __uint_as_float(s[0])+__uint_as_float(s[1]); }
DI float sw32max(float a){ auto s=__builtin_amdgcn_permlane32_swap(__float_as_uint(a),__float_as_uint(a),false,false); return fmaxf(__uint_as_float(s[0]),__uint_as_float(s[1])); }
DI float sw16max(float a){ auto s=__builtin_amdgcn_permlane16_swap(__float_as_uint(a),__float_as_uint(a),false,false); return fmaxf(__uint_as_float(s[0]),__uint_as_float(s[1])); }
DI float wave_sum(float v){ v+=dpp<DXOR1>(v); v+=dpp<DXOR2>(v); v+=dpp<DHMIR>(v); v+=dpp<DROR8>(v); v=sw16add(v,v); v=sw32add(v,v); return v; }
DI void dma16(const void* gbase,u32 voff,u32 ldsb){
  u32 keep;
  asm volatile("s_mov_b32 %0, m0\n\ts_mov_b32 m0, %3\n\ts_nop 0\n\tglobal_load_lds_dwordx4 %1, %2 nt\n\ts_mov_b32 m0, %0" : "=&s"(keep) : "v"(voff),"s"(gbase),"s"(ldsb) : "memory");
}
DI constexpr int fsw(int row){ return ((row&3)<<2)|((row>>2)&3); }
DI void wait_flags128(__amdgpu_buffer_rsrc_t rs,int off,u64 want,int lane){
  for(int sp=0;sp<SPIN_CAP;++sp){ FENCE();
    v4u f=ld128<AUX_SC1>(rs,off+lane*16);
    bool ok=(f.x==(u32)want)&(f.y==(u32)(want>>32))&(f.z==(u32)want)&(f.w==(u32)(want>>32));
    if(__all(ok)) break; __builtin_amdgcn_s_sleep(SLP);
  }
  FENCE();
}

__global__ void __launch_bounds__(512,2) mla_mlp_kernel(const u16* __restrict__ x,const u16* __restrict__ ckv,const u16* __restrict__ wq,const u16* __restrict__ wuk,
    const u16* __restrict__ wuv,const u16* __restrict__ wo,const u16* __restrict__ w1,const u16* __restrict__ b1,const u16* __restrict__ w2,const u16* __restrict__ b2,
    u16* __restrict__ out,unsigned char* __restrict__ ws){
  extern __shared__ __attribute__((aligned(16))) unsigned char smem[];
  lds8* L=(lds8*)smem;
  const u32 Lb=(u32)(size_t)L;
  const int tid=threadIdx.x, lane=tid&63, w=__builtin_amdgcn_readfirstlane(tid>>6);
  const int wg=blockIdx.x, xc=wg&7, j=wg>>3, jj=j&15;
  const bool isF=j<16;
  const int fi=xc*16+jj, th=fi>>2, tq=fi&3;
  const int g=lane>>4, q4=(lane&15)>>2, p4=lane&3, hh=lane>>5, l31=lane&31;
  const __amdgpu_buffer_rsrc_t rws=mkrs(ws,WS_END);
  const u64 t_start=rt();
  asm volatile("" :: "s"(x),"s"(ckv),"s"(wq),"s"(wuk),"s"(ws));
  if(isF){
    const bool mlp = fi<FF/16;
    if(w==7) st128(rws,WS_QA+g*32768+th*1024+tq*256+(lane&15)*16,v4u{SENT,SENT,SENT,SENT});
    const u32 rowoff=(u32)((128*tq+16*w+g)*8192+th*256);
#define DMA_T(p,dst,i) dma16(p,rowoff+(u32)(4*(i)*8192)+16*((lane&15)^(2*(g|(((i)>>1)<<2)))),Lb+(dst)+(16*w+4*(i))*256)
    #pragma unroll
    for(int i=0;i<4;i++) DMA_T(wq,L_WQ,i);
    v4u xr={0,0,0,0};
    if(w<4) asm volatile("global_load_dwordx4 %0, %1, %2" : "+v"(xr) : "v"(w*1024+16*lane), "s"(x) : "memory");
    if(w==7) asm volatile("global_load_dwordx4 %0, %1, %2" : "+v"(xr) : "v"(0), "s"(ws) : "memory");
    while(rt()-t_start<T_WUK) __builtin_amdgcn_s_sleep(1);
    #pragma unroll
    for(int i=0;i<4;i++) dma16(wuk,rowoff+(u32)(4*i*8192)+16*((lane&15)^(4*i+g)),Lb+L_WUK+(16*w+4*i)*256);
    asm volatile("s_waitcnt vmcnt(4)" : "+v"(xr) :: "memory");
    if(w==7){ const u64 wn=(((u64)xr.y<<32)|xr.x)+1; if(lane<8) __hip_atomic_store((gu64*)(ws+WS_FLAG1)+lane*128+fi,wn,RLX_AGENT);
      if(lane==0) *(LAS u64*)(L+L_EP)=wn;
    }
    if(w<4){
      float v[8]={blo(xr.x),bhi(xr.x),blo(xr.y),bhi(xr.y),blo(xr.z),bhi(xr.z),blo(xr.w),bhi(xr.w)};
      float s=((v[0]+v[1])+(v[2]+v[3]))+((v[4]+v[5])+(v[6]+v[7]));
      const float mean=wave_sum(s)*(1.f/DM);
      float qq=0.f;
      #pragma unroll
      for(int i=0;i<8;i++){ float d=v[i]-mean; qq+=d*d; }
      const float var=wave_sum(qq)*(1.f/DM);
      const float mb=rbf(mean), isd=__builtin_amdgcn_rcpf(rbf(__builtin_amdgcn_sqrtf(rbf(rbf(var)+1e-5f))));
      float y[8];
      #pragma unroll
      for(int i=0;i<8;i++) y[i]=rbf(v[i]-mb)*isd;
      *(LAS v4u*)(L+L_XN+w*1088+16*lane)=v4u{pk2(y[0],y[1]),pk2(y[2],y[3]),pk2(y[4],y[5]),pk2(y[6],y[7])};
    }
    BAR();
    STAMP(1);
    const int troff=(8*g+q4)*256+(w^(q4|((g&1)<<2)))*32+p4*8;
    u32 tag; u64 want;
    {
      v4u aq[4], bq[4];
      #pragma unroll
      for(int ks=0;ks<4;ks++){ lds8* a=L+L_WQ+ks*8192+troff; aq[ks]=trd2(a,a+1024); bq[ks]=lrd128(L+L_XN+p4*1088+(128*tq+32*ks+8*g)*2); }
      const v2u e=*(LAS v2u*)(L+L_EP);
      SB();
      f32x4 acc={0,0,0,0};
      #pragma unroll
      for(int ks=0;ks<4;ks++) acc=mfma16(aq[ks],bq[ks],acc);
      tag=e.x;
      if((lane&15)<4){ const u32 lo=pk2(acc[0],acc[1]), hi=pk2(acc[2],acc[3]);
        st128(rws,WS_Q1+fi*2048+w*256+g*64+(lane&3)*16,v4u{lo,tag,hi,tag});
        *(LAS v2u*)(L+L_QL+tq*1024+(w>>1)*256+g*64+p4*16+(w&1)*8)=v2u{lo,hi}; }
      want=((u64)__builtin_amdgcn_readfirstlane(e.y)<<32)|(u32)__builtin_amdgcn_readfirstlane(e.x);
    }
    STAMP(2);
    VMW(1);
    v4u auk[4];
    #pragma unroll
    for(int k=0;k<4;k++){
      lds8* a=L+L_WUK+(16*w+(lane&15))*256+8*(g&1);
      const v2u lo=*(LAS v2u*)(a+16*((4*k+(g>>1))^(lane&15))), hi=*(LAS v2u*)(a+16*((4*k+2+(g>>1))^(lane&15)));
      auk[k]=v4u{lo.x,lo.y,hi.x,hi.y};
    }
    SB();
    if(w<6){
      const int pr=(tq+1+(w>>1))&3;
      v4u qf;
      for(int sp=0;sp<SPIN_CAP;++sp){ FENCE();
        qf=ld128<AUX_SC1>(rws,WS_Q1+(th*4+pr)*2048+(w&1)*1024+lane*16);
        if(__all((qf.y==tag)&(qf.w==tag))) break;
        __builtin_amdgcn_s_sleep(SLP_E1);
      }
      const int t=4*(w&1)+g;
      *(LAS v2u*)(L+L_QL+pr*1024+(t>>1)*256+(lane&15)*16+(t&1)*8)=v2u{qf.x,qf.z};
    }
    BAR();
    STAMP(3);
    {
      v4u qb[4];
      #pragma unroll
      for(int k=0;k<4;k++) qb[k]=lrd128(L+L_QL+q4*1024+k*256+g*64+p4*16);
      SB();
      f32x4 acc={0,0,0,0};
      #pragma unroll
      for(int k=0;k<4;k++) acc=mfma16(auk[k],qb[k],acc);
      #pragma unroll
      for(int r=0;r<4;r++){ acc[r]+=dpp<DROR4>(acc[r]); acc[r]+=dpp<DROR8>(acc[r]); }
      const float SC=0.08838834764831845f*1.4426950408889634f;
      if((lane&15)<4) *(LAS v2u*)(L+L_QS+(lane&15)*256+(16*w+4*g)*2)=v2u{nos(pk2(acc[0]*SC,acc[1]*SC)),nos(pk2(acc[2]*SC,acc[3]*SC))};
    }
    BAR();
    if(w==0){ v4u v=lrd128(L+L_QS+lane*16); st128(rws,WS_QA+g*32768+th*1024+tq*256+(lane&15)*16,v); }
    STAMP(4);
    while(rt()-t_start<T_TAIL) __builtin_amdgcn_s_sleep(1);
    u32 b1raw[4];
    #pragma unroll
    for(int r=0;r<4;r++) b1raw[r]=__builtin_amdgcn_raw_buffer_load_b16(mkrs(b1,FF*2),(16*fi+4*g+r)*2,0,0);
    u32 xraw=__builtin_amdgcn_raw_buffer_load_b16(mkrs(x,NB*DM*2),(p4*DM+4*fi+q4)*2,0,0);
    u32 b2raw=__builtin_amdgcn_raw_buffer_load_b16(mkrs(b2,DM*2),(4*fi+q4)*2,0,0);
    if(mlp){
      #pragma unroll
      for(int i=0;i<2;i++){ const int r=8*i+(lane>>3); dma16(w1,(u32)((128*tq+16*w+r)*(FF*2)+th*128+16*((lane&7)^(2*(((r>>1)&1)|(i<<1))))),Lb+L_W1+(16*w+8*i)*128); }
    }
    #pragma unroll
    for(int i=0;i<4;i++) DMA_T(wuv,L_WUV,i);
    #pragma unroll
    for(int i=0;i<4;i++){ const int r=8*i+w; dma16(wo,(u32)((th*128+32*tq+r)*1024+16*(lane^(2*(r&7)))),Lb+L_WO+r*1024); }
    if(mlp){
      #pragma unroll
      for(int i=0;i<2;i++){ const int r=8*i+w; dma16(w2,(u32)((16*fi+r)*1024+16*(lane^(2*(r&7)))),Lb+L_W2+r*1024); }
      VMW(10);
      BAR();
      if(w<4){
        v4u a1[4], bq[4];
        #pragma unroll
        for(int ks=0;ks<4;ks++){ lds8* a=L+L_W1+(32*ks+8*g+q4)*128+32*(w^((q4>>1)|((g&1)<<1)))+p4*8; a1[ks]=trd2(a,a+512); bq[ks]=lrd128(L+L_XN+p4*1088+(128*tq+32*ks+8*g)*2); }
        SB();
        f32x4 acc={0,0,0,0};
        #pragma unroll
        for(int ks=0;ks<4;ks++) acc=mfma16(a1[ks],bq[ks],acc);
        if((lane&15)<4){
          st128(rws,WS_HM+fi*2048+w*512+g*128+p4*16,v4u{__float_as_uint(acc[0]),tag,__float_as_uint(acc[1]),tag});
          st128(rws,WS_HM+fi*2048+w*512+g*128+64+p4*16,v4u{__float_as_uint(acc[2]),tag,__float_as_uint(acc[3]),tag}); }
      }
      if(w==4){
        v4u h0,h1;
        for(int sp=0;sp<SPIN_CAP;++sp){ FENCE();
          h0=ld128<AUX_SC1>(rws,WS_HM+(th*4+q4)*2048+tq*512+g*128+p4*16);
          h1=ld128<AUX_SC1>(rws,WS_HM+(th*4+q4)*2048+tq*512+g*128+64+p4*16);
          if(__all((h0.y==tag)&(h0.w==tag)&(h1.y==tag)&(h1.w==tag))) break;
          __builtin_amdgcn_s_sleep(SLP);
        }
        float hv[4]={__uint_as_float(h0.x),__uint_as_float(h0.z),__uint_as_float(h1.x),__uint_as_float(h1.z)};
        #pragma unroll
        for(int r=0;r<4;r++){ float sv=hv[r]; sv+=dpp<DROR4>(sv); sv+=dpp<DROR8>(sv); float h=rbf(rbf(sv)+blo(b1raw[r])); h=h>0.f?h:0.f; hv[r]=q4==0? h*h:0.f; }
        *(LAS v2u*)(L+L_H2+lane*8)=v2u{pk2(hv[0],hv[1]),pk2(hv[2],hv[3])};
      }
    }
    VMW(0);
    BAR();
    STAMP(6);
    asm volatile("" : "+v"(xraw), "+v"(b2raw));
    const float xb_res=blo(xraw)+blo(b2raw);
    f32x4 oacc[4];
    v4u auv[4], awo[4];
    {
      #pragma unroll
      for(int k=0;k<4;k++){ lds8* a=L+L_WUV+k*8192+troff; auv[k]=trd2(a,a+1024); }
      const int r0=4*g+q4;
      #pragma unroll
      for(int t=0;t<4;t++){ lds8* a=L+L_WO+r0*1024+(w>>1)*256+32*((4*(w&1)+t)^(r0&7))+p4*8; awo[t]=trd2(a,a+16384); }
      #pragma unroll
      for(int t=0;t<4;t++) oacc[t]=f32x4{0,0,0,0};
      if(mlp){
        const v2u h2=*(LAS v2u*)(L+L_H2+lane*8);
        #pragma unroll
        for(int t=0;t<4;t++){ const v2u a=trd(L+L_W2+r0*1024+(w>>1)*256+32*((4*(w&1)+t)^(r0&7))+p4*8); oacc[t]=mfma16(v4u{a.x,a.y,0,0},v4u{h2.x,h2.y,0,0},oacc[t]); }
      }
    }
    SB();
    if(w==0){
      while(rt()-t_start<T_GUN) __builtin_amdgcn_s_sleep(1);
      v2u v0,v1;
      for(int sp=0;sp<SPIN_CAP;++sp){ FENCE();
        v0=__builtin_amdgcn_raw_buffer_load_b64(rws,WS_LSE+(th>>2)*4096+lane*32+(th&3)*8,0,AUX_SC1);
        v1=__builtin_amdgcn_raw_buffer_load_b64(rws,WS_LSE+(th>>2)*4096+2048+lane*32+(th&3)*8,0,AUX_SC1);
        if(__all((v0.y==tag)&(v1.y==tag))) break; __builtin_amdgcn_s_sleep(SLP_GUN);
      }
      FENCE();
      STAMP(7);
      float l0=__uint_as_float(v0.x), l1=__uint_as_float(v1.x), m0=l0, m1=l1;
      m0=fmaxf(m0,dpp<DXOR1>(m0)); m1=fmaxf(m1,dpp<DXOR1>(m1)); m0=fmaxf(m0,dpp<DXOR2>(m0)); m1=fmaxf(m1,dpp<DXOR2>(m1));
      m0=fmaxf(m0,dpp<DHMIR>(m0)); m1=fmaxf(m1,dpp<DHMIR>(m1)); m0=fmaxf(m0,dpp<DROR8>(m0)); m1=fmaxf(m1,dpp<DROR8>(m1)); m0=sw16max(m0); m1=sw16max(m1);
      const float e0=__builtin_amdgcn_exp2f(l0-m0), e1=__builtin_amdgcn_exp2f(l1-m1);
      float s0=e0, s1=e1;
      s0+=dpp<DXOR1>(s0); s1+=dpp<DXOR1>(s1); s0+=dpp<DXOR2>(s0); s1+=dpp<DXOR2>(s1); s0+=dpp<DHMIR>(s0); s1+=dpp<DHMIR>(s1); s0+=dpp<DROR8>(s0); s1+=dpp<DROR8>(s1);
      s0=sw16add(s0,s0); s1=sw16add(s1,s1);
      *(LAS float*)(L+L_WG+hh*128+(l31&7)*16+(l31>>3)*4)=e0*__builtin_amdgcn_rcpf(s0);
      *(LAS float*)(L+L_WG+256+hh*128+(l31&7)*16+(l31>>3)*4)=e1*__builtin_amdgcn_rcpf(s1);
    } else {
    }
    BAR();
    {
      const int b=w>>1, half=w&1, slo=lane>>3, q=lane&7;
      v4u po[4];
      #pragma unroll
      for(int i=0;i<4;i++) po[i]=ld128<AUX_SC1>(rws,WS_PO+(fi*128+b*32+slo+8*i)*256+half*128+q*16);
      const f32x4 e=*(LAS f32x4*)(L+L_WG+b*128+slo*16);
      STAMP(8);
      float a[8];
      #pragma unroll
      for(int k=0;k<8;k++) a[k]=0.f;
      #pragma unroll
      for(int i=0;i<4;i++){
        a[0]+=e[i]*blo(po[i].x); a[1]+=e[i]*bhi(po[i].x); a[2]+=e[i]*blo(po[i].y); a[3]+=e[i]*bhi(po[i].y);
        a[4]+=e[i]*blo(po[i].z); a[5]+=e[i]*bhi(po[i].z); a[6]+=e[i]*blo(po[i].w); a[7]+=e[i]*bhi(po[i].w);
      }
      float c4[4], c2[2];
      #pragma unroll
      for(int k=0;k<4;k++) c4[k]=sw32add(a[k],a[k+4]);
      c2[0]=sw16add(c4[0],c4[2]); c2[1]=sw16add(c4[1],c4[3]);
      const bool b3=(lane>>3)&1;
      const float keep=b3?c2[1]:c2[0], send=b3?c2[0]:c2[1];
      const float r=keep+dpp<DROR8>(send);
      const int col=8*q+4*hh+2*((lane>>4)&1)+(b3?1:0);
      *(LAS u16*)(L+L_CL+b*320+(half*64+col)*2)=(u16)pk2(r,0.f);
    }
    BAR();
    STAMP(9);
    {
      v4u bc[4];
      #pragma unroll
      for(int k=0;k<4;k++) bc[k]=lrd128(L+L_CL+p4*320+(32*k+8*g)*2);
      SB();
      f32x4 acc={0,0,0,0};
      #pragma unroll
      for(int k=0;k<4;k++) acc=mfma16(auv[k],bc[k],acc);
      if((lane&15)<4) st128(rws,WS_CX+fi*2048+w*256+g*64+(lane&3)*16,v4u{pk2(acc[0],acc[1]),tag,pk2(acc[2],acc[3]),tag});
    }
    STAMP(10);
    __builtin_amdgcn_s_sleep(D_E3B);
    {
      v4u c0,c1;
      for(int sp=0;sp<SPIN_CAP;++sp){ FENCE();
        c0=ld128<AUX_SC1>(rws,WS_CX+(th*4+q4)*2048+(2*tq)*256+g*64+p4*16);
        c1=ld128<AUX_SC1>(rws,WS_CX+(th*4+q4)*2048+(2*tq+1)*256+g*64+p4*16);
        if(__all((c0.y==tag)&(c0.w==tag)&(c1.y==tag)&(c1.w==tag))) break;
        __builtin_amdgcn_s_sleep(SLP_E1);
      }
      STAMP(11);
      const v4u bc={c0.x,c0.z,c1.x,c1.z};
      #pragma unroll
      for(int t=0;t<4;t++) oacc[t]=mfma16(awo[t],bc,oacc[t]);
      const bool b3=(lane>>3)&1, b2_=(lane>>2)&1;
      f32x4 u[2], v;
      #pragma unroll
      for(int t=0;t<2;t++)
        #pragma unroll
        for(int r=0;r<4;r++){ const float keep=b3?oacc[t+2][r]:oacc[t][r], send=b3?oacc[t][r]:oacc[t+2][r]; u[t][r]=keep+dpp<DROR8>(send); }
      #pragma unroll
      for(int r=0;r<4;r++){ const float keep=b2_?u[1][r]:u[0][r], send=b2_?u[0][r]:u[1][r];
        v[r]=keep+__builtin_bit_cast(float,__builtin_amdgcn_ds_swizzle(__builtin_bit_cast(int,send),0x101F)); }
      st128(rws,WS_OUT+(16*w+4*q4+g)*16384+fi*128+p4*16,v4u{__float_as_uint(v[0]),tag,__float_as_uint(v[1]),tag});
      st128(rws,WS_OUT+(16*w+4*q4+g)*16384+fi*128+64+p4*16,v4u{__float_as_uint(v[2]),tag,__float_as_uint(v[3]),tag});
    }
    STAMP(12);
    { const u64 t1=rt(); while(rt()-t1<D_E4) __builtin_amdgcn_s_sleep(1); }
    v4u pv[2];
    #pragma unroll
    for(int i=0;i<2;i++) pv[i]=ld128<AUX_SC1>(rws,WS_OUT+fi*16384+i*8192+tid*16);
    for(int sp=0;sp<SPIN_CAP;++sp){
      const bool k0=(pv[0].y==tag)&(pv[0].w==tag), k1=(pv[1].y==tag)&(pv[1].w==tag);
      if(__all(k0&k1)) break;
      __builtin_amdgcn_s_sleep(SLP_E4); FENCE();
      if(!k0) pv[0]=ld128<AUX_SC1>(rws,WS_OUT+fi*16384+tid*16);
      if(!k1) pv[1]=ld128<AUX_SC1>(rws,WS_OUT+fi*16384+8192+tid*16);
    }
    STAMP(13);
    float v=sw32add(__uint_as_float(pv[0].x)+__uint_as_float(pv[1].x),__uint_as_float(pv[0].z)+__uint_as_float(pv[1].z));
    v=sw16add(v,v); v+=dpp<DROR8>(v);
    if((lane&24)==0) *(LAS float*)(L+L_RED+w*64+((2*((lane>>2)&1)+hh)*4+p4)*4)=v;
    BAR();
    if(tid<16){
      float a=0.f;
      #pragma unroll
      for(int ww=0;ww<8;ww++) a+=*(LAS float*)(L+L_RED+ww*64+tid*4);
      out[(tid&3)*DM+4*fi+(tid>>2)]=(u16)pk2(xb_res+a,0.f);
    }
    if(wg==0 && tid==0) __hip_atomic_store((gu64*)(ws+WS_EPOCH),want,RLX_AGENT);
  } else {
    const int ab=fi>>5, as=fi&31;
    const int fl=fsw(l31);
    const int blk=(lane>>4)&1, sgm=w>>1;
    v4u vf[8][2];
#define LOAD_VF() do{ _Pragma("unroll") for(int ks=0;ks<8;ks++) _Pragma("unroll") for(int t=0;t<2;t++){ \
        const int ch=8*(w&1)+4*t+2*blk+(p4>>1); \
        lds8* a0=L+L_CKV+(16*ks+8*hh+q4)*1024+256*sgm+16*(ch^((q4<<2)|(2*hh)))+8*(p4&1); \
        lds8* a1=L+L_CKV+(16*ks+8*hh+4+q4)*1024+256*sgm+16*(ch^((q4<<2)|(2*hh+1)))+8*(p4&1); \
        vf[ks][t]=trd2(a0,a1); } SB(); }while(0)
    float mx;
    u64 want=0;
    if(w>=4 || w==0){ const u64 e=*(const GAS u64*)(ws+WS_EPOCH); want=(((u64)__builtin_amdgcn_readfirstlane((u32)(e>>32))<<32)|(u32)__builtin_amdgcn_readfirstlane((u32)e))+1; }
    if(tid<4) *(volatile LAS u32*)(L+L_QAS+tid*8192)=0;
    BAR();
    if(w<4){
      const u16* gb=ckv+((size_t)ab*SK+as*128+32*w)*DC;
      while(rt()-t_start<T_CKV) __builtin_amdgcn_s_sleep(1);
      #pragma unroll
      for(int r=0;r<32;r++){
        if(r>=CKV_INFL) VMW(CKV_INFL-1);
        dma16(gb+r*DC,(u32)((lane*16)^(16*fsw(r))),Lb+L_CKV+(32*w+r)*1024);
        if(r==R_GO && w==0 && lane<4) *(volatile LAS u32*)(L+L_QAS+lane*8192)=1;
      }
      VMW(0);
      v4u af[32];
      #pragma unroll
      for(int k=0;k<32;k++) af[k]=lrd128(L+L_CKV+(32*w+l31)*1024+256*(k>>3)+16*((2*(k&7)+hh)^fl));
      BAR();
      f32x16 sacc;
      #pragma unroll
      for(int i=0;i<16;i++) sacc[i]=0.f;
      v4u bf[12];
      #pragma unroll
      for(int k=0;k<12;k++) bf[k]=lrd128(L+L_QAS+l31*1024+256*(k>>3)+16*((2*(k&7)+hh)^fl));
      SB();
      #pragma unroll
      for(int k=0;k<32;k++){
        sacc=mfma32(af[k],bf[k%12],sacc);
        if(k+12<32) bf[k%12]=lrd128(L+L_QAS+l31*1024+256*((k+12)>>3)+16*((2*((k+12)&7)+hh)^fl));
        SB();
      }
      float mloc=sacc[0];
      #pragma unroll
      for(int i=1;i<16;i++) mloc=fmaxf(mloc,sacc[i]);
      mloc=sw32max(mloc);
      BAR();
      STAMP(4);
      if(lane<32) *(LAS float*)(L+L_MX+w*128+lane*4)=mloc;
      BAR();
      mx=fmaxf(fmaxf(*(LAS float*)(L+L_MX+l31*4),*(LAS float*)(L+L_MX+128+l31*4)),fmaxf(*(LAS float*)(L+L_MX+256+l31*4),*(LAS float*)(L+L_MX+384+l31*4)));
      float ls=0.f;
      #pragma unroll
      for(int a=0;a<4;a++){
        float p0=__builtin_amdgcn_exp2f(sacc[4*a]-mx), p1=__builtin_amdgcn_exp2f(sacc[4*a+1]-mx), p2=__builtin_amdgcn_exp2f(sacc[4*a+2]-mx), p3=__builtin_amdgcn_exp2f(sacc[4*a+3]-mx);
        const u32 u0=pk2(p0,p1), u1=pk2(p2,p3);
        ls+=(blo(u0)+bhi(u0))+(blo(u1)+bhi(u1));
        *(LAS v2u*)(L+L_P+l31*256+16*((4*w+a)^fl)+8*hh)=v2u{u0,u1};
      }
      ls=sw32add(ls,ls);
      if(lane<32) *(LAS float*)(L+L_LS+w*128+lane*4)=ls;
      BAR();
      STAMP(5);
      LOAD_VF();
    } else {
      while(rt()-t_start<T_FLAG1) __builtin_amdgcn_s_sleep(1);
      for(int sp=0;sp<SPIN_CAP;++sp){ FENCE();
        v4u f=ld128<AUX_SC1>(rws,WS_FLAG1+xc*1024+(w-4)*256+(lane&15)*16);
        if(__all((f.x==(u32)want)&(f.y==(u32)(want>>32))&(f.z==(u32)want)&(f.w==(u32)(want>>32)))) break; __builtin_amdgcn_s_sleep(SLP);
      }
      FENCE();
      for(int sp=0;sp<SPIN_CAP && *(volatile LAS u32*)(L+L_QAS+(w-4)*8192)==0;++sp) __builtin_amdgcn_s_sleep(1);
      const int wv=w-4;
      v4u qa[8];
      #pragma unroll
      for(int i=0;i<8;i++) qa[i]=ld128<AUX_SC1>(rws,WS_QA+ab*32768+(8*wv+i)*1024+16*lane);
      for(int sp=0;sp<SPIN_CAP;++sp){
        u32 bad=0;
        #pragma unroll
        for(int i=0;i<8;i++) bad|=(__all(ok4(qa[i]))?0u:1u)<<i;
        if(!bad) break;
        __builtin_amdgcn_s_sleep(SLP_QA); FENCE();
        #pragma unroll
        for(int i=0;i<8;i++) if((bad>>i)&1) qa[i]=ld128<AUX_SC1>(rws,WS_QA+ab*32768+(8*wv+i)*1024+16*lane);
      }
      #pragma unroll
      for(int i=0;i<8;i++){ const int head=8*wv+i; *(LAS v4u*)(L+L_QAS+head*1024+((lane*16)^(16*fsw(head&15))))=qa[i]; }
      BAR();
      LOAD_VF();
      BAR();
      BAR();
      mx=0.f;
      BAR();
    }
    {
      v4u pb[8];
      #pragma unroll
      for(int ks=0;ks<8;ks++) pb[ks]=lrd128(L+L_P+l31*256+16*((2*ks+hh)^fl));
      const float lsum=(*(LAS float*)(L+L_LS+l31*4)+*(LAS float*)(L+L_LS+128+l31*4))+(*(LAS float*)(L+L_LS+256+l31*4)+*(LAS float*)(L+L_LS+384+l31*4));
      SB();
      f32x16 o[2];
      #pragma unroll
      for(int i=0;i<16;i++){ o[0][i]=0.f; o[1][i]=0.f; }
      #pragma unroll
      for(int t=0;t<2;t++)
        #pragma unroll
        for(int ks=0;ks<8;ks++) o[t]=mfma32(vf[ks][t],pb[ks],o[t]);
      const float inv=__builtin_amdgcn_rcpf(lsum);
      const float lse=mx+__builtin_amdgcn_logf(lsum);
      STAMP(6);
      #pragma unroll
      for(int t=0;t<2;t++)
        #pragma unroll
        for(int a=0;a<4;a++)
          *(LAS v2u*)(L+L_CKV+l31*1024+256*sgm+16*((8*(w&1)+4*t+a)^fl)+8*hh)=v2u{pk2(o[t][4*a]*inv,o[t][4*a+1]*inv),pk2(o[t][4*a+2]*inv,o[t][4*a+3]*inv)};
      v4u ov[4];
      #pragma unroll
      for(int i=0;i<4;i++){ const int head=8*i+(lane>>3); ov[i]=lrd128(L+L_CKV+head*1024+256*sgm+16*((8*(w&1)+(lane&7))^fsw(head))); }
      SB();
      #pragma unroll
      for(int i=0;i<4;i++){ const int head=8*i+(lane>>3); st128(rws,WS_PO+((head*4+sgm)*128+ab*32+as)*256+(w&1)*128+(lane&7)*16,ov[i]); }
      STAMP(7);
      VMW(0);
      BAR();
      if(w==0 && lane<32) __builtin_amdgcn_raw_buffer_store_b64(v2u{__float_as_uint(lse),(u32)want},rws,WS_LSE+(lane>>2)*4096+fi*32+(lane&3)*8,0,AUX_SC1);
      STAMP(8);
    }
  }
  STAMP(15);
}

extern "C" void kernel_launch(void* const* d_in,const int* in_sizes,int n_in,void* d_out,int out_size,void* d_ws,size_t ws_size,hipStream_t stream){
  (void)hipFuncSetAttribute((const void*)mla_mlp_kernel,hipFuncAttributeMaxDynamicSharedMemorySize,LDS_BYTES);
  mla_mlp_kernel<<<256,512,LDS_BYTES,stream>>>((const u16*)d_in[0],(const u16*)d_in[1],(const u16*)d_in[2],(const u16*)d_in[3],(const u16*)d_in[4],(const u16*)d_in[5],
     (const u16*)d_in[7],(const u16*)d_in[8],(const u16*)d_in[9],(const u16*)d_in[10],(u16*)d_out,(unsigned char*)d_ws);
}
```

```cpp
#include <hip/hip_runtime.h>
#include <cstring>
typedef unsigned short u16; typedef unsigned u32; typedef unsigned long long u64;
#define GAS __attribute__((address_space(1)))
#define LAS __attribute__((address_space(3)))
typedef u32 v4u __attribute__((ext_vector_type(4)));
typedef u32 v2u __attribute__((ext_vector_type(2)));
typedef float f32x2 __attribute__((ext_vector_type(2)));
typedef float f32x4 __attribute__((ext_vector_type(4)));
typedef float f32x16 __attribute__((ext_vector_type(16)));
typedef __bf16 bf16x8 __attribute__((ext_vector_type(8)));
typedef __bf16 bf16x2 __attribute__((ext_vector_type(2)));
typedef short s16x4 __attribute__((ext_vector_type(4)));
typedef GAS u64 gu64;
typedef LAS unsigned char lds8;
#define RLX_AGENT __ATOMIC_RELAXED, __HIP_MEMORY_SCOPE_AGENT
#define DI __device__ __forceinline__

constexpr int NB=4, NH=32, SK=4096, HD=128, DM=512, DC=512, FF=1792;
constexpr int WS_EPOCH=0, WS_FLAG1=8192, WS_FLAG2=16384, WS_Q1=32768, WS_CX=WS_Q1+262144, WS_HM=WS_CX+262144, WS_QA=WS_HM+262144, WS_LSE=WS_QA+131072, WS_PO=WS_LSE+32768,
              WS_OUT=WS_PO+(4<<20), WS_END=WS_OUT+(2<<20), WS_STAMP=WS_END;
constexpr int L_CKV=0, L_QAS=131072, L_P=L_QAS, L_MX=L_QAS+8192, L_LS=L_QAS+8704;
constexpr int L_WQ=0, L_WUK=32768, L_WUV=65536, L_WO=98304, L_W2=131072, L_W1=L_WQ, L_XN=147456, L_QS=152064, L_HP=153088, L_CL=155136, L_H2=153088, L_RED=157184, L_EP=157696, L_STF=158720, L_QL=159232, L_WG=L_QL, L_GO=L_QAS+16384;
constexpr int LDS_BYTES=163840;
constexpr u32 SENT=0xFFFFFFFFu;
constexpr int SPIN_CAP=1<<20;
constexpr int AUX_NT=2, AUX_SC1=16;
#define CKV_INFL 16
#define SLP 8
#define GUN_MASK 3
#define D_E3F 40
#define D_E3A 70
#define D_E4 60
#define XOFF ((w&3)*1024+16*lane)
#define SLP_E1 2
#define D_E1 6
#define TW_INFL 6
#define D_E3B 0
#define CL_INFL 1
#define T_CLR 390
#define R_GO 31
#define T_GUN 600
#define SLP_GUN 4
#define SLP_QA 2
#define SLP_E4 0
#define T_WUK 60
#define T_CKV 120
#define T_TAIL 450
#define T_FLAG1 230
#define T_GATHER 330
#define STAMP(k) do{}while(0)

DI u64 rt(){ return __builtin_amdgcn_s_memrealtime(); }
DI u64 ct(){ u64 t; asm volatile("s_memtime %0\n\ts_waitcnt lgkmcnt(0)" : "=s"(t) :: "memory"); return t; }
DI __amdgpu_buffer_rsrc_t mkrs(const void* p, int bytes){ return __builtin_amdgcn_make_buffer_rsrc((void*)p,0,bytes,0x00020000); }
template<int AUX> DI v4u ld128(__amdgpu_buffer_rsrc_t rs,int off){ return __builtin_amdgcn_raw_buffer_load_b128(rs,off,0,AUX); }
template<int AUX> DI u32 ld32(__amdgpu_buffer_rsrc_t rs,int off){ return __builtin_amdgcn_raw_buffer_load_b32(rs,off,0,AUX); }
DI void st128(__amdgpu_buffer_rsrc_t rs,int off,v4u v){ __builtin_amdgcn_raw_buffer_store_b128(v,rs,off,0,AUX_SC1); }
DI void st32(__amdgpu_buffer_rsrc_t rs,int off,u32 v){ __builtin_amdgcn_raw_buffer_store_b32(v,rs,off,0,AUX_SC1); }
DI f32x4 mfma16(v4u a,v4u b,f32x4 c){ return __builtin_amdgcn_mfma_f32_16x16x32_bf16(__builtin_bit_cast(bf16x8,a),__builtin_bit_cast(bf16x8,b),c,0,0,0); }
DI f32x16 mfma32(v4u a,v4u b,f32x16 c){ return __builtin_amdgcn_mfma_f32_32x32x16_bf16(__builtin_bit_cast(bf16x8,a),__builtin_bit_cast(bf16x8,b),c,0,0,0); }
DI v2u trd(lds8* p){ return __builtin_bit_cast(v2u,__builtin_amdgcn_ds_read_tr16_b64_v4i16((LAS s16x4*)p)); }
DI v4u trd2(lds8* p0,lds8* p1){ v2u a=trd(p0), b=trd(p1); return v4u{a.x,a.y,b.x,b.y}; }
DI v4u lrd128(lds8* p){ return *(LAS v4u*)p; }
DI u32 pk2(float lo,float hi){ f32x2 f={lo,hi}; return __builtin_bit_cast(u32,__builtin_convertvector(f,bf16x2)); }
DI float rbf(float x){ return __uint_as_float(pk2(x,0.f)<<16); }
DI float blo(u32 w){ return __uint_as_float(w<<16); }
DI float bhi(u32 w){ return __uint_as_float(w&0xFFFF0000u); }
DI u32 nos(u32 w){ return w<0xFFFFFFFEu? w:0xFFFFFFFEu; }
DI v4u nos4(v4u v){ return v4u{nos(v.x),nos(v.y),nos(v.z),nos(v.w)}; }
DI bool ok4(v4u v){ return (v.x!=SENT)&(v.y!=SENT)&(v.z!=SENT)&(v.w!=SENT); }
#define BAR() asm volatile("s_waitcnt lgkmcnt(0)\n\ts_barrier" ::: "memory")
#define VMW(n) asm volatile("s_waitcnt vmcnt(%0)" :: "i"(n) : "memory")
#define FENCE() asm volatile("" ::: "memory")
#define SB() __builtin_amdgcn_sched_barrier(0)
template<int CTRL> DI float dpp(float x){ return __builtin_bit_cast(float,__builtin_amdgcn_mov_dpp(__builtin_bit_cast(int,x),CTRL,0xf,0xf,true)); }
constexpr int DXOR1=0xB1, DXOR2=0x4E, DHMIR=0x141, DROR8=0x128, DROR4=0x124;
DI float sw32add(float a,float b){ auto s=__builtin_amdgcn_permlane32_swap(__float_as_uint(a),__float_as_uint(b),false,false); return __uint_as_float(s[0])+__uint_as_float(s[1]); }
DI float sw16add(float a,float b){ auto s=__builtin_amdgcn_permlane16_swap(__float_as_uint(a),__float_as_uint(b),false,false); return __uint_as_float(s[0])+__uint_as_float(s[1]); }
DI float sw32max(float a){ auto s=__builtin_amdgcn_permlane32_swap(__float_as_uint(a),__float_as_uint(a),false,false); return fmaxf(__uint_as_float(s[0]),__uint_as_float(s[1])); }
DI float sw16max(float a){ auto s=__builtin_amdgcn_permlane16_swap(__float_as_uint(a),__float_as_uint(a),false,false); return fmaxf(__uint_as_float(s[0]),__uint_as_float(s[1])); }
DI float wave_sum(float v){ v+=dpp<DXOR1>(v); v+=dpp<DXOR2>(v); v+=dpp<DHMIR>(v); v+=dpp<DROR8>(v); v=sw16add(v,v); v=sw32add(v,v); return v; }
DI void dma16(const void* gbase,u32 voff,u32 ldsb){
  u32 keep;
  asm volatile("s_mov_b32 %0, m0\n\ts_mov_b32 m0, %3\n\ts_nop 0\n\tglobal_load_lds_dwordx4 %1, %2 nt\n\ts_mov_b32 m0, %0" : "=&s"(keep) : "v"(voff),"s"(gbase),"s"(ldsb) : "memory");
}
DI constexpr int fsw(int row){ return ((row&3)<<2)|((row>>2)&3); }
DI void wait_flags128(__amdgpu_buffer_rsrc_t rs,int off,u64 want,int lane){
  for(int sp=0;sp<SPIN_CAP;++sp){ FENCE();
    v4u f=ld128<AUX_SC1>(rs,off+lane*16);
    bool ok=(f.x==(u32)want)&(f.y==(u32)(want>>32))&(f.z==(u32)want)&(f.w==(u32)(want>>32));
    if(__all(ok)) break; __builtin_amdgcn_s_sleep(SLP);
  }
  FENCE();
}

__global__ void __launch_bounds__(512,2) mla_mlp_kernel(const u16* __restrict__ x,const u16* __restrict__ ckv,const u16* __restrict__ wq,const u16* __restrict__ wuk,
    const u16* __restrict__ wuv,const u16* __restrict__ wo,const u16* __restrict__ w1,const u16* __restrict__ b1,const u16* __restrict__ w2,const u16* __restrict__ b2,
    u16* __restrict__ out,unsigned char* __restrict__ ws){
  extern __shared__ __attribute__((aligned(16))) unsigned char smem[];
  lds8* L=(lds8*)smem;
  const u32 Lb=(u32)(size_t)L;
  const int tid=threadIdx.x, lane=tid&63, w=__builtin_amdgcn_readfirstlane(tid>>6);
  const int wg=blockIdx.x, xc=wg&7, j=wg>>3, jj=j&15;
  const bool isF=j<16;
  const int fi=xc*16+jj, th=fi>>2, tq=fi&3;
  const int g=lane>>4, q4=(lane&15)>>2, p4=lane&3, hh=lane>>5, l31=lane&31;
  const __amdgpu_buffer_rsrc_t rws=mkrs(ws,WS_END);
  const u64 t_start=rt();
  asm volatile("" :: "s"(x),"s"(ckv),"s"(wq),"s"(wuk),"s"(ws));
  if(tid==448) __hip_atomic_store((gu64*)(ws+WS_END)+wg,t_start,RLX_AGENT);
  if(isF){
    const bool mlp = fi<FF/16;
    if(w==7) st128(rws,WS_QA+g*32768+th*1024+tq*256+(lane&15)*16,v4u{SENT,SENT,SENT,SENT});
    const u32 rowoff=(u32)((128*tq+16*w+g)*8192+th*256);
#define DMA_T(p,dst,i) dma16(p,rowoff+(u32)(4*(i)*8192)+16*((lane&15)^(2*(g|(((i)>>1)<<2)))),Lb+(dst)+(16*w+4*(i))*256)
    #pragma unroll
    for(int i=0;i<4;i++) DMA_T(wq,L_WQ,i);
    v4u xr={0,0,0,0};
    if(w<4) asm volatile("global_load_dwordx4 %0, %1, %2" : "+v"(xr) : "v"(w*1024+16*lane), "s"(x) : "memory");
    if(w==7) asm volatile("global_load_dwordx4 %0, %1, %2" : "+v"(xr) : "v"(0), "s"(ws) : "memory");
    while(rt()-t_start<T_WUK) __builtin_amdgcn_s_sleep(1);
    #pragma unroll
    for(int i=0;i<4;i++) dma16(wuk,rowoff+(u32)(4*i*8192)+16*((lane&15)^(4*i+g)),Lb+L_WUK+(16*w+4*i)*256);
    asm volatile("s_waitcnt vmcnt(4)" : "+v"(xr) :: "memory");
    if(w==7){ const u64 wn=(((u64)xr.y<<32)|xr.x)+1; if(lane<8) __hip_atomic_store((gu64*)(ws+WS_FLAG1)+lane*128+fi,wn,RLX_AGENT);
      if(lane==0) *(LAS u64*)(L+L_EP)=wn;
    }
    if(w<4){
      float v[8]={blo(xr.x),bhi(xr.x),blo(xr.y),bhi(xr.y),blo(xr.z),bhi(xr.z),blo(xr.w),bhi(xr.w)};
      float s=((v[0]+v[1])+(v[2]+v[3]))+((v[4]+v[5])+(v[6]+v[7]));
      const float mean=wave_sum(s)*(1.f/DM);
      float qq=0.f;
      #pragma unroll
      for(int i=0;i<8;i++){ float d=v[i]-mean; qq+=d*d; }
      const float var=wave_sum(qq)*(1.f/DM);
      const float mb=rbf(mean), isd=__builtin_amdgcn_rcpf(rbf(__builtin_amdgcn_sqrtf(rbf(rbf(var)+1e-5f))));
      float y[8];
      #pragma unroll
      for(int i=0;i<8;i++) y[i]=rbf(v[i]-mb)*isd;
      *(LAS v4u*)(L+L_XN+w*1088+16*lane)=v4u{pk2(y[0],y[1]),pk2(y[2],y[3]),pk2(y[4],y[5]),pk2(y[6],y[7])};
    }
    BAR();
    STAMP(1);
    const int troff=(8*g+q4)*256+(w^(q4|((g&1)<<2)))*32+p4*8;
    u32 tag; u64 want;
    {
      v4u aq[4], bq[4];
      #pragma unroll
      for(int ks=0;ks<4;ks++){ lds8* a=L+L_WQ+ks*8192+troff; aq[ks]=trd2(a,a+1024); bq[ks]=lrd128(L+L_XN+p4*1088+(128*tq+32*ks+8*g)*2); }
      const v2u e=*(LAS v2u*)(L+L_EP);
      SB();
      f32x4 acc={0,0,0,0};
      #pragma unroll
      for(int ks=0;ks<4;ks++) acc=mfma16(aq[ks],bq[ks],acc);
      tag=e.x;
      if((lane&15)<4){ const u32 lo=pk2(acc[0],acc[1]), hi=pk2(acc[2],acc[3]);
        st128(rws,WS_Q1+fi*2048+w*256+g*64+(lane&3)*16,v4u{lo,tag,hi,tag});
        *(LAS v2u*)(L+L_QL+tq*1024+(w>>1)*256+g*64+p4*16+(w&1)*8)=v2u{lo,hi}; }
      want=((u64)__builtin_amdgcn_readfirstlane(e.y)<<32)|(u32)__builtin_amdgcn_readfirstlane(e.x);
    }
    STAMP(2);
    VMW(1);
    v4u auk[4];
    #pragma unroll
    for(int k=0;k<4;k++){
      lds8* a=L+L_WUK+(16*w+(lane&15))*256+8*(g&1);
      const v2u lo=*(LAS v2u*)(a+16*((4*k+(g>>1))^(lane&15))), hi=*(LAS v2u*)(a+16*((4*k+2+(g>>1))^(lane&15)));
      auk[k]=v4u{lo.x,lo.y,hi.x,hi.y};
    }
    SB();
    if(w<6){
      const int pr=(tq+1+(w>>1))&3;
      v4u qf;
      for(int sp=0;sp<SPIN_CAP;++sp){ FENCE();
        qf=ld128<AUX_SC1>(rws,WS_Q1+(th*4+pr)*2048+(w&1)*1024+lane*16);
        if(__all((qf.y==tag)&(qf.w==tag))) break;
        __builtin_amdgcn_s_sleep(SLP_E1);
      }
      const int t=4*(w&1)+g;
      *(LAS v2u*)(L+L_QL+pr*1024+(t>>1)*256+(lane&15)*16+(t&1)*8)=v2u{qf.x,qf.z};
    }
    BAR();
    STAMP(3);
    {
      v4u qb[4];
      #pragma unroll
      for(int k=0;k<4;k++) qb[k]=lrd128(L+L_QL+q4*1024+k*256+g*64+p4*16);
      SB();
      f32x4 acc={0,0,0,0};
      #pragma unroll
      for(int k=0;k<4;k++) acc=mfma16(auk[k],qb[k],acc);
      #pragma unroll
      for(int r=0;r<4;r++){ acc[r]+=dpp<DROR4>(acc[r]); acc[r]+=dpp<DROR8>(acc[r]); }
      const float SC=0.08838834764831845f*1.4426950408889634f;
      if((lane&15)<4) *(LAS v2u*)(L+L_QS+(lane&15)*256+(16*w+4*g)*2)=v2u{nos(pk2(acc[0]*SC,acc[1]*SC)),nos(pk2(acc[2]*SC,acc[3]*SC))};
    }
    BAR();
    if(w==0){ v4u v=lrd128(L+L_QS+lane*16); st128(rws,WS_QA+g*32768+th*1024+tq*256+(lane&15)*16,v); }
    STAMP(4);
    while(rt()-t_start<T_TAIL) __builtin_amdgcn_s_sleep(1);
    u32 b1raw[4];
    #pragma unroll
    for(int r=0;r<4;r++) b1raw[r]=__builtin_amdgcn_raw_buffer_load_b16(mkrs(b1,FF*2),(16*fi+4*g+r)*2,0,0);
    u32 xraw=__builtin_amdgcn_raw_buffer_load_b16(mkrs(x,NB*DM*2),(p4*DM+4*fi+q4)*2,0,0);
    u32 b2raw=__builtin_amdgcn_raw_buffer_load_b16(mkrs(b2,DM*2),(4*fi+q4)*2,0,0);
    if(mlp){
      #pragma unroll
      for(int i=0;i<2;i++){ const int r=8*i+(lane>>3); dma16(w1,(u32)((128*tq+16*w+r)*(FF*2)+th*128+16*((lane&7)^(2*(((r>>1)&1)|(i<<1))))),Lb+L_W1+(16*w+8*i)*128); }
    }
    #pragma unroll
    for(int i=0;i<4;i++) DMA_T(wuv,L_WUV,i);
    #pragma unroll
    for(int i=0;i<4;i++){ const int r=8*i+w; dma16(wo,(u32)((th*128+32*tq+r)*1024+16*(lane^(2*(r&7)))),Lb+L_WO+r*1024); }
    if(mlp){
      #pragma unroll
      for(int i=0;i<2;i++){ const int r=8*i+w; dma16(w2,(u32)((16*fi+r)*1024+16*(lane^(2*(r&7)))),Lb+L_W2+r*1024); }
      VMW(10);
      BAR();
      if(w<4){
        v4u a1[4], bq[4];
        #pragma unroll
        for(int ks=0;ks<4;ks++){ lds8* a=L+L_W1+(32*ks+8*g+q4)*128+32*(w^((q4>>1)|((g&1)<<1)))+p4*8; a1[ks]=trd2(a,a+512); bq[ks]=lrd128(L+L_XN+p4*1088+(128*tq+32*ks+8*g)*2); }
        SB();
        f32x4 acc={0,0,0,0};
        #pragma unroll
        for(int ks=0;ks<4;ks++) acc=mfma16(a1[ks],bq[ks],acc);
        if((lane&15)<4){
          st128(rws,WS_HM+fi*2048+w*512+g*128+p4*16,v4u{__float_as_uint(acc[0]),tag,__float_as_uint(acc[1]),tag});
          st128(rws,WS_HM+fi*2048+w*512+g*128+64+p4*16,v4u{__float_as_uint(acc[2]),tag,__float_as_uint(acc[3]),tag}); }
      }
      if(w==4){
        v4u h0,h1;
        for(int sp=0;sp<SPIN_CAP;++sp){ FENCE();
          h0=ld128<AUX_SC1>(rws,WS_HM+(th*4+q4)*2048+tq*512+g*128+p4*16);
          h1=ld128<AUX_SC1>(rws,WS_HM+(th*4+q4)*2048+tq*512+g*128+64+p4*16);
          if(__all((h0.y==tag)&(h0.w==tag)&(h1.y==tag)&(h1.w==tag))) break;
          __builtin_amdgcn_s_sleep(SLP);
        }
        float hv[4]={__uint_as_float(h0.x),__uint_as_float(h0.z),__uint_as_float(h1.x),__uint_as_float(h1.z)};
        #pragma unroll
        for(int r=0;r<4;r++){ float sv=hv[r]; sv+=dpp<DROR4>(sv); sv+=dpp<DROR8>(sv); float h=rbf(rbf(sv)+blo(b1raw[r])); h=h>0.f?h:0.f; hv[r]=q4==0? h*h:0.f; }
        *(LAS v2u*)(L+L_H2+lane*8)=v2u{pk2(hv[0],hv[1]),pk2(hv[2],hv[3])};
      }
    }
    VMW(0);
    BAR();
    STAMP(6);
    asm volatile("" : "+v"(xraw), "+v"(b2raw));
    const float xb_res=blo(xraw)+blo(b2raw);
    f32x4 oacc[4];
    v4u auv[4], awo[4];
    {
      #pragma unroll
      for(int k=0;k<4;k++){ lds8* a=L+L_WUV+k*8192+troff; auv[k]=trd2(a,a+1024); }
      const int r0=4*g+q4;
      #pragma unroll
      for(int t=0;t<4;t++){ lds8* a=L+L_WO+r0*1024+(w>>1)*256+32*((4*(w&1)+t)^(r0&7))+p4*8; awo[t]=trd2(a,a+16384); }
      #pragma unroll
      for(int t=0;t<4;t++) oacc[t]=f32x4{0,0,0,0};
      if(mlp){
        const v2u h2=*(LAS v2u*)(L+L_H2+lane*8);
        #pragma unroll
        for(int t=0;t<4;t++){ const v2u a=trd(L+L_W2+r0*1024+(w>>1)*256+32*((4*(w&1)+t)^(r0&7))+p4*8); oacc[t]=mfma16(v4u{a.x,a.y,0,0},v4u{h2.x,h2.y,0,0},oacc[t]); }
      }
    }
    SB();
    if(w==0){
      while(rt()-t_start<T_GUN) __builtin_amdgcn_s_sleep(1);
      v2u v0,v1;
      for(int sp=0;sp<SPIN_CAP;++sp){ FENCE();
        v0=__builtin_amdgcn_raw_buffer_load_b64(rws,WS_LSE+(th>>2)*4096+lane*32+(th&3)*8,0,AUX_SC1);
        v1=__builtin_amdgcn_raw_buffer_load_b64(rws,WS_LSE+(th>>2)*4096+2048+lane*32+(th&3)*8,0,AUX_SC1);
        if(__all((v0.y==tag)&(v1.y==tag))) break; __builtin_amdgcn_s_sleep(SLP_GUN);
      }
      FENCE();
      STAMP(7);
      float l0=__uint_as_float(v0.x), l1=__uint_as_float(v1.x), m0=l0, m1=l1;
      m0=fmaxf(m0,dpp<DXOR1>(m0)); m1=fmaxf(m1,dpp<DXOR1>(m1)); m0=fmaxf(m0,dpp<DXOR2>(m0)); m1=fmaxf(m1,dpp<DXOR2>(m1));
      m0=fmaxf(m0,dpp<DHMIR>(m0)); m1=fmaxf(m1,dpp<DHMIR>(m1)); m0=fmaxf(m0,dpp<DROR8>(m0)); m1=fmaxf(m1,dpp<DROR8>(m1)); m0=sw16max(m0); m1=sw16max(m1);
      const float e0=__builtin_amdgcn_exp2f(l0-m0), e1=__builtin_amdgcn_exp2f(l1-m1);
      float s0=e0, s1=e1;
      s0+=dpp<DXOR1>(s0); s1+=dpp<DXOR1>(s1); s0+=dpp<DXOR2>(s0); s1+=dpp<DXOR2>(s1); s0+=dpp<DHMIR>(s0); s1+=dpp<DHMIR>(s1); s0+=dpp<DROR8>(s0); s1+=dpp<DROR8>(s1);
      s0=sw16add(s0,s0); s1=sw16add(s1,s1);
      *(LAS float*)(L+L_WG+hh*128+(l31&7)*16+(l31>>3)*4)=e0*__builtin_amdgcn_rcpf(s0);
      *(LAS float*)(L+L_WG+256+hh*128+(l31&7)*16+(l31>>3)*4)=e1*__builtin_amdgcn_rcpf(s1);
    } else {
    }
    BAR();
    {
      const int b=w>>1, half=w&1, slo=lane>>3, q=lane&7;
      v4u po[4];
      #pragma unroll
      for(int i=0;i<4;i++) po[i]=ld128<AUX_SC1>(rws,WS_PO+(fi*128+b*32+slo+8*i)*256+half*128+q*16);
      const f32x4 e=*(LAS f32x4*)(L+L_WG+b*128+slo*16);
      STAMP(8);
      float a[8];
      #pragma unroll
      for(int k=0;k<8;k++) a[k]=0.f;
      #pragma unroll
      for(int i=0;i<4;i++){
        a[0]+=e[i]*blo(po[i].x); a[1]+=e[i]*bhi(po[i].x); a[2]+=e[i]*blo(po[i].y); a[3]+=e[i]*bhi(po[i].y);
        a[4]+=e[i]*blo(po[i].z); a[5]+=e[i]*bhi(po[i].z); a[6]+=e[i]*blo(po[i].w); a[7]+=e[i]*bhi(po[i].w);
      }
      float c4[4], c2[2];
      #pragma unroll
      for(int k=0;k<4;k++) c4[k]=sw32add(a[k],a[k+4]);
      c2[0]=sw16add(c4[0],c4[2]); c2[1]=sw16add(c4[1],c4[3]);
      const bool b3=(lane>>3)&1;
      const float keep=b3?c2[1]:c2[0], send=b3?c2[0]:c2[1];
      const float r=keep+dpp<DROR8>(send);
      const int col=8*q+4*hh+2*((lane>>4)&1)+(b3?1:0);
      *(LAS u16*)(L+L_CL+b*320+(half*64+col)*2)=(u16)pk2(r,0.f);
    }
    BAR();
    STAMP(9);
    {
      v4u bc[4];
      #pragma unroll
      for(int k=0;k<4;k++) bc[k]=lrd128(L+L_CL+p4*320+(32*k+8*g)*2);
      SB();
      f32x4 acc={0,0,0,0};
      #pragma unroll
      for(int k=0;k<4;k++) acc=mfma16(auv[k],bc[k],acc);
      if((lane&15)<4) st128(rws,WS_CX+fi*2048+w*256+g*64+(lane&3)*16,v4u{pk2(acc[0],acc[1]),tag,pk2(acc[2],acc[3]),tag});
    }
    STAMP(10);
    __builtin_amdgcn_s_sleep(D_E3B);
    {
      v4u c0,c1;
      for(int sp=0;sp<SPIN_CAP;++sp){ FENCE();
        c0=ld128<AUX_SC1>(rws,WS_CX+(th*4+q4)*2048+(2*tq)*256+g*64+p4*16);
        c1=ld128<AUX_SC1>(rws,WS_CX+(th*4+q4)*2048+(2*tq+1)*256+g*64+p4*16);
        if(__all((c0.y==tag)&(c0.w==tag)&(c1.y==tag)&(c1.w==tag))) break;
        __builtin_amdgcn_s_sleep(SLP_E1);
      }
      STAMP(11);
      const v4u bc={c0.x,c0.z,c1.x,c1.z};
      #pragma unroll
      for(int t=0;t<4;t++) oacc[t]=mfma16(awo[t],bc,oacc[t]);
      const bool b3=(lane>>3)&1, b2_=(lane>>2)&1;
      f32x4 u[2], v;
      #pragma unroll
      for(int t=0;t<2;t++)
        #pragma unroll
        for(int r=0;r<4;r++){ const float keep=b3?oacc[t+2][r]:oacc[t][r], send=b3?oacc[t][r]:oacc[t+2][r]; u[t][r]=keep+dpp<DROR8>(send); }
      #pragma unroll
      for(int r=0;r<4;r++){ const float keep=b2_?u[1][r]:u[0][r], send=b2_?u[0][r]:u[1][r];
        v[r]=keep+__builtin_bit_cast(float,__builtin_amdgcn_ds_swizzle(__builtin_bit_cast(int,send),0x101F)); }
      st128(rws,WS_OUT+(16*w+4*q4+g)*16384+fi*128+p4*16,v4u{__float_as_uint(v[0]),tag,__float_as_uint(v[1]),tag});
      st128(rws,WS_OUT+(16*w+4*q4+g)*16384+fi*128+64+p4*16,v4u{__float_as_uint(v[2]),tag,__float_as_uint(v[3]),tag});
    }
    STAMP(12);
    { const u64 t1=rt(); while(rt()-t1<D_E4) __builtin_amdgcn_s_sleep(1); }
    v4u pv[2];
    #pragma unroll
    for(int i=0;i<2;i++) pv[i]=ld128<AUX_SC1>(rws,WS_OUT+fi*16384+i*8192+tid*16);
    for(int sp=0;sp<SPIN_CAP;++sp){
      const bool k0=(pv[0].y==tag)&(pv[0].w==tag), k1=(pv[1].y==tag)&(pv[1].w==tag);
      if(__all(k0&k1)) break;
      __builtin_amdgcn_s_sleep(SLP_E4); FENCE();
      if(!k0) pv[0]=ld128<AUX_SC1>(rws,WS_OUT+fi*16384+tid*16);
      if(!k1) pv[1]=ld128<AUX_SC1>(rws,WS_OUT+fi*16384+8192+tid*16);
    }
    STAMP(13);
    float v=sw32add(__uint_as_float(pv[0].x)+__uint_as_float(pv[1].x),__uint_as_float(pv[0].z)+__uint_as_float(pv[1].z));
    v=sw16add(v,v); v+=dpp<DROR8>(v);
    if((lane&24)==0) *(LAS float*)(L+L_RED+w*64+((2*((lane>>2)&1)+hh)*4+p4)*4)=v;
    BAR();
    if(tid<16){
      float a=0.f;
      #pragma unroll
      for(int ww=0;ww<8;ww++) a+=*(LAS float*)(L+L_RED+ww*64+tid*4);
      out[(tid&3)*DM+4*fi+(tid>>2)]=(u16)pk2(xb_res+a,0.f);
    }
    if(wg==0 && tid==0) __hip_atomic_store((gu64*)(ws+WS_EPOCH),want,RLX_AGENT);
    if(wg==0 && w==0){ u64 mn=~0ull,mx=0;
      for(int i=0;i<4;i++){ u64 v=__hip_atomic_load((gu64*)(ws+WS_END)+lane+64*i,RLX_AGENT); mn=v<mn?v:mn; mx=v>mx?v:mx; }
      for(int o=32;o>=1;o>>=1){ u64 a_=__shfl_xor(mn,o), b_=__shfl_xor(mx,o); mn=a_<mn?a_:mn; mx=b_>mx?b_:mx; }
      const u64 t1=rt(); const u64 dd=(u64)0; while(rt()-t1<10*dd) __builtin_amdgcn_s_sleep(1); }
  } else {
    const int ab=fi>>5, as=fi&31;
    const int fl=fsw(l31);
    const int blk=(lane>>4)&1, sgm=w>>1;
    v4u vf[8][2];
#define LOAD_VF() do{ _Pragma("unroll") for(int ks=0;ks<8;ks++) _Pragma("unroll") for(int t=0;t<2;t++){ \
        const int ch=8*(w&1)+4*t+2*blk+(p4>>1); \
        lds8* a0=L+L_CKV+(16*ks+8*hh+q4)*1024+256*sgm+16*(ch^((q4<<2)|(2*hh)))+8*(p4&1); \
        lds8* a1=L+L_CKV+(16*ks+8*hh+4+q4)*1024+256*sgm+16*(ch^((q4<<2)|(2*hh+1)))+8*(p4&1); \
        vf[ks][t]=trd2(a0,a1); } SB(); }while(0)
    float mx;
    u64 want=0;
    if(w>=4 || w==0){ const u64 e=*(const GAS u64*)(ws+WS_EPOCH); want=(((u64)__builtin_amdgcn_readfirstlane((u32)(e>>32))<<32)|(u32)__builtin_amdgcn_readfirstlane((u32)e))+1; }
    if(tid<4) *(volatile LAS u32*)(L+L_QAS+tid*8192)=0;
    BAR();
    if(w<4){
      const u16* gb=ckv+((size_t)ab*SK+as*128+32*w)*DC;
      while(rt()-t_start<T_CKV) __builtin_amdgcn_s_sleep(1);
      #pragma unroll
      for(int r=0;r<32;r++){
        if(r>=CKV_INFL) VMW(CKV_INFL-1);
        dma16(gb+r*DC,(u32)((lane*16)^(16*fsw(r))),Lb+L_CKV+(32*w+r)*1024);
        if(r==R_GO && w==0 && lane<4) *(volatile LAS u32*)(L+L_QAS+lane*8192)=1;
      }
      VMW(0);
      v4u af[32];
      #pragma unroll
      for(int k=0;k<32;k++) af[k]=lrd128(L+L_CKV+(32*w+l31)*1024+256*(k>>3)+16*((2*(k&7)+hh)^fl));
      BAR();
      f32x16 sacc;
      #pragma unroll
      for(int i=0;i<16;i++) sacc[i]=0.f;
      v4u bf[12];
      #pragma unroll
      for(int k=0;k<12;k++) bf[k]=lrd128(L+L_QAS+l31*1024+256*(k>>3)+16*((2*(k&7)+hh)^fl));
      SB();
      #pragma unroll
      for(int k=0;k<32;k++){
        sacc=mfma32(af[k],bf[k%12],sacc);
        if(k+12<32) bf[k%12]=lrd128(L+L_QAS+l31*1024+256*((k+12)>>3)+16*((2*((k+12)&7)+hh)^fl));
        SB();
      }
      float mloc=sacc[0];
      #pragma unroll
      for(int i=1;i<16;i++) mloc=fmaxf(mloc,sacc[i]);
      mloc=sw32max(mloc);
      BAR();
      STAMP(4);
      if(lane<32) *(LAS float*)(L+L_MX+w*128+lane*4)=mloc;
      BAR();
      mx=fmaxf(fmaxf(*(LAS float*)(L+L_MX+l31*4),*(LAS float*)(L+L_MX+128+l31*4)),fmaxf(*(LAS float*)(L+L_MX+256+l31*4),*(LAS float*)(L+L_MX+384+l31*4)));
      float ls=0.f;
      #pragma unroll
      for(int a=0;a<4;a++){
        float p0=__builtin_amdgcn_exp2f(sacc[4*a]-mx), p1=__builtin_amdgcn_exp2f(sacc[4*a+1]-mx), p2=__builtin_amdgcn_exp2f(sacc[4*a+2]-mx), p3=__builtin_amdgcn_exp2f(sacc[4*a+3]-mx);
        const u32 u0=pk2(p0,p1), u1=pk2(p2,p3);
        ls+=(blo(u0)+bhi(u0))+(blo(u1)+bhi(u1));
        *(LAS v2u*)(L+L_P+l31*256+16*((4*w+a)^fl)+8*hh)=v2u{u0,u1};
      }
      ls=sw32add(ls,ls);
      if(lane<32) *(LAS float*)(L+L_LS+w*128+lane*4)=ls;
      BAR();
      STAMP(5);
      LOAD_VF();
    } else {
      while(rt()-t_start<T_FLAG1) __builtin_amdgcn_s_sleep(1);
      for(int sp=0;sp<SPIN_CAP;++sp){ FENCE();
        v4u f=ld128<AUX_SC1>(rws,WS_FLAG1+xc*1024+(w-4)*256+(lane&15)*16);
        if(__all((f.x==(u32)want)&(f.y==(u32)(want>>32))&(f.z==(u32)want)&(f.w==(u32)(want>>32)))) break; __builtin_amdgcn_s_sleep(SLP);
      }
      FENCE();
      for(int sp=0;sp<SPIN_CAP && *(volatile LAS u32*)(L+L_QAS+(w-4)*8192)==0;++sp) __builtin_amdgcn_s_sleep(1);
      const int wv=w-4;
      v4u qa[8];
      #pragma unroll
      for(int i=0;i<8;i++) qa[i]=ld128<AUX_SC1>(rws,WS_QA+ab*32768+(8*wv+i)*1024+16*lane);
      for(int sp=0;sp<SPIN_CAP;++sp){
        u32 bad=0;
        #pragma unroll
        for(int i=0;i<8;i++) bad|=(__all(ok4(qa[i]))?0u:1u)<<i;
        if(!bad) break;
        __builtin_amdgcn_s_sleep(SLP_QA); FENCE();
        #pragma unroll
        for(int i=0;i<8;i++) if((bad>>i)&1) qa[i]=ld128<AUX_SC1>(rws,WS_QA+ab*32768+(8*wv+i)*1024+16*lane);
      }
      #pragma unroll
      for(int i=0;i<8;i++){ const int head=8*wv+i; *(LAS v4u*)(L+L_QAS+head*1024+((lane*16)^(16*fsw(head&15))))=qa[i]; }
      BAR();
      LOAD_VF();
      BAR();
      BAR();
      mx=0.f;
      BAR();
    }
    {
      v4u pb[8];
      #pragma unroll
      for(int ks=0;ks<8;ks++) pb[ks]=lrd128(L+L_P+l31*256+16*((2*ks+hh)^fl));
      const float lsum=(*(LAS float*)(L+L_LS+l31*4)+*(LAS float*)(L+L_LS+128+l31*4))+(*(LAS float*)(L+L_LS+256+l31*4)+*(LAS float*)(L+L_LS+384+l31*4));
      SB();
      f32x16 o[2];
      #pragma unroll
      for(int i=0;i<16;i++){ o[0][i]=0.f; o[1][i]=0.f; }
      #pragma unroll
      for(int t=0;t<2;t++)
        #pragma unroll
        for(int ks=0;ks<8;ks++) o[t]=mfma32(vf[ks][t],pb[ks],o[t]);
      const float inv=__builtin_amdgcn_rcpf(lsum);
      const float lse=mx+__builtin_amdgcn_logf(lsum);
      STAMP(6);
      #pragma unroll
      for(int t=0;t<2;t++)
        #pragma unroll
        for(int a=0;a<4;a++)
          *(LAS v2u*)(L+L_CKV+l31*1024+256*sgm+16*((8*(w&1)+4*t+a)^fl)+8*hh)=v2u{pk2(o[t][4*a]*inv,o[t][4*a+1]*inv),pk2(o[t][4*a+2]*inv,o[t][4*a+3]*inv)};
      v4u ov[4];
      #pragma unroll
      for(int i=0;i<4;i++){ const int head=8*i+(lane>>3); ov[i]=lrd128(L+L_CKV+head*1024+256*sgm+16*((8*(w&1)+(lane&7))^fsw(head))); }
      SB();
      #pragma unroll
      for(int i=0;i<4;i++){ const int head=8*i+(lane>>3); st128(rws,WS_PO+((head*4+sgm)*128+ab*32+as)*256+(w&1)*128+(lane&7)*16,ov[i]); }
      STAMP(7);
      VMW(0);
      BAR();
      if(w==0 && lane<32) __builtin_amdgcn_raw_buffer_store_b64(v2u{__float_as_uint(lse),(u32)want},rws,WS_LSE+(lane>>2)*4096+fi*32+(lane&3)*8,0,AUX_SC1);
      STAMP(8);
    }
  }
  STAMP(15);
}

extern "C" void kernel_launch(void* const* d_in,const int* in_sizes,int n_in,void* d_out,int out_size,void* d_ws,size_t ws_size,hipStream_t stream){
  (void)hipFuncSetAttribute((const void*)mla_mlp_kernel,hipFuncAttributeMaxDynamicSharedMemorySize,LDS_BYTES);
  mla_mlp_kernel<<<256,512,LDS_BYTES,stream>>>((const u16*)d_in[0],(const u16*)d_in[1],(const u16*)d_in[2],(const u16*)d_in[3],(const u16*)d_in[4],(const u16*)d_in[5],
     (const u16*)d_in[7],(const u16*)d_in[8],(const u16*)d_in[9],(const u16*)d_in[10],(u16*)d_out,(unsigned char*)d_ws);
}
```

```cpp
#include <hip/hip_runtime.h>
#include <cstring>
typedef unsigned short u16; typedef unsigned u32; typedef unsigned long long u64;
#define GAS __attribute__((address_space(1)))
#define LAS __attribute__((address_space(3)))
typedef u32 v4u __attribute__((ext_vector_type(4)));
typedef u32 v2u __attribute__((ext_vector_type(2)));
typedef float f32x2 __attribute__((ext_vector_type(2)));
typedef float f32x4 __attribute__((ext_vector_type(4)));
typedef float f32x16 __attribute__((ext_vector_type(16)));
typedef __bf16 bf16x8 __attribute__((ext_vector_type(8)));
typedef __bf16 bf16x2 __attribute__((ext_vector_type(2)));
typedef short s16x4 __attribute__((ext_vector_type(4)));
typedef GAS u64 gu64;
typedef LAS unsigned char lds8;
#define RLX_AGENT __ATOMIC_RELAXED, __HIP_MEMORY_SCOPE_AGENT
#define DI __device__ __forceinline__

constexpr int NB=4, NH=32, SK=4096, HD=128, DM=512, DC=512, FF=1792;
constexpr int WS_EPOCH=0, WS_FLAG1=8192, WS_FLAG2=16384, WS_Q1=32768, WS_CX=WS_Q1+262144, WS_HM=WS_CX+262144, WS_QA=WS_HM+262144, WS_LSE=WS_QA+131072, WS_PO=WS_LSE+32768,
              WS_OUT=WS_PO+(4<<20), WS_END=WS_OUT+(2<<20), WS_STAMP=WS_END;
constexpr int L_CKV=0, L_QAS=131072, L_P=L_QAS, L_MX=L_QAS+8192, L_LS=L_QAS+8704;
constexpr int L_WQ=0, L_WUK=32768, L_WUV=65536, L_WO=98304, L_W2=131072, L_W1=L_WQ, L_XN=147456, L_QS=152064, L_HP=153088, L_CL=155136, L_H2=153088, L_RED=157184, L_EP=157696, L_STF=158720, L_QL=159232, L_WG=L_QL, L_GO=L_QAS+16384;
constexpr int LDS_BYTES=163840;
constexpr u32 SENT=0xFFFFFFFFu;
constexpr int SPIN_CAP=1<<20;
constexpr int AUX_NT=2, AUX_SC1=16;
#define CKV_INFL 16
#define SLP 8
#define GUN_MASK 3
#define D_E3F 40
#define D_E3A 70
#define D_E4 60
#define XOFF ((w&3)*1024+16*lane)
#define SLP_E1 2
#define D_E1 6
#define TW_INFL 6
#define D_E3B 0
#define CL_INFL 1
#define T_CLR 390
#define R_GO 31
#define T_GUN 600
#define SLP_GUN 4
#define SLP_QA 2
#define SLP_E4 0
#define T_WUK 60
#define T_CKV 120
#define T_TAIL 450
#define T_FLAG1 230
#define T_GATHER 330
#define STAMP(k) do{}while(0)

DI u64 rt(){ return __builtin_amdgcn_s_memrealtime(); }
DI u64 ct(){ u64 t; asm volatile("s_memtime %0\n\ts_waitcnt lgkmcnt(0)" : "=s"(t) :: "memory"); return t; }
DI __amdgpu_buffer_rsrc_t mkrs(const void* p, int bytes){ return __builtin_amdgcn_make_buffer_rsrc((void*)p,0,bytes,0x00020000); }
template<int AUX> DI v4u ld128(__amdgpu_buffer_rsrc_t rs,int off){ return __builtin_amdgcn_raw_buffer_load_b128(rs,off,0,AUX); }
template<int AUX> DI u32 ld32(__amdgpu_buffer_rsrc_t rs,int off){ return __builtin_amdgcn_raw_buffer_load_b32(rs,off,0,AUX); }
DI void st128(__amdgpu_buffer_rsrc_t rs,int off,v4u v){ __builtin_amdgcn_raw_buffer_store_b128(v,rs,off,0,AUX_SC1); }
DI void st32(__amdgpu_buffer_rsrc_t rs,int off,u32 v){ __builtin_amdgcn_raw_buffer_store_b32(v,rs,off,0,AUX_SC1); }
DI f32x4 mfma16(v4u a,v4u b,f32x4 c){ return __builtin_amdgcn_mfma_f32_16x16x32_bf16(__builtin_bit_cast(bf16x8,a),__builtin_bit_cast(bf16x8,b),c,0,0,0); }
DI f32x16 mfma32(v4u a,v4u b,f32x16 c){ return __builtin_amdgcn_mfma_f32_32x32x16_bf16(__builtin_bit_cast(bf16x8,a),__builtin_bit_cast(bf16x8,b),c,0,0,0); }
DI v2u trd(lds8* p){ return __builtin_bit_cast(v2u,__builtin_amdgcn_ds_read_tr16_b64_v4i16((LAS s16x4*)p)); }
DI v4u trd2(lds8* p0,lds8* p1){ v2u a=trd(p0), b=trd(p1); return v4u{a.x,a.y,b.x,b.y}; }
DI v4u lrd128(lds8* p){ return *(LAS v4u*)p; }
DI u32 pk2(float lo,float hi){ f32x2 f={lo,hi}; return __builtin_bit_cast(u32,__builtin_convertvector(f,bf16x2)); }
DI float rbf(float x){ return __uint_as_float(pk2(x,0.f)<<16); }
DI float blo(u32 w){ return __uint_as_float(w<<16); }
DI float bhi(u32 w){ return __uint_as_float(w&0xFFFF0000u); }
DI u32 nos(u32 w){ return w<0xFFFFFFFEu? w:0xFFFFFFFEu; }
DI v4u nos4(v4u v){ return v4u{nos(v.x),nos(v.y),nos(v.z),nos(v.w)}; }
DI bool ok4(v4u v){ return (v.x!=SENT)&(v.y!=SENT)&(v.z!=SENT)&(v.w!=SENT); }
#define BAR() asm volatile("s_waitcnt lgkmcnt(0)\n\ts_barrier" ::: "memory")
#define VMW(n) asm volatile("s_waitcnt vmcnt(%0)" :: "i"(n) : "memory")
#define FENCE() asm volatile("" ::: "memory")
#define SB() __builtin_amdgcn_sched_barrier(0)
template<int CTRL> DI float dpp(float x){ return __builtin_bit_cast(float,__builtin_amdgcn_mov_dpp(__builtin_bit_cast(int,x),CTRL,0xf,0xf,true)); }
constexpr int DXOR1=0xB1, DXOR2=0x4E, DHMIR=0x141, DROR8=0x128, DROR4=0x124;
DI float sw32add(float a,float b){ auto s=__builtin_amdgcn_permlane32_swap(__float_as_uint(a),__float_as_uint(b),false,false); return __uint_as_float(s[0])+__uint_as_float(s[1]); }
DI float sw16add(float a,float b){ auto s=__builtin_amdgcn_permlane16_swap(__float_as_uint(a),__float_as_uint(b),false,false); return __uint_as_float(s[0])+__uint_as_float(s[1]); }
DI float sw32max(float a){ auto s=__builtin_amdgcn_permlane32_swap(__float_as_uint(a),__float_as_uint(a),false,false); return fmaxf(__uint_as_float(s[0]),__uint_as_float(s[1])); }
DI float sw16max(float a){ auto s=__builtin_amdgcn_permlane16_swap(__float_as_uint(a),__float_as_uint(a),false,false); return fmaxf(__uint_as_float(s[0]),__uint_as_float(s[1])); }
DI float wave_sum(float v){ v+=dpp<DXOR1>(v); v+=dpp<DXOR2>(v); v+=dpp<DHMIR>(v); v+=dpp<DROR8>(v); v=sw16add(v,v); v=sw32add(v,v); return v; }
DI void dma16(const void* gbase,u32 voff,u32 ldsb){
  u32 keep;
  asm volatile("s_mov_b32 %0, m0\n\ts_mov_b32 m0, %3\n\ts_nop 0\n\tglobal_load_lds_dwordx4 %1, %2 nt\n\ts_mov_b32 m0, %0" : "=&s"(keep) : "v"(voff),"s"(gbase),"s"(ldsb) : "memory");
}
DI constexpr int fsw(int row){ return ((row&3)<<2)|((row>>2)&3); }
DI void wait_flags128(__amdgpu_buffer_rsrc_t rs,int off,u64 want,int lane){
  for(int sp=0;sp<SPIN_CAP;++sp){ FENCE();
    v4u f=ld128<AUX_SC1>(rs,off+lane*16);
    bool ok=(f.x==(u32)want)&(f.y==(u32)(want>>32))&(f.z==(u32)want)&(f.w==(u32)(want>>32));
    if(__all(ok)) break; __builtin_amdgcn_s_sleep(SLP);
  }
  FENCE();
}

__global__ void __launch_bounds__(512,2) mla_mlp_kernel(const u16* __restrict__ x,const u16* __restrict__ ckv,const u16* __restrict__ wq,const u16* __restrict__ wuk,
    const u16* __restrict__ wuv,const u16* __restrict__ wo,const u16* __restrict__ w1,const u16* __restrict__ b1,const u16* __restrict__ w2,const u16* __restrict__ b2,
    u16* __restrict__ out,unsigned char* __restrict__ ws){
  extern __shared__ __attribute__((aligned(16))) unsigned char smem[];
  lds8* L=(lds8*)smem;
  const u32 Lb=(u32)(size_t)L;
  const int tid=threadIdx.x, lane=tid&63, w=__builtin_amdgcn_readfirstlane(tid>>6);
  const int wg=blockIdx.x, xc=wg&7, j=wg>>3, jj=j&15;
  const bool isF=j<16;
  const int fi=xc*16+jj, th=fi>>2, tq=fi&3;
  const int g=lane>>4, q4=(lane&15)>>2, p4=lane&3, hh=lane>>5, l31=lane&31;
  const __amdgpu_buffer_rsrc_t rws=mkrs(ws,WS_END);
  const u64 t_start=rt();
  asm volatile("" :: "s"(x),"s"(ckv),"s"(wq),"s"(wuk),"s"(ws));
  if(isF){
    const bool mlp = fi<FF/16;
    if(w==7) st128(rws,WS_QA+g*32768+th*1024+tq*256+(lane&15)*16,v4u{SENT,SENT,SENT,SENT});
    const u32 rowoff=(u32)((128*tq+16*w+g)*8192+th*256);
#define DMA_T(p,dst,i) dma16(p,rowoff+(u32)(4*(i)*8192)+16*((lane&15)^(2*(g|(((i)>>1)<<2)))),Lb+(dst)+(16*w+4*(i))*256)
    #pragma unroll
    for(int i=0;i<4;i++) DMA_T(wq,L_WQ,i);
    v4u xr={0,0,0,0};
    if(w<4) asm volatile("global_load_dwordx4 %0, %1, %2" : "+v"(xr) : "v"(w*1024+16*lane), "s"(x) : "memory");
    if(w==7) asm volatile("global_load_dwordx4 %0, %1, %2" : "+v"(xr) : "v"(0), "s"(ws) : "memory");
    while(rt()-t_start<T_WUK) __builtin_amdgcn_s_sleep(1);
    #pragma unroll
    for(int i=0;i<4;i++) dma16(wuk,rowoff+(u32)(4*i*8192)+16*((lane&15)^(4*i+g)),Lb+L_WUK+(16*w+4*i)*256);
    asm volatile("s_waitcnt vmcnt(4)" : "+v"(xr) :: "memory");
    if(w==7){ const u64 wn=(((u64)xr.y<<32)|xr.x)+1; if(lane<8) __hip_atomic_store((gu64*)(ws+WS_FLAG1)+lane*128+fi,wn,RLX_AGENT);
      if(lane==0) *(LAS u64*)(L+L_EP)=wn;
    }
    if(w<4){
      float v[8]={blo(xr.x),bhi(xr.x),blo(xr.y),bhi(xr.y),blo(xr.z),bhi(xr.z),blo(xr.w),bhi(xr.w)};
      float s=((v[0]+v[1])+(v[2]+v[3]))+((v[4]+v[5])+(v[6]+v[7]));
      const float mean=wave_sum(s)*(1.f/DM);
      float qq=0.f;
      #pragma unroll
      for(int i=0;i<8;i++){ float d=v[i]-mean; qq+=d*d; }
      const float var=wave_sum(qq)*(1.f/DM);
      const float mb=rbf(mean), isd=__builtin_amdgcn_rcpf(rbf(__builtin_amdgcn_sqrtf(rbf(rbf(var)+1e-5f))));
      float y[8];
      #pragma unroll
      for(int i=0;i<8;i++) y[i]=rbf(v[i]-mb)*isd;
      *(LAS v4u*)(L+L_XN+w*1088+16*lane)=v4u{pk2(y[0],y[1]),pk2(y[2],y[3]),pk2(y[4],y[5]),pk2(y[6],y[7])};
    }
    BAR();
    STAMP(1);
    const int troff=(8*g+q4)*256+(w^(q4|((g&1)<<2)))*32+p4*8;
    u32 tag; u64 want;
    {
      v4u aq[4], bq[4];
      #pragma unroll
      for(int ks=0;ks<4;ks++){ lds8* a=L+L_WQ+ks*8192+troff; aq[ks]=trd2(a,a+1024); bq[ks]=lrd128(L+L_XN+p4*1088+(128*tq+32*ks+8*g)*2); }
      const v2u e=*(LAS v2u*)(L+L_EP);
      SB();
      f32x4 acc={0,0,0,0};
      #pragma unroll
      for(int ks=0;ks<4;ks++) acc=mfma16(aq[ks],bq[ks],acc);
      tag=e.x;
      if((lane&15)<4){ const u32 lo=pk2(acc[0],acc[1]), hi=pk2(acc[2],acc[3]);
        st128(rws,WS_Q1+fi*2048+w*256+g*64+(lane&3)*16,v4u{lo,tag,hi,tag});
        *(LAS v2u*)(L+L_QL+tq*1024+(w>>1)*256+g*64+p4*16+(w&1)*8)=v2u{lo,hi}; }
      want=((u64)__builtin_amdgcn_readfirstlane(e.y)<<32)|(u32)__builtin_amdgcn_readfirstlane(e.x);
    }
    STAMP(2);
    VMW(1);
    v4u auk[4];
    #pragma unroll
    for(int k=0;k<4;k++){
      lds8* a=L+L_WUK+(16*w+(lane&15))*256+8*(g&1);
      const v2u lo=*(LAS v2u*)(a+16*((4*k+(g>>1))^(lane&15))), hi=*(LAS v2u*)(a+16*((4*k+2+(g>>1))^(lane&15)));
      auk[k]=v4u{lo.x,lo.y,hi.x,hi.y};
    }
    SB();
    if(w<6){
      const int pr=(tq+1+(w>>1))&3;
      v4u qf;
      for(int sp=0;sp<SPIN_CAP;++sp){ FENCE();
        qf=ld128<AUX_SC1>(rws,WS_Q1+(th*4+pr)*2048+(w&1)*1024+lane*16);
        if(__all((qf.y==tag)&(qf.w==tag))) break;
        __builtin_amdgcn_s_sleep(SLP_E1);
      }
      const int t=4*(w&1)+g;
      *(LAS v2u*)(L+L_QL+pr*1024+(t>>1)*256+(lane&15)*16+(t&1)*8)=v2u{qf.x,qf.z};
    }
    BAR();
    STAMP(3);
    {
      v4u qb[4];
      #pragma unroll
      for(int k=0;k<4;k++) qb[k]=lrd128(L+L_QL+q4*1024+k*256+g*64+p4*16);
      SB();
      f32x4 acc={0,0,0,0};
      #pragma unroll
      for(int k=0;k<4;k++) acc=mfma16(auk[k],qb[k],acc);
      #pragma unroll
      for(int r=0;r<4;r++){ acc[r]+=dpp<DROR4>(acc[r]); acc[r]+=dpp<DROR8>(acc[r]); }
      const float SC=0.08838834764831845f*1.4426950408889634f;
      if((lane&15)<4) *(LAS v2u*)(L+L_QS+(lane&15)*256+(16*w+4*g)*2)=v2u{nos(pk2(acc[0]*SC,acc[1]*SC)),nos(pk2(acc[2]*SC,acc[3]*SC))};
    }
    BAR();
    if(w==0){ v4u v=lrd128(L+L_QS+lane*16); st128(rws,WS_QA+g*32768+th*1024+tq*256+(lane&15)*16,v); }
    STAMP(4);
    while(rt()-t_start<T_TAIL) __builtin_amdgcn_s_sleep(1);
    u32 b1raw[4];
    #pragma unroll
    for(int r=0;r<4;r++) b1raw[r]=__builtin_amdgcn_raw_buffer_load_b16(mkrs(b1,FF*2),(16*fi+4*g+r)*2,0,0);
    u32 xraw=__builtin_amdgcn_raw_buffer_load_b16(mkrs(x,NB*DM*2),(p4*DM+4*fi+q4)*2,0,0);
    u32 b2raw=__builtin_amdgcn_raw_buffer_load_b16(mkrs(b2,DM*2),(4*fi+q4)*2,0,0);
    if(mlp){
      #pragma unroll
      for(int i=0;i<2;i++){ const int r=8*i+(lane>>3); dma16(w1,(u32)((128*tq+16*w+r)*(FF*2)+th*128+16*((lane&7)^(2*(((r>>1)&1)|(i<<1))))),Lb+L_W1+(16*w+8*i)*128); }
    }
    #pragma unroll
    for(int i=0;i<4;i++) DMA_T(wuv,L_WUV,i);
    #pragma unroll
    for(int i=0;i<4;i++){ const int r=8*i+w; dma16(wo,(u32)((th*128+32*tq+r)*1024+16*(lane^(2*(r&7)))),Lb+L_WO+r*1024); }
    if(mlp){
      #pragma unroll
      for(int i=0;i<2;i++){ const int r=8*i+w; dma16(w2,(u32)((16*fi+r)*1024+16*(lane^(2*(r&7)))),Lb+L_W2+r*1024); }
      VMW(10);
      BAR();
      if(w<4){
        v4u a1[4], bq[4];
        #pragma unroll
        for(int ks=0;ks<4;ks++){ lds8* a=L+L_W1+(32*ks+8*g+q4)*128+32*(w^((q4>>1)|((g&1)<<1)))+p4*8; a1[ks]=trd2(a,a+512); bq[ks]=lrd128(L+L_XN+p4*1088+(128*tq+32*ks+8*g)*2); }
        SB();
        f32x4 acc={0,0,0,0};
        #pragma unroll
        for(int ks=0;ks<4;ks++) acc=mfma16(a1[ks],bq[ks],acc);
        if((lane&15)<4){
          st128(rws,WS_HM+fi*2048+w*512+g*128+p4*16,v4u{__float_as_uint(acc[0]),tag,__float_as_uint(acc[1]),tag});
          st128(rws,WS_HM+fi*2048+w*512+g*128+64+p4*16,v4u{__float_as_uint(acc[2]),tag,__float_as_uint(acc[3]),tag}); }
      }
      if(w==4){
        v4u h0,h1;
        for(int sp=0;sp<SPIN_CAP;++sp){ FENCE();
          h0=ld128<AUX_SC1>(rws,WS_HM+(th*4+q4)*2048+tq*512+g*128+p4*16);
          h1=ld128<AUX_SC1>(rws,WS_HM+(th*4+q4)*2048+tq*512+g*128+64+p4*16);
          if(__all((h0.y==tag)&(h0.w==tag)&(h1.y==tag)&(h1.w==tag))) break;
          __builtin_amdgcn_s_sleep(SLP);
        }
        float hv[4]={__uint_as_float(h0.x),__uint_as_float(h0.z),__uint_as_float(h1.x),__uint_as_float(h1.z)};
        #pragma unroll
        for(int r=0;r<4;r++){ float sv=hv[r]; sv+=dpp<DROR4>(sv); sv+=dpp<DROR8>(sv); float h=rbf(rbf(sv)+blo(b1raw[r])); h=h>0.f?h:0.f; hv[r]=q4==0? h*h:0.f; }
        *(LAS v2u*)(L+L_H2+lane*8)=v2u{pk2(hv[0],hv[1]),pk2(hv[2],hv[3])};
      }
    }
    VMW(0);
    BAR();
    STAMP(6);
    asm volatile("" : "+v"(xraw), "+v"(b2raw));
    const float xb_res=blo(xraw)+blo(b2raw);
    f32x4 oacc[4];
    v4u auv[4], awo[4];
    {
      #pragma unroll
      for(int k=0;k<4;k++){ lds8* a=L+L_WUV+k*8192+troff; auv[k]=trd2(a,a+1024); }
      const int r0=4*g+q4;
      #pragma unroll
      for(int t=0;t<4;t++){ lds8* a=L+L_WO+r0*1024+(w>>1)*256+32*((4*(w&1)+t)^(r0&7))+p4*8; awo[t]=trd2(a,a+16384); }
      #pragma unroll
      for(int t=0;t<4;t++) oacc[t]=f32x4{0,0,0,0};
      if(mlp){
        const v2u h2=*(LAS v2u*)(L+L_H2+lane*8);
        #pragma unroll
        for(int t=0;t<4;t++){ const v2u a=trd(L+L_W2+r0*1024+(w>>1)*256+32*((4*(w&1)+t)^(r0&7))+p4*8); oacc[t]=mfma16(v4u{a.x,a.y,0,0},v4u{h2.x,h2.y,0,0},oacc[t]); }
      }
    }
    SB();
    if(w==0){
      while(rt()-t_start<T_GUN) __builtin_amdgcn_s_sleep(1);
      v2u v0,v1;
      for(int sp=0;sp<SPIN_CAP;++sp){ FENCE();
        v0=__builtin_amdgcn_raw_buffer_load_b64(rws,WS_LSE+(th>>2)*4096+lane*32+(th&3)*8,0,AUX_SC1);
        v1=__builtin_amdgcn_raw_buffer_load_b64(rws,WS_LSE+(th>>2)*4096+2048+lane*32+(th&3)*8,0,AUX_SC1);
        if(__all((v0.y==tag)&(v1.y==tag))) break; __builtin_amdgcn_s_sleep(SLP_GUN);
      }
      FENCE();
      STAMP(7);
      float l0=__uint_as_float(v0.x), l1=__uint_as_float(v1.x), m0=l0, m1=l1;
      m0=fmaxf(m0,dpp<DXOR1>(m0)); m1=fmaxf(m1,dpp<DXOR1>(m1)); m0=fmaxf(m0,dpp<DXOR2>(m0)); m1=fmaxf(m1,dpp<DXOR2>(m1));
      m0=fmaxf(m0,dpp<DHMIR>(m0)); m1=fmaxf(m1,dpp<DHMIR>(m1)); m0=fmaxf(m0,dpp<DROR8>(m0)); m1=fmaxf(m1,dpp<DROR8>(m1)); m0=sw16max(m0); m1=sw16max(m1);
      const float e0=__builtin_amdgcn_exp2f(l0-m0), e1=__builtin_amdgcn_exp2f(l1-m1);
      float s0=e0, s1=e1;
      s0+=dpp<DXOR1>(s0); s1+=dpp<DXOR1>(s1); s0+=dpp<DXOR2>(s0); s1+=dpp<DXOR2>(s1); s0+=dpp<DHMIR>(s0); s1+=dpp<DHMIR>(s1); s0+=dpp<DROR8>(s0); s1+=dpp<DROR8>(s1);
      s0=sw16add(s0,s0); s1=sw16add(s1,s1);
      *(LAS float*)(L+L_WG+hh*128+(l31&7)*16+(l31>>3)*4)=e0*__builtin_amdgcn_rcpf(s0);
      *(LAS float*)(L+L_WG+256+hh*128+(l31&7)*16+(l31>>3)*4)=e1*__builtin_amdgcn_rcpf(s1);
    } else {
    }
    BAR();
    {
      const int b=w>>1, half=w&1, slo=lane>>3, q=lane&7;
      v4u po[4];
      #pragma unroll
      for(int i=0;i<4;i++) po[i]=ld128<AUX_SC1>(rws,WS_PO+(fi*128+b*32+slo+8*i)*256+half*128+q*16);
      const f32x4 e=*(LAS f32x4*)(L+L_WG+b*128+slo*16);
      STAMP(8);
      float a[8];
      #pragma unroll
      for(int k=0;k<8;k++) a[k]=0.f;
      #pragma unroll
      for(int i=0;i<4;i++){
        a[0]+=e[i]*blo(po[i].x); a[1]+=e[i]*bhi(po[i].x); a[2]+=e[i]*blo(po[i].y); a[3]+=e[i]*bhi(po[i].y);
        a[4]+=e[i]*blo(po[i].z); a[5]+=e[i]*bhi(po[i].z); a[6]+=e[i]*blo(po[i].w); a[7]+=e[i]*bhi(po[i].w);
      }
      float c4[4], c2[2];
      #pragma unroll
      for(int k=0;k<4;k++) c4[k]=sw32add(a[k],a[k+4]);
      c2[0]=sw16add(c4[0],c4[2]); c2[1]=sw16add(c4[1],c4[3]);
      const bool b3=(lane>>3)&1;
      const float keep=b3?c2[1]:c2[0], send=b3?c2[0]:c2[1];
      const float r=keep+dpp<DROR8>(send);
      const int col=8*q+4*hh+2*((lane>>4)&1)+(b3?1:0);
      *(LAS u16*)(L+L_CL+b*320+(half*64+col)*2)=(u16)pk2(r,0.f);
    }
    BAR();
    STAMP(9);
    {
      v4u bc[4];
      #pragma unroll
      for(int k=0;k<4;k++) bc[k]=lrd128(L+L_CL+p4*320+(32*k+8*g)*2);
      SB();
      f32x4 acc={0,0,0,0};
      #pragma unroll
      for(int k=0;k<4;k++) acc=mfma16(auv[k],bc[k],acc);
      if((lane&15)<4) st128(rws,WS_CX+fi*2048+w*256+g*64+(lane&3)*16,v4u{pk2(acc[0],acc[1]),tag,pk2(acc[2],acc[3]),tag});
    }
    STAMP(10);
    __builtin_amdgcn_s_sleep(D_E3B);
    {
      v4u c0,c1;
      for(int sp=0;sp<SPIN_CAP;++sp){ FENCE();
        c0=ld128<AUX_SC1>(rws,WS_CX+(th*4+q4)*2048+(2*tq)*256+g*64+p4*16);
        c1=ld128<AUX_SC1>(rws,WS_CX+(th*4+q4)*2048+(2*tq+1)*256+g*64+p4*16);
        if(__all((c0.y==tag)&(c0.w==tag)&(c1.y==tag)&(c1.w==tag))) break;
        __builtin_amdgcn_s_sleep(SLP_E1);
      }
      STAMP(11);
      const v4u bc={c0.x,c0.z,c1.x,c1.z};
      #pragma unroll
      for(int t=0;t<4;t++) oacc[t]=mfma16(awo[t],bc,oacc[t]);
      const bool b3=(lane>>3)&1, b2_=(lane>>2)&1;
      f32x4 u[2], v;
      #pragma unroll
      for(int t=0;t<2;t++)
        #pragma unroll
        for(int r=0;r<4;r++){ const float keep=b3?oacc[t+2][r]:oacc[t][r], send=b3?oacc[t][r]:oacc[t+2][r]; u[t][r]=keep+dpp<DROR8>(send); }
      #pragma unroll
      for(int r=0;r<4;r++){ const float keep=b2_?u[1][r]:u[0][r], send=b2_?u[0][r]:u[1][r];
        v[r]=keep+__builtin_bit_cast(float,__builtin_amdgcn_ds_swizzle(__builtin_bit_cast(int,send),0x101F)); }
      st128(rws,WS_OUT+(16*w+4*q4+g)*16384+fi*128+p4*16,v4u{__float_as_uint(v[0]),tag,__float_as_uint(v[1]),tag});
      st128(rws,WS_OUT+(16*w+4*q4+g)*16384+fi*128+64+p4*16,v4u{__float_as_uint(v[2]),tag,__float_as_uint(v[3]),tag});
    }
    STAMP(12);
    { const u64 t1=rt(); while(rt()-t1<D_E4) __builtin_amdgcn_s_sleep(1); }
    v4u pv[2];
    #pragma unroll
    for(int i=0;i<2;i++) pv[i]=ld128<AUX_SC1>(rws,WS_OUT+fi*16384+i*8192+tid*16);
    for(int sp=0;sp<SPIN_CAP;++sp){
      const bool k0=(pv[0].y==tag)&(pv[0].w==tag), k1=(pv[1].y==tag)&(pv[1].w==tag);
      if(__all(k0&k1)) break;
      __builtin_amdgcn_s_sleep(SLP_E4); FENCE();
      if(!k0) pv[0]=ld128<AUX_SC1>(rws,WS_OUT+fi*16384+tid*16);
      if(!k1) pv[1]=ld128<AUX_SC1>(rws,WS_OUT+fi*16384+8192+tid*16);
    }
    STAMP(13);
    float v=sw32add(__uint_as_float(pv[0].x)+__uint_as_float(pv[1].x),__uint_as_float(pv[0].z)+__uint_as_float(pv[1].z));
    v=sw16add(v,v); v+=dpp<DROR8>(v);
    if((lane&24)==0) *(LAS float*)(L+L_RED+w*64+((2*((lane>>2)&1)+hh)*4+p4)*4)=v;
    BAR();
    if(tid<16){
      float a=0.f;
      #pragma unroll
      for(int ww=0;ww<8;ww++) a+=*(LAS float*)(L+L_RED+ww*64+tid*4);
      out[(tid&3)*DM+4*fi+(tid>>2)]=(u16)pk2(xb_res+a,0.f);
    }
    if(wg==0 && tid==0) __hip_atomic_store((gu64*)(ws+WS_EPOCH),want,RLX_AGENT);
  } else {
    const int ab=fi>>5, as=fi&31;
    const int fl=fsw(l31);
    const int blk=(lane>>4)&1, sgm=w>>1;
    v4u vf[8][2];
#define LOAD_VF() do{ _Pragma("unroll") for(int ks=0;ks<8;ks++) _Pragma("unroll") for(int t=0;t<2;t++){ \
        const int ch=8*(w&1)+4*t+2*blk+(p4>>1); \
        lds8* a0=L+L_CKV+(16*ks+8*hh+q4)*1024+256*sgm+16*(ch^((q4<<2)|(2*hh)))+8*(p4&1); \
        lds8* a1=L+L_CKV+(16*ks+8*hh+4+q4)*1024+256*sgm+16*(ch^((q4<<2)|(2*hh+1)))+8*(p4&1); \
        vf[ks][t]=trd2(a0,a1); } SB(); }while(0)
    float mx;
    u64 want=0;
    if(w>=4 || w==0){ const u64 e=*(const GAS u64*)(ws+WS_EPOCH); want=(((u64)__builtin_amdgcn_readfirstlane((u32)(e>>32))<<32)|(u32)__builtin_amdgcn_readfirstlane((u32)e))+1; }
    if(tid<4) *(volatile LAS u32*)(L+L_QAS+tid*8192)=0;
    BAR();
    if(w<4){
      const u16* gb=ckv+((size_t)ab*SK+as*128+32*w)*DC;
      while(rt()-t_start<T_CKV) __builtin_amdgcn_s_sleep(1);
      #pragma unroll
      for(int r=0;r<32;r++){
        if(r>=CKV_INFL) VMW(CKV_INFL-1);
        dma16(gb+r*DC,(u32)((lane*16)^(16*fsw(r))),Lb+L_CKV+(32*w+r)*1024);
        if(r==R_GO && w==0 && lane<4) *(volatile LAS u32*)(L+L_QAS+lane*8192)=1;
      }
      VMW(0);
      v4u af[32];
      #pragma unroll
      for(int k=0;k<32;k++) af[k]=lrd128(L+L_CKV+(32*w+l31)*1024+256*(k>>3)+16*((2*(k&7)+hh)^fl));
      BAR();
      f32x16 sacc;
      #pragma unroll
      for(int i=0;i<16;i++) sacc[i]=0.f;
      v4u bf[12];
      #pragma unroll
      for(int k=0;k<12;k++) bf[k]=lrd128(L+L_QAS+l31*1024+256*(k>>3)+16*((2*(k&7)+hh)^fl));
      SB();
      #pragma unroll
      for(int k=0;k<32;k++){
        sacc=mfma32(af[k],bf[k%12],sacc);
        if(k+12<32) bf[k%12]=lrd128(L+L_QAS+l31*1024+256*((k+12)>>3)+16*((2*((k+12)&7)+hh)^fl));
        SB();
      }
      float mloc=sacc[0];
      #pragma unroll
      for(int i=1;i<16;i++) mloc=fmaxf(mloc,sacc[i]);
      mloc=sw32max(mloc);
      BAR();
      STAMP(4);
      if(lane<32) *(LAS float*)(L+L_MX+w*128+lane*4)=mloc;
      BAR();
      mx=fmaxf(fmaxf(*(LAS float*)(L+L_MX+l31*4),*(LAS float*)(L+L_MX+128+l31*4)),fmaxf(*(LAS float*)(L+L_MX+256+l31*4),*(LAS float*)(L+L_MX+384+l31*4)));
      float ls=0.f;
      #pragma unroll
      for(int a=0;a<4;a++){
        float p0=__builtin_amdgcn_exp2f(sacc[4*a]-mx), p1=__builtin_amdgcn_exp2f(sacc[4*a+1]-mx), p2=__builtin_amdgcn_exp2f(sacc[4*a+2]-mx), p3=__builtin_amdgcn_exp2f(sacc[4*a+3]-mx);
        const u32 u0=pk2(p0,p1), u1=pk2(p2,p3);
        ls+=(blo(u0)+bhi(u0))+(blo(u1)+bhi(u1));
        *(LAS v2u*)(L+L_P+l31*256+16*((4*w+a)^fl)+8*hh)=v2u{u0,u1};
      }
      ls=sw32add(ls,ls);
      if(lane<32) *(LAS float*)(L+L_LS+w*128+lane*4)=ls;
      BAR();
      STAMP(5);
      LOAD_VF();
    } else {
      if(jj==0 && w==7){ __builtin_amdgcn_fence(__ATOMIC_RELEASE,"agent"); VMW(0); }
      while(rt()-t_start<T_FLAG1) __builtin_amdgcn_s_sleep(1);
      for(int sp=0;sp<SPIN_CAP;++sp){ FENCE();
        v4u f=ld128<AUX_SC1>(rws,WS_FLAG1+xc*1024+(w-4)*256+(lane&15)*16);
        if(__all((f.x==(u32)want)&(f.y==(u32)(want>>32))&(f.z==(u32)want)&(f.w==(u32)(want>>32)))) break; __builtin_amdgcn_s_sleep(SLP);
      }
      FENCE();
      for(int sp=0;sp<SPIN_CAP && *(volatile LAS u32*)(L+L_QAS+(w-4)*8192)==0;++sp) __builtin_amdgcn_s_sleep(1);
      const int wv=w-4;
      v4u qa[8];
      #pragma unroll
      for(int i=0;i<8;i++) qa[i]=ld128<AUX_SC1>(rws,WS_QA+ab*32768+(8*wv+i)*1024+16*lane);
      for(int sp=0;sp<SPIN_CAP;++sp){
        u32 bad=0;
        #pragma unroll
        for(int i=0;i<8;i++) bad|=(__all(ok4(qa[i]))?0u:1u)<<i;
        if(!bad) break;
        __builtin_amdgcn_s_sleep(SLP_QA); FENCE();
        #pragma unroll
        for(int i=0;i<8;i++) if((bad>>i)&1) qa[i]=ld128<AUX_SC1>(rws,WS_QA+ab*32768+(8*wv+i)*1024+16*lane);
      }
      #pragma unroll
      for(int i=0;i<8;i++){ const int head=8*wv+i; *(LAS v4u*)(L+L_QAS+head*1024+((lane*16)^(16*fsw(head&15))))=qa[i]; }
      BAR();
      LOAD_VF();
      BAR();
      BAR();
      mx=0.f;
      BAR();
    }
    {
      v4u pb[8];
      #pragma unroll
      for(int ks=0;ks<8;ks++) pb[ks]=lrd128(L+L_P+l31*256+16*((2*ks+hh)^fl));
      const float lsum=(*(LAS float*)(L+L_LS+l31*4)+*(LAS float*)(L+L_LS+128+l31*4))+(*(LAS float*)(L+L_LS+256+l31*4)+*(LAS float*)(L+L_LS+384+l31*4));
      SB();
      f32x16 o[2];
      #pragma unroll
      for(int i=0;i<16;i++){ o[0][i]=0.f; o[1][i]=0.f; }
      #pragma unroll
      for(int t=0;t<2;t++)
        #pragma unroll
        for(int ks=0;ks<8;ks++) o[t]=mfma32(vf[ks][t],pb[ks],o[t]);
      const float inv=__builtin_amdgcn_rcpf(lsum);
      const float lse=mx+__builtin_amdgcn_logf(lsum);
      STAMP(6);
      #pragma unroll
      for(int t=0;t<2;t++)
        #pragma unroll
        for(int a=0;a<4;a++)
          *(LAS v2u*)(L+L_CKV+l31*1024+256*sgm+16*((8*(w&1)+4*t+a)^fl)+8*hh)=v2u{pk2(o[t][4*a]*inv,o[t][4*a+1]*inv),pk2(o[t][4*a+2]*inv,o[t][4*a+3]*inv)};
      v4u ov[4];
      #pragma unroll
      for(int i=0;i<4;i++){ const int head=8*i+(lane>>3); ov[i]=lrd128(L+L_CKV+head*1024+256*sgm+16*((8*(w&1)+(lane&7))^fsw(head))); }
      SB();
      #pragma unroll
      for(int i=0;i<4;i++){ const int head=8*i+(lane>>3); st128(rws,WS_PO+((head*4+sgm)*128+ab*32+as)*256+(w&1)*128+(lane&7)*16,ov[i]); }
      STAMP(7);
      VMW(0);
      BAR();
      if(w==0 && lane<32) __builtin_amdgcn_raw_buffer_store_b64(v2u{__float_as_uint(lse),(u32)want},rws,WS_LSE+(lane>>2)*4096+fi*32+(lane&3)*8,0,AUX_SC1);
      STAMP(8);
    }
  }
  STAMP(15);
}

extern "C" void kernel_launch(void* const* d_in,const int* in_sizes,int n_in,void* d_out,int out_size,void* d_ws,size_t ws_size,hipStream_t stream){
  (void)hipFuncSetAttribute((const void*)mla_mlp_kernel,hipFuncAttributeMaxDynamicSharedMemorySize,LDS_BYTES);
  mla_mlp_kernel<<<256,512,LDS_BYTES,stream>>>((const u16*)d_in[0],(const u16*)d_in[1],(const u16*)d_in[2],(const u16*)d_in[3],(const u16*)d_in[4],(const u16*)d_in[5],
     (const u16*)d_in[7],(const u16*)d_in[8],(const u16*)d_in[9],(const u16*)d_in[10],(u16*)d_out,(unsigned char*)d_ws);
}
```

```cpp
#include <hip/hip_runtime.h>
#include <cstring>
typedef unsigned short u16; typedef unsigned u32; typedef unsigned long long u64;
#define GAS __attribute__((address_space(1)))
#define LAS __attribute__((address_space(3)))
typedef u32 v4u __attribute__((ext_vector_type(4)));
typedef u32 v2u __attribute__((ext_vector_type(2)));
typedef float f32x2 __attribute__((ext_vector_type(2)));
typedef float f32x4 __attribute__((ext_vector_type(4)));
typedef float f32x16 __attribute__((ext_vector_type(16)));
typedef __bf16 bf16x8 __attribute__((ext_vector_type(8)));
typedef __bf16 bf16x2 __attribute__((ext_vector_type(2)));
typedef short s16x4 __attribute__((ext_vector_type(4)));
typedef GAS u64 gu64;
typedef LAS unsigned char lds8;
#define RLX_AGENT __ATOMIC_RELAXED, __HIP_MEMORY_SCOPE_AGENT
#define DI __device__ __forceinline__

constexpr int NB=4, NH=32, SK=4096, HD=128, DM=512, DC=512, FF=1792;
constexpr int WS_EPOCH=0, WS_FLAG1=8192, WS_FLAG2=16384, WS_Q1=32768, WS_CX=WS_Q1+262144, WS_HM=WS_CX+262144, WS_QA=WS_HM+262144, WS_LSE=WS_QA+131072, WS_PO=WS_LSE+32768,
              WS_OUT=WS_PO+(4<<20), WS_END=WS_OUT+(2<<20), WS_STAMP=WS_END;
constexpr int L_CKV=0, L_QAS=131072, L_P=L_QAS, L_MX=L_QAS+8192, L_LS=L_QAS+8704;
constexpr int L_WQ=0, L_WUK=32768, L_WUV=65536, L_WO=98304, L_W2=131072, L_W1=L_WQ, L_XN=147456, L_QS=152064, L_HP=153088, L_CL=155136, L_H2=153088, L_RED=157184, L_EP=157696, L_STF=158720, L_QL=159232, L_WG=L_QL, L_GO=L_QAS+16384;
constexpr int LDS_BYTES=163840;
constexpr u32 SENT=0xFFFFFFFFu;
constexpr int SPIN_CAP=1<<20;
constexpr int AUX_NT=2, AUX_SC1=16;
#define CKV_INFL 16
#define SLP 8
#define GUN_MASK 3
#define D_E3F 40
#define D_E3A 70
#define D_E4 60
#define XOFF ((w&3)*1024+16*lane)
#define SLP_E1 2
#define D_E1 6
#define TW_INFL 6
#define D_E3B 0
#define CL_INFL 1
#define T_CLR 390
#define R_GO 31
#define T_GUN 600
#define SLP_GUN 4
#define SLP_QA 2
#define SLP_E4 0
#define T_WUK 60
#define T_CKV 120
#define T_TAIL 450
#define T_FLAG1 230
#define T_GATHER 330
#define STAMP(k) do{}while(0)

DI u64 rt(){ return __builtin_amdgcn_s_memrealtime(); }
DI u64 ct(){ u64 t; asm volatile("s_memtime %0\n\ts_waitcnt lgkmcnt(0)" : "=s"(t) :: "memory"); return t; }
DI __amdgpu_buffer_rsrc_t mkrs(const void* p, int bytes){ return __builtin_amdgcn_make_buffer_rsrc((void*)p,0,bytes,0x00020000); }
template<int AUX> DI v4u ld128(__amdgpu_buffer_rsrc_t rs,int off){ return __builtin_amdgcn_raw_buffer_load_b128(rs,off,0,AUX); }
template<int AUX> DI u32 ld32(__amdgpu_buffer_rsrc_t rs,int off){ return __builtin_amdgcn_raw_buffer_load_b32(rs,off,0,AUX); }
DI void st128(__amdgpu_buffer_rsrc_t rs,int off,v4u v){ __builtin_amdgcn_raw_buffer_store_b128(v,rs,off,0,AUX_SC1); }
DI void st32(__amdgpu_buffer_rsrc_t rs,int off,u32 v){ __builtin_amdgcn_raw_buffer_store_b32(v,rs,off,0,AUX_SC1); }
DI f32x4 mfma16(v4u a,v4u b,f32x4 c){ return __builtin_amdgcn_mfma_f32_16x16x32_bf16(__builtin_bit_cast(bf16x8,a),__builtin_bit_cast(bf16x8,b),c,0,0,0); }
DI f32x16 mfma32(v4u a,v4u b,f32x16 c){ return __builtin_amdgcn_mfma_f32_32x32x16_bf16(__builtin_bit_cast(bf16x8,a),__builtin_bit_cast(bf16x8,b),c,0,0,0); }
DI v2u trd(lds8* p){ return __builtin_bit_cast(v2u,__builtin_amdgcn_ds_read_tr16_b64_v4i16((LAS s16x4*)p)); }
DI v4u trd2(lds8* p0,lds8* p1){ v2u a=trd(p0), b=trd(p1); return v4u{a.x,a.y,b.x,b.y}; }
DI v4u lrd128(lds8* p){ return *(LAS v4u*)p; }
DI u32 pk2(float lo,float hi){ f32x2 f={lo,hi}; return __builtin_bit_cast(u32,__builtin_convertvector(f,bf16x2)); }
DI float rbf(float x){ return __uint_as_float(pk2(x,0.f)<<16); }
DI float blo(u32 w){ return __uint_as_float(w<<16); }
DI float bhi(u32 w){ return __uint_as_float(w&0xFFFF0000u); }
DI u32 nos(u32 w){ return w<0xFFFFFFFEu? w:0xFFFFFFFEu; }
DI v4u nos4(v4u v){ return v4u{nos(v.x),nos(v.y),nos(v.z),nos(v.w)}; }
DI bool ok4(v4u v){ return (v.x!=SENT)&(v.y!=SENT)&(v.z!=SENT)&(v.w!=SENT); }
#define BAR() asm volatile("s_waitcnt lgkmcnt(0)\n\ts_barrier" ::: "memory")
#define VMW(n) asm volatile("s_waitcnt vmcnt(%0)" :: "i"(n) : "memory")
#define FENCE() asm volatile("" ::: "memory")
#define SB() __builtin_amdgcn_sched_barrier(0)
template<int CTRL> DI float dpp(float x){ return __builtin_bit_cast(float,__builtin_amdgcn_mov_dpp(__builtin_bit_cast(int,x),CTRL,0xf,0xf,true)); }
constexpr int DXOR1=0xB1, DXOR2=0x4E, DHMIR=0x141, DROR8=0x128, DROR4=0x124;
DI float sw32add(float a,float b){ auto s=__builtin_amdgcn_permlane32_swap(__float_as_uint(a),__float_as_uint(b),false,false); return __uint_as_float(s[0])+__uint_as_float(s[1]); }
DI float sw16add(float a,float b){ auto s=__builtin_amdgcn_permlane16_swap(__float_as_uint(a),__float_as_uint(b),false,false); return __uint_as_float(s[0])+__uint_as_float(s[1]); }
DI float sw32max(float a){ auto s=__builtin_amdgcn_permlane32_swap(__float_as_uint(a),__float_as_uint(a),false,false); return fmaxf(__uint_as_float(s[0]),__uint_as_float(s[1])); }
DI float sw16max(float a){ auto s=__builtin_amdgcn_permlane16_swap(__float_as_uint(a),__float_as_uint(a),false,false); return fmaxf(__uint_as_float(s[0]),__uint_as_float(s[1])); }
DI float wave_sum(float v){ v+=dpp<DXOR1>(v); v+=dpp<DXOR2>(v); v+=dpp<DHMIR>(v); v+=dpp<DROR8>(v); v=sw16add(v,v); v=sw32add(v,v); return v; }
DI void dma16(const void* gbase,u32 voff,u32 ldsb){
  u32 keep;
  asm volatile("s_mov_b32 %0, m0\n\ts_mov_b32 m0, %3\n\ts_nop 0\n\tglobal_load_lds_dwordx4 %1, %2 nt\n\ts_mov_b32 m0, %0" : "=&s"(keep) : "v"(voff),"s"(gbase),"s"(ldsb) : "memory");
}
DI constexpr int fsw(int row){ return ((row&3)<<2)|((row>>2)&3); }
DI void wait_flags128(__amdgpu_buffer_rsrc_t rs,int off,u64 want,int lane){
  for(int sp=0;sp<SPIN_CAP;++sp){ FENCE();
    v4u f=ld128<AUX_SC1>(rs,off+lane*16);
    bool ok=(f.x==(u32)want)&(f.y==(u32)(want>>32))&(f.z==(u32)want)&(f.w==(u32)(want>>32));
    if(__all(ok)) break; __builtin_amdgcn_s_sleep(SLP);
  }
  FENCE();
}

__global__ void __launch_bounds__(512,2) mla_mlp_kernel(const u16* __restrict__ x,const u16* __restrict__ ckv,const u16* __restrict__ wq,const u16* __restrict__ wuk,
    const u16* __restrict__ wuv,const u16* __restrict__ wo,const u16* __restrict__ w1,const u16* __restrict__ b1,const u16* __restrict__ w2,const u16* __restrict__ b2,
    u16* __restrict__ out,unsigned char* __restrict__ ws){
  extern __shared__ __attribute__((aligned(16))) unsigned char smem[];
  lds8* L=(lds8*)smem;
  const u32 Lb=(u32)(size_t)L;
  const int tid=threadIdx.x, lane=tid&63, w=__builtin_amdgcn_readfirstlane(tid>>6);
  const int wg=blockIdx.x, xc=wg&7, j=wg>>3, jj=j&15;
  const bool isF=j<16;
  const int fi=xc*16+jj, th=fi>>2, tq=fi&3;
  const int g=lane>>4, q4=(lane&15)>>2, p4=lane&3, hh=lane>>5, l31=lane&31;
  const __amdgpu_buffer_rsrc_t rws=mkrs(ws,WS_END);
  const u64 t_start=rt();
  asm volatile("" :: "s"(x),"s"(ckv),"s"(wq),"s"(wuk),"s"(ws));
  if(isF){
    const bool mlp = fi<FF/16;
    if(w==7) st128(rws,WS_QA+g*32768+th*1024+tq*256+(lane&15)*16,v4u{SENT,SENT,SENT,SENT});
    const u32 rowoff=(u32)((128*tq+16*w+g)*8192+th*256);
#define DMA_T(p,dst,i) dma16(p,rowoff+(u32)(4*(i)*8192)+16*((lane&15)^(2*(g|(((i)>>1)<<2)))),Lb+(dst)+(16*w+4*(i))*256)
    #pragma unroll
    for(int i=0;i<4;i++) DMA_T(wq,L_WQ,i);
    v4u xr={0,0,0,0};
    if(w<4) asm volatile("global_load_dwordx4 %0, %1, %2" : "+v"(xr) : "v"(w*1024+16*lane), "s"(x) : "memory");
    if(w==7) asm volatile("global_load_dwordx4 %0, %1, %2" : "+v"(xr) : "v"(0), "s"(ws) : "memory");
    while(rt()-t_start<T_WUK) __builtin_amdgcn_s_sleep(1);
    #pragma unroll
    for(int i=0;i<4;i++) dma16(wuk,rowoff+(u32)(4*i*8192)+16*((lane&15)^(4*i+g)),Lb+L_WUK+(16*w+4*i)*256);
    asm volatile("s_waitcnt vmcnt(4)" : "+v"(xr) :: "memory");
    if(w==7){ const u64 wn=(((u64)xr.y<<32)|xr.x)+1; if(lane<8) __hip_atomic_store((gu64*)(ws+WS_FLAG1)+lane*128+fi,wn,RLX_AGENT);
      if(lane==0) *(LAS u64*)(L+L_EP)=wn;
    }
    if(w<4){
      float v[8]={blo(xr.x),bhi(xr.x),blo(xr.y),bhi(xr.y),blo(xr.z),bhi(xr.z),blo(xr.w),bhi(xr.w)};
      float s=((v[0]+v[1])+(v[2]+v[3]))+((v[4]+v[5])+(v[6]+v[7]));
      const float mean=wave_sum(s)*(1.f/DM);
      float qq=0.f;
      #pragma unroll
      for(int i=0;i<8;i++){ float d=v[i]-mean; qq+=d*d; }
      const float var=wave_sum(qq)*(1.f/DM);
      const float mb=rbf(mean), isd=__builtin_amdgcn_rcpf(rbf(__builtin_amdgcn_sqrtf(rbf(rbf(var)+1e-5f))));
      float y[8];
      #pragma unroll
      for(int i=0;i<8;i++) y[i]=rbf(v[i]-mb)*isd;
      *(LAS v4u*)(L+L_XN+w*1088+16*lane)=v4u{pk2(y[0],y[1]),pk2(y[2],y[3]),pk2(y[4],y[5]),pk2(y[6],y[7])};
    }
    BAR();
    STAMP(1);
    const int troff=(8*g+q4)*256+(w^(q4|((g&1)<<2)))*32+p4*8;
    u32 tag; u64 want;
    {
      v4u aq[4], bq[4];
      #pragma unroll
      for(int ks=0;ks<4;ks++){ lds8* a=L+L_WQ+ks*8192+troff; aq[ks]=trd2(a,a+1024); bq[ks]=lrd128(L+L_XN+p4*1088+(128*tq+32*ks+8*g)*2); }
      const v2u e=*(LAS v2u*)(L+L_EP);
      SB();
      f32x4 acc={0,0,0,0};
      #pragma unroll
      for(int ks=0;ks<4;ks++) acc=mfma16(aq[ks],bq[ks],acc);
      tag=e.x;
      if((lane&15)<4){ const u32 lo=pk2(acc[0],acc[1]), hi=pk2(acc[2],acc[3]);
        st128(rws,WS_Q1+fi*2048+w*256+g*64+(lane&3)*16,v4u{lo,tag,hi,tag});
        *(LAS v2u*)(L+L_QL+tq*1024+(w>>1)*256+g*64+p4*16+(w&1)*8)=v2u{lo,hi}; }
      want=((u64)__builtin_amdgcn_readfirstlane(e.y)<<32)|(u32)__builtin_amdgcn_readfirstlane(e.x);
    }
    STAMP(2);
    VMW(1);
    v4u auk[4];
    #pragma unroll
    for(int k=0;k<4;k++){
      lds8* a=L+L_WUK+(16*w+(lane&15))*256+8*(g&1);
      const v2u lo=*(LAS v2u*)(a+16*((4*k+(g>>1))^(lane&15))), hi=*(LAS v2u*)(a+16*((4*k+2+(g>>1))^(lane&15)));
      auk[k]=v4u{lo.x,lo.y,hi.x,hi.y};
    }
    SB();
    if(w<6){
      const int pr=(tq+1+(w>>1))&3;
      v4u qf;
      for(int sp=0;sp<SPIN_CAP;++sp){ FENCE();
        qf=ld128<AUX_SC1>(rws,WS_Q1+(th*4+pr)*2048+(w&1)*1024+lane*16);
        if(__all((qf.y==tag)&(qf.w==tag))) break;
        __builtin_amdgcn_s_sleep(SLP_E1);
      }
      const int t=4*(w&1)+g;
      *(LAS v2u*)(L+L_QL+pr*1024+(t>>1)*256+(lane&15)*16+(t&1)*8)=v2u{qf.x,qf.z};
    }
    BAR();
    STAMP(3);
    {
      v4u qb[4];
      #pragma unroll
      for(int k=0;k<4;k++) qb[k]=lrd128(L+L_QL+q4*1024+k*256+g*64+p4*16);
      SB();
      f32x4 acc={0,0,0,0};
      #pragma unroll
      for(int k=0;k<4;k++) acc=mfma16(auk[k],qb[k],acc);
      #pragma unroll
      for(int r=0;r<4;r++){ acc[r]+=dpp<DROR4>(acc[r]); acc[r]+=dpp<DROR8>(acc[r]); }
      const float SC=0.08838834764831845f*1.4426950408889634f;
      if((lane&15)<4) *(LAS v2u*)(L+L_QS+(lane&15)*256+(16*w+4*g)*2)=v2u{nos(pk2(acc[0]*SC,acc[1]*SC)),nos(pk2(acc[2]*SC,acc[3]*SC))};
    }
    BAR();
    if(w==0){ v4u v=lrd128(L+L_QS+lane*16); st128(rws,WS_QA+g*32768+th*1024+tq*256+(lane&15)*16,v); }
    STAMP(4);
    while(rt()-t_start<T_TAIL) __builtin_amdgcn_s_sleep(1);
    u32 b1raw[4];
    #pragma unroll
    for(int r=0;r<4;r++) b1raw[r]=__builtin_amdgcn_raw_buffer_load_b16(mkrs(b1,FF*2),(16*fi+4*g+r)*2,0,0);
    u32 xraw=__builtin_amdgcn_raw_buffer_load_b16(mkrs(x,NB*DM*2),(p4*DM+4*fi+q4)*2,0,0);
    u32 b2raw=__builtin_amdgcn_raw_buffer_load_b16(mkrs(b2,DM*2),(4*fi+q4)*2,0,0);
    if(mlp){
      #pragma unroll
      for(int i=0;i<2;i++){ const int r=8*i+(lane>>3); dma16(w1,(u32)((128*tq+16*w+r)*(FF*2)+th*128+16*((lane&7)^(2*(((r>>1)&1)|(i<<1))))),Lb+L_W1+(16*w+8*i)*128); }
    }
    #pragma unroll
    for(int i=0;i<4;i++) DMA_T(wuv,L_WUV,i);
    #pragma unroll
    for(int i=0;i<4;i++){ const int r=8*i+w; dma16(wo,(u32)((th*128+32*tq+r)*1024+16*(lane^(2*(r&7)))),Lb+L_WO+r*1024); }
    if(mlp){
      #pragma unroll
      for(int i=0;i<2;i++){ const int r=8*i+w; dma16(w2,(u32)((16*fi+r)*1024+16*(lane^(2*(r&7)))),Lb+L_W2+r*1024); }
      VMW(10);
      BAR();
      if(w<4){
        v4u a1[4], bq[4];
        #pragma unroll
        for(int ks=0;ks<4;ks++){ lds8* a=L+L_W1+(32*ks+8*g+q4)*128+32*(w^((q4>>1)|((g&1)<<1)))+p4*8; a1[ks]=trd2(a,a+512); bq[ks]=lrd128(L+L_XN+p4*1088+(128*tq+32*ks+8*g)*2); }
        SB();
        f32x4 acc={0,0,0,0};
        #pragma unroll
        for(int ks=0;ks<4;ks++) acc=mfma16(a1[ks],bq[ks],acc);
        if((lane&15)<4){
          st128(rws,WS_HM+fi*2048+w*512+g*128+p4*16,v4u{__float_as_uint(acc[0]),tag,__float_as_uint(acc[1]),tag});
          st128(rws,WS_HM+fi*2048+w*512+g*128+64+p4*16,v4u{__float_as_uint(acc[2]),tag,__float_as_uint(acc[3]),tag}); }
      }
      if(w==4){
        v4u h0,h1;
        for(int sp=0;sp<SPIN_CAP;++sp){ FENCE();
          h0=ld128<AUX_SC1>(rws,WS_HM+(th*4+q4)*2048+tq*512+g*128+p4*16);
          h1=ld128<AUX_SC1>(rws,WS_HM+(th*4+q4)*2048+tq*512+g*128+64+p4*16);
          if(__all((h0.y==tag)&(h0.w==tag)&(h1.y==tag)&(h1.w==tag))) break;
          __builtin_amdgcn_s_sleep(SLP);
        }
        float hv[4]={__uint_as_float(h0.x),__uint_as_float(h0.z),__uint_as_float(h1.x),__uint_as_float(h1.z)};
        #pragma unroll
        for(int r=0;r<4;r++){ float sv=hv[r]; sv+=dpp<DROR4>(sv); sv+=dpp<DROR8>(sv); float h=rbf(rbf(sv)+blo(b1raw[r])); h=h>0.f?h:0.f; hv[r]=q4==0? h*h:0.f; }
        *(LAS v2u*)(L+L_H2+lane*8)=v2u{pk2(hv[0],hv[1]),pk2(hv[2],hv[3])};
      }
    }
    VMW(0);
    BAR();
    STAMP(6);
    asm volatile("" : "+v"(xraw), "+v"(b2raw));
    const float xb_res=blo(xraw)+blo(b2raw);
    f32x4 oacc[4];
    v4u auv[4], awo[4];
    {
      #pragma unroll
      for(int k=0;k<4;k++){ lds8* a=L+L_WUV+k*8192+troff; auv[k]=trd2(a,a+1024); }
      const int r0=4*g+q4;
      #pragma unroll
      for(int t=0;t<4;t++){ lds8* a=L+L_WO+r0*1024+(w>>1)*256+32*((4*(w&1)+t)^(r0&7))+p4*8; awo[t]=trd2(a,a+16384); }
      #pragma unroll
      for(int t=0;t<4;t++) oacc[t]=f32x4{0,0,0,0};
      if(mlp){
        const v2u h2=*(LAS v2u*)(L+L_H2+lane*8);
        #pragma unroll
        for(int t=0;t<4;t++){ const v2u a=trd(L+L_W2+r0*1024+(w>>1)*256+32*((4*(w&1)+t)^(r0&7))+p4*8); oacc[t]=mfma16(v4u{a.x,a.y,0,0},v4u{h2.x,h2.y,0,0},oacc[t]); }
      }
    }
    SB();
    if(w==0){
      while(rt()-t_start<T_GUN) __builtin_amdgcn_s_sleep(1);
      v2u v0,v1;
      for(int sp=0;sp<SPIN_CAP;++sp){ FENCE();
        v0=__builtin_amdgcn_raw_buffer_load_b64(rws,WS_LSE+(th>>2)*4096+lane*32+(th&3)*8,0,AUX_SC1);
        v1=__builtin_amdgcn_raw_buffer_load_b64(rws,WS_LSE+(th>>2)*4096+2048+lane*32+(th&3)*8,0,AUX_SC1);
        if(__all((v0.y==tag)&(v1.y==tag))) break; __builtin_amdgcn_s_sleep(SLP_GUN);
      }
      FENCE();
      STAMP(7);
      float l0=__uint_as_float(v0.x), l1=__uint_as_float(v1.x), m0=l0, m1=l1;
      m0=fmaxf(m0,dpp<DXOR1>(m0)); m1=fmaxf(m1,dpp<DXOR1>(m1)); m0=fmaxf(m0,dpp<DXOR2>(m0)); m1=fmaxf(m1,dpp<DXOR2>(m1));
      m0=fmaxf(m0,dpp<DHMIR>(m0)); m1=fmaxf(m1,dpp<DHMIR>(m1)); m0=fmaxf(m0,dpp<DROR8>(m0)); m1=fmaxf(m1,dpp<DROR8>(m1)); m0=sw16max(m0); m1=sw16max(m1);
      const float e0=__builtin_amdgcn_exp2f(l0-m0), e1=__builtin_amdgcn_exp2f(l1-m1);
      float s0=e0, s1=e1;
      s0+=dpp<DXOR1>(s0); s1+=dpp<DXOR1>(s1); s0+=dpp<DXOR2>(s0); s1+=dpp<DXOR2>(s1); s0+=dpp<DHMIR>(s0); s1+=dpp<DHMIR>(s1); s0+=dpp<DROR8>(s0); s1+=dpp<DROR8>(s1);
      s0=sw16add(s0,s0); s1=sw16add(s1,s1);
      *(LAS float*)(L+L_WG+hh*128+(l31&7)*16+(l31>>3)*4)=e0*__builtin_amdgcn_rcpf(s0);
      *(LAS float*)(L+L_WG+256+hh*128+(l31&7)*16+(l31>>3)*4)=e1*__builtin_amdgcn_rcpf(s1);
    } else {
    }
    BAR();
    {
      const int b=w>>1, half=w&1, slo=lane>>3, q=lane&7;
      v4u po[4];
      #pragma unroll
      for(int i=0;i<4;i++) po[i]=ld128<AUX_SC1>(rws,WS_PO+(fi*128+b*32+slo+8*i)*256+half*128+q*16);
      const f32x4 e=*(LAS f32x4*)(L+L_WG+b*128+slo*16);
      STAMP(8);
      float a[8];
      #pragma unroll
      for(int k=0;k<8;k++) a[k]=0.f;
      #pragma unroll
      for(int i=0;i<4;i++){
        a[0]+=e[i]*blo(po[i].x); a[1]+=e[i]*bhi(po[i].x); a[2]+=e[i]*blo(po[i].y); a[3]+=e[i]*bhi(po[i].y);
        a[4]+=e[i]*blo(po[i].z); a[5]+=e[i]*bhi(po[i].z); a[6]+=e[i]*blo(po[i].w); a[7]+=e[i]*bhi(po[i].w);
      }
      float c4[4], c2[2];
      #pragma unroll
      for(int k=0;k<4;k++) c4[k]=sw32add(a[k],a[k+4]);
      c2[0]=sw16add(c4[0],c4[2]); c2[1]=sw16add(c4[1],c4[3]);
      const bool b3=(lane>>3)&1;
      const float keep=b3?c2[1]:c2[0], send=b3?c2[0]:c2[1];
      const float r=keep+dpp<DROR8>(send);
      const int col=8*q+4*hh+2*((lane>>4)&1)+(b3?1:0);
      *(LAS u16*)(L+L_CL+b*320+(half*64+col)*2)=(u16)pk2(r,0.f);
    }
    BAR();
    STAMP(9);
    {
      v4u bc[4];
      #pragma unroll
      for(int k=0;k<4;k++) bc[k]=lrd128(L+L_CL+p4*320+(32*k+8*g)*2);
      SB();
      f32x4 acc={0,0,0,0};
      #pragma unroll
      for(int k=0;k<4;k++) acc=mfma16(auv[k],bc[k],acc);
      if((lane&15)<4) st128(rws,WS_CX+fi*2048+w*256+g*64+(lane&3)*16,v4u{pk2(acc[0],acc[1]),tag,pk2(acc[2],acc[3]),tag});
    }
    STAMP(10);
    __builtin_amdgcn_s_sleep(D_E3B);
    {
      v4u c0,c1;
      for(int sp=0;sp<SPIN_CAP;++sp){ FENCE();
        c0=ld128<AUX_SC1>(rws,WS_CX+(th*4+q4)*2048+(2*tq)*256+g*64+p4*16);
        c1=ld128<AUX_SC1>(rws,WS_CX+(th*4+q4)*2048+(2*tq+1)*256+g*64+p4*16);
        if(__all((c0.y==tag)&(c0.w==tag)&(c1.y==tag)&(c1.w==tag))) break;
        __builtin_amdgcn_s_sleep(SLP_E1);
      }
      STAMP(11);
      const v4u bc={c0.x,c0.z,c1.x,c1.z};
      #pragma unroll
      for(int t=0;t<4;t++) oacc[t]=mfma16(awo[t],bc,oacc[t]);
      const bool b3=(lane>>3)&1, b2_=(lane>>2)&1;
      f32x4 u[2], v;
      #pragma unroll
      for(int t=0;t<2;t++)
        #pragma unroll
        for(int r=0;r<4;r++){ const float keep=b3?oacc[t+2][r]:oacc[t][r], send=b3?oacc[t][r]:oacc[t+2][r]; u[t][r]=keep+dpp<DROR8>(send); }
      #pragma unroll
      for(int r=0;r<4;r++){ const float keep=b2_?u[1][r]:u[0][r], send=b2_?u[0][r]:u[1][r];
        v[r]=keep+__builtin_bit_cast(float,__builtin_amdgcn_ds_swizzle(__builtin_bit_cast(int,send),0x101F)); }
      st128(rws,WS_OUT+(16*w+4*q4+g)*16384+fi*128+p4*16,v4u{__float_as_uint(v[0]),tag,__float_as_uint(v[1]),tag});
      st128(rws,WS_OUT+(16*w+4*q4+g)*16384+fi*128+64+p4*16,v4u{__float_as_uint(v[2]),tag,__float_as_uint(v[3]),tag});
    }
    STAMP(12);
    { const u64 t1=rt(); while(rt()-t1<D_E4) __builtin_amdgcn_s_sleep(1); }
    v4u pv[2];
    #pragma unroll
    for(int i=0;i<2;i++) pv[i]=ld128<AUX_SC1>(rws,WS_OUT+fi*16384+i*8192+tid*16);
    for(int sp=0;sp<SPIN_CAP;++sp){
      const bool k0=(pv[0].y==tag)&(pv[0].w==tag), k1=(pv[1].y==tag)&(pv[1].w==tag);
      if(__all(k0&k1)) break;
      __builtin_amdgcn_s_sleep(SLP_E4); FENCE();
      if(!k0) pv[0]=ld128<AUX_SC1>(rws,WS_OUT+fi*16384+tid*16);
      if(!k1) pv[1]=ld128<AUX_SC1>(rws,WS_OUT+fi*16384+8192+tid*16);
    }
    STAMP(13);
    float v=sw32add(__uint_as_float(pv[0].x)+__uint_as_float(pv[1].x),__uint_as_float(pv[0].z)+__uint_as_float(pv[1].z));
    v=sw16add(v,v); v+=dpp<DROR8>(v);
    if((lane&24)==0) *(LAS float*)(L+L_RED+w*64+((2*((lane>>2)&1)+hh)*4+p4)*4)=v;
    BAR();
    if(tid<16){
      float a=0.f;
      #pragma unroll
      for(int ww=0;ww<8;ww++) a+=*(LAS float*)(L+L_RED+ww*64+tid*4);
      out[(tid&3)*DM+4*fi+(tid>>2)]=(u16)pk2(xb_res+a,0.f);
    }
    if(wg==0 && tid==0) __hip_atomic_store((gu64*)(ws+WS_EPOCH),want,RLX_AGENT);
  } else {
    const int ab=fi>>5, as=fi&31;
    const int fl=fsw(l31);
    const int blk=(lane>>4)&1, sgm=w>>1;
    v4u vf[8][2];
#define LOAD_VF() do{ _Pragma("unroll") for(int ks=0;ks<8;ks++) _Pragma("unroll") for(int t=0;t<2;t++){ \
        const int ch=8*(w&1)+4*t+2*blk+(p4>>1); \
        lds8* a0=L+L_CKV+(16*ks+8*hh+q4)*1024+256*sgm+16*(ch^((q4<<2)|(2*hh)))+8*(p4&1); \
        lds8* a1=L+L_CKV+(16*ks+8*hh+4+q4)*1024+256*sgm+16*(ch^((q4<<2)|(2*hh+1)))+8*(p4&1); \
        vf[ks][t]=trd2(a0,a1); } SB(); }while(0)
    float mx;
    u64 want=0;
    if(w>=4 || w==0){ const u64 e=*(const GAS u64*)(ws+WS_EPOCH); want=(((u64)__builtin_amdgcn_readfirstlane((u32)(e>>32))<<32)|(u32)__builtin_amdgcn_readfirstlane((u32)e))+1; }
    if(tid<4) *(volatile LAS u32*)(L+L_QAS+tid*8192)=0;
    BAR();
    if(w<4){
      const u16* gb=ckv+((size_t)ab*SK+as*128+32*w)*DC;
      while(rt()-t_start<T_CKV) __builtin_amdgcn_s_sleep(1);
      #pragma unroll
      for(int r=0;r<32;r++){
        if(r>=CKV_INFL) VMW(CKV_INFL-1);
        dma16(gb+r*DC,(u32)((lane*16)^(16*fsw(r))),Lb+L_CKV+(32*w+r)*1024);
        if(r==R_GO && w==0 && lane<4) *(volatile LAS u32*)(L+L_QAS+lane*8192)=1;
      }
      VMW(0);
      v4u af[32];
      #pragma unroll
      for(int k=0;k<32;k++) af[k]=lrd128(L+L_CKV+(32*w+l31)*1024+256*(k>>3)+16*((2*(k&7)+hh)^fl));
      BAR();
      f32x16 sacc;
      #pragma unroll
      for(int i=0;i<16;i++) sacc[i]=0.f;
      v4u bf[12];
      #pragma unroll
      for(int k=0;k<12;k++) bf[k]=lrd128(L+L_QAS+l31*1024+256*(k>>3)+16*((2*(k&7)+hh)^fl));
      SB();
      #pragma unroll
      for(int k=0;k<32;k++){
        sacc=mfma32(af[k],bf[k%12],sacc);
        if(k+12<32) bf[k%12]=lrd128(L+L_QAS+l31*1024+256*((k+12)>>3)+16*((2*((k+12)&7)+hh)^fl));
        SB();
      }
      float mloc=sacc[0];
      #pragma unroll
      for(int i=1;i<16;i++) mloc=fmaxf(mloc,sacc[i]);
      mloc=sw32max(mloc);
      BAR();
      STAMP(4);
      if(lane<32) *(LAS float*)(L+L_MX+w*128+lane*4)=mloc;
      BAR();
      mx=fmaxf(fmaxf(*(LAS float*)(L+L_MX+l31*4),*(LAS float*)(L+L_MX+128+l31*4)),fmaxf(*(LAS float*)(L+L_MX+256+l31*4),*(LAS float*)(L+L_MX+384+l31*4)));
      float ls=0.f;
      #pragma unroll
      for(int a=0;a<4;a++){
        float p0=__builtin_amdgcn_exp2f(sacc[4*a]-mx), p1=__builtin_amdgcn_exp2f(sacc[4*a+1]-mx), p2=__builtin_amdgcn_exp2f(sacc[4*a+2]-mx), p3=__builtin_amdgcn_exp2f(sacc[4*a+3]-mx);
        const u32 u0=pk2(p0,p1), u1=pk2(p2,p3);
        ls+=(blo(u0)+bhi(u0))+(blo(u1)+bhi(u1));
        *(LAS v2u*)(L+L_P+l31*256+16*((4*w+a)^fl)+8*hh)=v2u{u0,u1};
      }
      ls=sw32add(ls,ls);
      if(lane<32) *(LAS float*)(L+L_LS+w*128+lane*4)=ls;
      BAR();
      STAMP(5);
      LOAD_VF();
    } else {
      while(rt()-t_start<T_FLAG1) __builtin_amdgcn_s_sleep(1);
      for(int sp=0;sp<SPIN_CAP;++sp){ FENCE();
        v4u f=ld128<AUX_SC1>(rws,WS_FLAG1+xc*1024+(w-4)*256+(lane&15)*16);
        if(__all((f.x==(u32)want)&(f.y==(u32)(want>>32))&(f.z==(u32)want)&(f.w==(u32)(want>>32)))) break; __builtin_amdgcn_s_sleep(SLP);
      }
      FENCE();
      for(int sp=0;sp<SPIN_CAP && *(volatile LAS u32*)(L+L_QAS+(w-4)*8192)==0;++sp) __builtin_amdgcn_s_sleep(1);
      const int wv=w-4;
      v4u qa[8];
      #pragma unroll
      for(int i=0;i<8;i++) qa[i]=ld128<AUX_SC1>(rws,WS_QA+ab*32768+(8*wv+i)*1024+16*lane);
      for(int sp=0;sp<SPIN_CAP;++sp){
        u32 bad=0;
        #pragma unroll
        for(int i=0;i<8;i++) bad|=(__all(ok4(qa[i]))?0u:1u)<<i;
        if(!bad) break;
        __builtin_amdgcn_s_sleep(SLP_QA); FENCE();
        #pragma unroll
        for(int i=0;i<8;i++) if((bad>>i)&1) qa[i]=ld128<AUX_SC1>(rws,WS_QA+ab*32768+(8*wv+i)*1024+16*lane);
      }
      #pragma unroll
      for(int i=0;i<8;i++){ const int head=8*wv+i; *(LAS v4u*)(L+L_QAS+head*1024+((lane*16)^(16*fsw(head&15))))=qa[i]; }
      BAR();
      LOAD_VF();
      BAR();
      BAR();
      mx=0.f;
      BAR();
    }
    {
      v4u pb[8];
      #pragma unroll
      for(int ks=0;ks<8;ks++) pb[ks]=lrd128(L+L_P+l31*256+16*((2*ks+hh)^fl));
      const float lsum=(*(LAS float*)(L+L_LS+l31*4)+*(LAS float*)(L+L_LS+128+l31*4))+(*(LAS float*)(L+L_LS+256+l31*4)+*(LAS float*)(L+L_LS+384+l31*4));
      SB();
      f32x16 o[2];
      #pragma unroll
      for(int i=0;i<16;i++){ o[0][i]=0.f; o[1][i]=0.f; }
      #pragma unroll
      for(int t=0;t<2;t++)
        #pragma unroll
        for(int ks=0;ks<8;ks++) o[t]=mfma32(vf[ks][t],pb[ks],o[t]);
      const float inv=__builtin_amdgcn_rcpf(lsum);
      const float lse=mx+__builtin_amdgcn_logf(lsum);
      STAMP(6);
      #pragma unroll
      for(int t=0;t<2;t++)
        #pragma unroll
        for(int a=0;a<4;a++)
          *(LAS v2u*)(L+L_CKV+l31*1024+256*sgm+16*((8*(w&1)+4*t+a)^fl)+8*hh)=v2u{pk2(o[t][4*a]*inv,o[t][4*a+1]*inv),pk2(o[t][4*a+2]*inv,o[t][4*a+3]*inv)};
      v4u ov[4];
      #pragma unroll
      for(int i=0;i<4;i++){ const int head=8*i+(lane>>3); ov[i]=lrd128(L+L_CKV+head*1024+256*sgm+16*((8*(w&1)+(lane&7))^fsw(head))); }
      SB();
      #pragma unroll
      for(int i=0;i<4;i++){ const int head=8*i+(lane>>3); st128(rws,WS_PO+((head*4+sgm)*128+ab*32+as)*256+(w&1)*128+(lane&7)*16,ov[i]); }
      STAMP(7);
      VMW(0);
      BAR();
      if(w==0 && lane<32) __builtin_amdgcn_raw_buffer_store_b64(v2u{__float_as_uint(lse),(u32)want},rws,WS_LSE+(lane>>2)*4096+fi*32+(lane&3)*8,0,AUX_SC1);
      STAMP(8);
    }
  }
  if(w==0){ while(rt()-t_start<6000) __builtin_amdgcn_s_sleep(1); }
  STAMP(15);
}

__global__ void __launch_bounds__(64) k_spin_tiny(unsigned char* p){ const u64 t=rt(); while(rt()-t<3000) __builtin_amdgcn_s_sleep(1); if(p==nullptr) p[0]=1; }
__global__ void __launch_bounds__(512,2) k_spin_big(unsigned char* p){ extern __shared__ unsigned char sm2[]; asm volatile("v_mov_b32 v218, 0":::"v218"); const u64 t=rt(); while(rt()-t<3000) __builtin_amdgcn_s_sleep(1); if(p==nullptr) p[0]=1; }
extern "C" void kernel_launch(void* const* d_in,const int* in_sizes,int n_in,void* d_out,int out_size,void* d_ws,size_t ws_size,hipStream_t stream){
  (void)hipFuncSetAttribute((const void*)mla_mlp_kernel,hipFuncAttributeMaxDynamicSharedMemorySize,LDS_BYTES);
  (void)hipFuncSetAttribute((const void*)k_spin_big,hipFuncAttributeMaxDynamicSharedMemorySize,LDS_BYTES);
  k_spin_big<<<256,512,LDS_BYTES,stream>>>((unsigned char*)d_ws);
  mla_mlp_kernel<<<256,512,LDS_BYTES,stream>>>((const u16*)d_in[0],(const u16*)d_in[1],(const u16*)d_in[2],(const u16*)d_in[3],(const u16*)d_in[4],(const u16*)d_in[5],
     (const u16*)d_in[7],(const u16*)d_in[8],(const u16*)d_in[9],(const u16*)d_in[10],(u16*)d_out,(unsigned char*)d_ws);
}
```

```cpp
#include <hip/hip_runtime.h>
#include <cstring>
typedef unsigned short u16; typedef unsigned u32; typedef unsigned long long u64;
#define GAS __attribute__((address_space(1)))
#define LAS __attribute__((address_space(3)))
typedef u32 v4u __attribute__((ext_vector_type(4)));
typedef u32 v2u __attribute__((ext_vector_type(2)));
typedef float f32x2 __attribute__((ext_vector_type(2)));
typedef float f32x4 __attribute__((ext_vector_type(4)));
typedef float f32x16 __attribute__((ext_vector_type(16)));
typedef __bf16 bf16x8 __attribute__((ext_vector_type(8)));
typedef __bf16 bf16x2 __attribute__((ext_vector_type(2)));
typedef short s16x4 __attribute__((ext_vector_type(4)));
typedef GAS u64 gu64;
typedef LAS unsigned char lds8;
#define RLX_AGENT __ATOMIC_RELAXED, __HIP_MEMORY_SCOPE_AGENT
#define DI __device__ __forceinline__

constexpr int NB=4, NH=32, SK=4096, HD=128, DM=512, DC=512, FF=1792;
constexpr int WS_EPOCH=0, WS_FLAG1=8192, WS_FLAG2=16384, WS_Q1=32768, WS_CX=WS_Q1+262144, WS_HM=WS_CX+262144, WS_QA=WS_HM+262144, WS_LSE=WS_QA+131072, WS_PO=WS_LSE+32768,
              WS_OUT=WS_PO+(4<<20), WS_END=WS_OUT+(2<<20), WS_STAMP=WS_END;
constexpr int L_CKV=0, L_QAS=131072, L_P=L_QAS, L_MX=L_QAS+8192, L_LS=L_QAS+8704;
constexpr int L_WQ=0, L_WUK=32768, L_WUV=65536, L_WO=98304, L_W2=131072, L_W1=L_WQ, L_XN=147456, L_QS=152064, L_HP=153088, L_CL=155136, L_H2=153088, L_RED=157184, L_EP=157696, L_STF=158720, L_QL=159232, L_WG=L_QL, L_GO=L_QAS+16384;
constexpr int LDS_BYTES=163840;
constexpr u32 SENT=0xFFFFFFFFu;
constexpr int SPIN_CAP=1<<20;
constexpr int AUX_NT=2, AUX_SC1=16;
#define CKV_INFL 16
#define SLP 8
#define GUN_MASK 3
#define D_E3F 40
#define D_E3A 70
#define D_E4 60
#define XOFF ((w&3)*1024+16*lane)
#define SLP_E1 2
#define D_E1 6
#define TW_INFL 6
#define D_E3B 0
#define CL_INFL 1
#define T_CLR 390
#define R_GO 31
#define T_GUN 600
#define SLP_GUN 4
#define SLP_QA 2
#define SLP_E4 0
#define PT_STRIDE (1<<20)
#define PT_BASE 0x60000
#define T_WUK 60
#define T_CKV 120
#define T_TAIL 450
#define T_FLAG1 230
#define T_GATHER 330
#define STAMP(k) do{}while(0)

DI u64 rt(){ return __builtin_amdgcn_s_memrealtime(); }
DI u64 ct(){ u64 t; asm volatile("s_memtime %0\n\ts_waitcnt lgkmcnt(0)" : "=s"(t) :: "memory"); return t; }
DI __amdgpu_buffer_rsrc_t mkrs(const void* p, int bytes){ return __builtin_amdgcn_make_buffer_rsrc((void*)p,0,bytes,0x00020000); }
template<int AUX> DI v4u ld128(__amdgpu_buffer_rsrc_t rs,int off){ return __builtin_amdgcn_raw_buffer_load_b128(rs,off,0,AUX); }
template<int AUX> DI u32 ld32(__amdgpu_buffer_rsrc_t rs,int off){ return __builtin_amdgcn_raw_buffer_load_b32(rs,off,0,AUX); }
DI void st128(__amdgpu_buffer_rsrc_t rs,int off,v4u v){ __builtin_amdgcn_raw_buffer_store_b128(v,rs,off,0,AUX_SC1); }
DI void st32(__amdgpu_buffer_rsrc_t rs,int off,u32 v){ __builtin_amdgcn_raw_buffer_store_b32(v,rs,off,0,AUX_SC1); }
DI f32x4 mfma16(v4u a,v4u b,f32x4 c){ return __builtin_amdgcn_mfma_f32_16x16x32_bf16(__builtin_bit_cast(bf16x8,a),__builtin_bit_cast(bf16x8,b),c,0,0,0); }
DI f32x16 mfma32(v4u a,v4u b,f32x16 c){ return __builtin_amdgcn_mfma_f32_32x32x16_bf16(__builtin_bit_cast(bf16x8,a),__builtin_bit_cast(bf16x8,b),c,0,0,0); }
DI v2u trd(lds8* p){ return __builtin_bit_cast(v2u,__builtin_amdgcn_ds_read_tr16_b64_v4i16((LAS s16x4*)p)); }
DI v4u trd2(lds8* p0,lds8* p1){ v2u a=trd(p0), b=trd(p1); return v4u{a.x,a.y,b.x,b.y}; }
DI v4u lrd128(lds8* p){ return *(LAS v4u*)p; }
DI u32 pk2(float lo,float hi){ f32x2 f={lo,hi}; return __builtin_bit_cast(u32,__builtin_convertvector(f,bf16x2)); }
DI float rbf(float x){ return __uint_as_float(pk2(x,0.f)<<16); }
DI float blo(u32 w){ return __uint_as_float(w<<16); }
DI float bhi(u32 w){ return __uint_as_float(w&0xFFFF0000u); }
DI u32 nos(u32 w){ return w<0xFFFFFFFEu? w:0xFFFFFFFEu; }
DI v4u nos4(v4u v){ return v4u{nos(v.x),nos(v.y),nos(v.z),nos(v.w)}; }
DI bool ok4(v4u v){ return (v.x!=SENT)&(v.y!=SENT)&(v.z!=SENT)&(v.w!=SENT); }
#define BAR() asm volatile("s_waitcnt lgkmcnt(0)\n\ts_barrier" ::: "memory")
#define VMW(n) asm volatile("s_waitcnt vmcnt(%0)" :: "i"(n) : "memory")
#define FENCE() asm volatile("" ::: "memory")
#define SB() __builtin_amdgcn_sched_barrier(0)
template<int CTRL> DI float dpp(float x){ return __builtin_bit_cast(float,__builtin_amdgcn_mov_dpp(__builtin_bit_cast(int,x),CTRL,0xf,0xf,true)); }
constexpr int DXOR1=0xB1, DXOR2=0x4E, DHMIR=0x141, DROR8=0x128, DROR4=0x124;
DI float sw32add(float a,float b){ auto s=__builtin_amdgcn_permlane32_swap(__float_as_uint(a),__float_as_uint(b),false,false); return __uint_as_float(s[0])+__uint_as_float(s[1]); }
DI float sw16add(float a,float b){ auto s=__builtin_amdgcn_permlane16_swap(__float_as_uint(a),__float_as_uint(b),false,false); return __uint_as_float(s[0])+__uint_as_float(s[1]); }
DI float sw32max(float a){ auto s=__builtin_amdgcn_permlane32_swap(__float_as_uint(a),__float_as_uint(a),false,false); return fmaxf(__uint_as_float(s[0]),__uint_as_float(s[1])); }
DI float sw16max(float a){ auto s=__builtin_amdgcn_permlane16_swap(__float_as_uint(a),__float_as_uint(a),false,false); return fmaxf(__uint_as_float(s[0]),__uint_as_float(s[1])); }
DI float wave_sum(float v){ v+=dpp<DXOR1>(v); v+=dpp<DXOR2>(v); v+=dpp<DHMIR>(v); v+=dpp<DROR8>(v); v=sw16add(v,v); v=sw32add(v,v); return v; }
DI void dma16(const void* gbase,u32 voff,u32 ldsb){
  u32 keep;
  asm volatile("s_mov_b32 %0, m0\n\ts_mov_b32 m0, %3\n\ts_nop 0\n\tglobal_load_lds_dwordx4 %1, %2 nt\n\ts_mov_b32 m0, %0" : "=&s"(keep) : "v"(voff),"s"(gbase),"s"(ldsb) : "memory");
}
DI constexpr int fsw(int row){ return ((row&3)<<2)|((row>>2)&3); }
DI void wait_flags128(__amdgpu_buffer_rsrc_t rs,int off,u64 want,int lane){
  for(int sp=0;sp<SPIN_CAP;++sp){ FENCE();
    v4u f=ld128<AUX_SC1>(rs,off+lane*16);
    bool ok=(f.x==(u32)want)&(f.y==(u32)(want>>32))&(f.z==(u32)want)&(f.w==(u32)(want>>32));
    if(__all(ok)) break; __builtin_amdgcn_s_sleep(SLP);
  }
  FENCE();
}

__global__ void __launch_bounds__(512,2) mla_mlp_kernel(const u16* __restrict__ x,const u16* __restrict__ ckv,const u16* __restrict__ wq,const u16* __restrict__ wuk,
    const u16* __restrict__ wuv,const u16* __restrict__ wo,const u16* __restrict__ w1,const u16* __restrict__ b1,const u16* __restrict__ w2,const u16* __restrict__ b2,
    u16* __restrict__ out,unsigned char* __restrict__ ws){
  extern __shared__ __attribute__((aligned(16))) unsigned char smem[];
  lds8* L=(lds8*)smem;
  const u32 Lb=(u32)(size_t)L;
  const int tid=threadIdx.x, lane=tid&63, w=__builtin_amdgcn_readfirstlane(tid>>6);
  const int wg=blockIdx.x, xc=wg&7, j=wg>>3, jj=j&15;
  const bool isF=j<16;
  const int fi=xc*16+jj, th=fi>>2, tq=fi&3;
  const int g=lane>>4, q4=(lane&15)>>2, p4=lane&3, hh=lane>>5, l31=lane&31;
  const __amdgpu_buffer_rsrc_t rws=mkrs(ws,WS_END);
  const u64 t_start=rt();
  asm volatile("" :: "s"(x),"s"(ckv),"s"(wq),"s"(wuk),"s"(ws));
  if(isF){
    const bool mlp = fi<FF/16;
    if(w==7) st128(rws,WS_QA+g*32768+th*1024+tq*256+(lane&15)*16,v4u{SENT,SENT,SENT,SENT});
    const u32 rowoff=(u32)((128*tq+16*w+g)*8192+th*256);
#define DMA_T(p,dst,i) dma16(p,rowoff+(u32)(4*(i)*8192)+16*((lane&15)^(2*(g|(((i)>>1)<<2)))),Lb+(dst)+(16*w+4*(i))*256)
    #pragma unroll
    for(int i=0;i<4;i++) DMA_T(wq,L_WQ,i);
    v4u xr={0,0,0,0};
    if(w<4) asm volatile("global_load_dwordx4 %0, %1, %2" : "+v"(xr) : "v"(w*1024+16*lane), "s"(x) : "memory");
    if(w==7) asm volatile("global_load_dwordx4 %0, %1, %2" : "+v"(xr) : "v"(0), "s"(ws) : "memory");
    while(rt()-t_start<T_WUK) __builtin_amdgcn_s_sleep(1);
    #pragma unroll
    for(int i=0;i<4;i++) dma16(wuk,rowoff+(u32)(4*i*8192)+16*((lane&15)^(4*i+g)),Lb+L_WUK+(16*w+4*i)*256);
    asm volatile("s_waitcnt vmcnt(4)" : "+v"(xr) :: "memory");
    if(w==7){ const u64 wn=(((u64)xr.y<<32)|xr.x)+1; if(lane<8) __hip_atomic_store((gu64*)(ws+WS_FLAG1)+lane*128+fi,wn,RLX_AGENT);
      if(lane==0) *(LAS u64*)(L+L_EP)=wn;
    }
    if(w<4){
      float v[8]={blo(xr.x),bhi(xr.x),blo(xr.y),bhi(xr.y),blo(xr.z),bhi(xr.z),blo(xr.w),bhi(xr.w)};
      float s=((v[0]+v[1])+(v[2]+v[3]))+((v[4]+v[5])+(v[6]+v[7]));
      const float mean=wave_sum(s)*(1.f/DM);
      float qq=0.f;
      #pragma unroll
      for(int i=0;i<8;i++){ float d=v[i]-mean; qq+=d*d; }
      const float var=wave_sum(qq)*(1.f/DM);
      const float mb=rbf(mean), isd=__builtin_amdgcn_rcpf(rbf(__builtin_amdgcn_sqrtf(rbf(rbf(var)+1e-5f))));
      float y[8];
      #pragma unroll
      for(int i=0;i<8;i++) y[i]=rbf(v[i]-mb)*isd;
      *(LAS v4u*)(L+L_XN+w*1088+16*lane)=v4u{pk2(y[0],y[1]),pk2(y[2],y[3]),pk2(y[4],y[5]),pk2(y[6],y[7])};
    }
    BAR();
    STAMP(1);
    const int troff=(8*g+q4)*256+(w^(q4|((g&1)<<2)))*32+p4*8;
    u32 tag; u64 want;
    {
      v4u aq[4], bq[4];
      #pragma unroll
      for(int ks=0;ks<4;ks++){ lds8* a=L+L_WQ+ks*8192+troff; aq[ks]=trd2(a,a+1024); bq[ks]=lrd128(L+L_XN+p4*1088+(128*tq+32*ks+8*g)*2); }
      const v2u e=*(LAS v2u*)(L+L_EP);
      SB();
      f32x4 acc={0,0,0,0};
      #pragma unroll
      for(int ks=0;ks<4;ks++) acc=mfma16(aq[ks],bq[ks],acc);
      tag=e.x;
      if((lane&15)<4){ const u32 lo=pk2(acc[0],acc[1]), hi=pk2(acc[2],acc[3]);
        st128(rws,WS_Q1+fi*2048+w*256+g*64+(lane&3)*16,v4u{lo,tag,hi,tag});
        *(LAS v2u*)(L+L_QL+tq*1024+(w>>1)*256+g*64+p4*16+(w&1)*8)=v2u{lo,hi}; }
      want=((u64)__builtin_amdgcn_readfirstlane(e.y)<<32)|(u32)__builtin_amdgcn_readfirstlane(e.x);
    }
    STAMP(2);
    VMW(1);
    v4u auk[4];
    #pragma unroll
    for(int k=0;k<4;k++){
      lds8* a=L+L_WUK+(16*w+(lane&15))*256+8*(g&1);
      const v2u lo=*(LAS v2u*)(a+16*((4*k+(g>>1))^(lane&15))), hi=*(LAS v2u*)(a+16*((4*k+2+(g>>1))^(lane&15)));
      auk[k]=v4u{lo.x,lo.y,hi.x,hi.y};
    }
    SB();
    if(w<6){
      const int pr=(tq+1+(w>>1))&3;
      v4u qf;
      for(int sp=0;sp<SPIN_CAP;++sp){ FENCE();
        qf=ld128<AUX_SC1>(rws,WS_Q1+(th*4+pr)*2048+(w&1)*1024+lane*16);
        if(__all((qf.y==tag)&(qf.w==tag))) break;
        __builtin_amdgcn_s_sleep(SLP_E1);
      }
      const int t=4*(w&1)+g;
      *(LAS v2u*)(L+L_QL+pr*1024+(t>>1)*256+(lane&15)*16+(t&1)*8)=v2u{qf.x,qf.z};
    }
    BAR();
    STAMP(3);
    {
      v4u qb[4];
      #pragma unroll
      for(int k=0;k<4;k++) qb[k]=lrd128(L+L_QL+q4*1024+k*256+g*64+p4*16);
      SB();
      f32x4 acc={0,0,0,0};
      #pragma unroll
      for(int k=0;k<4;k++) acc=mfma16(auk[k],qb[k],acc);
      #pragma unroll
      for(int r=0;r<4;r++){ acc[r]+=dpp<DROR4>(acc[r]); acc[r]+=dpp<DROR8>(acc[r]); }
      const float SC=0.08838834764831845f*1.4426950408889634f;
      if((lane&15)<4) *(LAS v2u*)(L+L_QS+(lane&15)*256+(16*w+4*g)*2)=v2u{nos(pk2(acc[0]*SC,acc[1]*SC)),nos(pk2(acc[2]*SC,acc[3]*SC))};
    }
    BAR();
    if(w==0){ v4u v=lrd128(L+L_QS+lane*16); st128(rws,WS_QA+g*32768+th*1024+tq*256+(lane&15)*16,v); }
    STAMP(4);
    while(rt()-t_start<T_TAIL) __builtin_amdgcn_s_sleep(1);
    u32 b1raw[4];
    #pragma unroll
    for(int r=0;r<4;r++) b1raw[r]=__builtin_amdgcn_raw_buffer_load_b16(mkrs(b1,FF*2),(16*fi+4*g+r)*2,0,0);
    u32 xraw=__builtin_amdgcn_raw_buffer_load_b16(mkrs(x,NB*DM*2),(p4*DM+4*fi+q4)*2,0,0);
    u32 b2raw=__builtin_amdgcn_raw_buffer_load_b16(mkrs(b2,DM*2),(4*fi+q4)*2,0,0);
    if(mlp){
      #pragma unroll
      for(int i=0;i<2;i++){ const int r=8*i+(lane>>3); dma16(w1,(u32)((128*tq+16*w+r)*(FF*2)+th*128+16*((lane&7)^(2*(((r>>1)&1)|(i<<1))))),Lb+L_W1+(16*w+8*i)*128); }
    }
    #pragma unroll
    for(int i=0;i<4;i++) DMA_T(wuv,L_WUV,i);
    #pragma unroll
    for(int i=0;i<4;i++){ const int r=8*i+w; dma16(wo,(u32)((th*128+32*tq+r)*1024+16*(lane^(2*(r&7)))),Lb+L_WO+r*1024); }
    if(mlp){
      #pragma unroll
      for(int i=0;i<2;i++){ const int r=8*i+w; dma16(w2,(u32)((16*fi+r)*1024+16*(lane^(2*(r&7)))),Lb+L_W2+r*1024); }
      VMW(10);
      BAR();
      if(w<4){
        v4u a1[4], bq[4];
        #pragma unroll
        for(int ks=0;ks<4;ks++){ lds8* a=L+L_W1+(32*ks+8*g+q4)*128+32*(w^((q4>>1)|((g&1)<<1)))+p4*8; a1[ks]=trd2(a,a+512); bq[ks]=lrd128(L+L_XN+p4*1088+(128*tq+32*ks+8*g)*2); }
        SB();
        f32x4 acc={0,0,0,0};
        #pragma unroll
        for(int ks=0;ks<4;ks++) acc=mfma16(a1[ks],bq[ks],acc);
        if((lane&15)<4){
          st128(rws,WS_HM+fi*2048+w*512+g*128+p4*16,v4u{__float_as_uint(acc[0]),tag,__float_as_uint(acc[1]),tag});
          st128(rws,WS_HM+fi*2048+w*512+g*128+64+p4*16,v4u{__float_as_uint(acc[2]),tag,__float_as_uint(acc[3]),tag}); }
      }
      if(w==4){
        v4u h0,h1;
        for(int sp=0;sp<SPIN_CAP;++sp){ FENCE();
          h0=ld128<AUX_SC1>(rws,WS_HM+(th*4+q4)*2048+tq*512+g*128+p4*16);
          h1=ld128<AUX_SC1>(rws,WS_HM+(th*4+q4)*2048+tq*512+g*128+64+p4*16);
          if(__all((h0.y==tag)&(h0.w==tag)&(h1.y==tag)&(h1.w==tag))) break;
          __builtin_amdgcn_s_sleep(SLP);
        }
        float hv[4]={__uint_as_float(h0.x),__uint_as_float(h0.z),__uint_as_float(h1.x),__uint_as_float(h1.z)};
        #pragma unroll
        for(int r=0;r<4;r++){ float sv=hv[r]; sv+=dpp<DROR4>(sv); sv+=dpp<DROR8>(sv); float h=rbf(rbf(sv)+blo(b1raw[r])); h=h>0.f?h:0.f; hv[r]=q4==0? h*h:0.f; }
        *(LAS v2u*)(L+L_H2+lane*8)=v2u{pk2(hv[0],hv[1]),pk2(hv[2],hv[3])};
      }
    }
    VMW(0);
    BAR();
    STAMP(6);
    asm volatile("" : "+v"(xraw), "+v"(b2raw));
    const float xb_res=blo(xraw)+blo(b2raw);
    f32x4 oacc[4];
    v4u auv[4], awo[4];
    {
      #pragma unroll
      for(int k=0;k<4;k++){ lds8* a=L+L_WUV+k*8192+troff; auv[k]=trd2(a,a+1024); }
      const int r0=4*g+q4;
      #pragma unroll
      for(int t=0;t<4;t++){ lds8* a=L+L_WO+r0*1024+(w>>1)*256+32*((4*(w&1)+t)^(r0&7))+p4*8; awo[t]=trd2(a,a+16384); }
      #pragma unroll
      for(int t=0;t<4;t++) oacc[t]=f32x4{0,0,0,0};
      if(mlp){
        const v2u h2=*(LAS v2u*)(L+L_H2+lane*8);
        #pragma unroll
        for(int t=0;t<4;t++){ const v2u a=trd(L+L_W2+r0*1024+(w>>1)*256+32*((4*(w&1)+t)^(r0&7))+p4*8); oacc[t]=mfma16(v4u{a.x,a.y,0,0},v4u{h2.x,h2.y,0,0},oacc[t]); }
      }
    }
    SB();
    if(w==0){
      while(rt()-t_start<T_GUN) __builtin_amdgcn_s_sleep(1);
      v2u v0,v1;
      for(int sp=0;sp<SPIN_CAP;++sp){ FENCE();
        v0=__builtin_amdgcn_raw_buffer_load_b64(rws,WS_LSE+(th>>2)*4096+lane*32+(th&3)*8,0,AUX_SC1);
        v1=__builtin_amdgcn_raw_buffer_load_b64(rws,WS_LSE+(th>>2)*4096+2048+lane*32+(th&3)*8,0,AUX_SC1);
        if(__all((v0.y==tag)&(v1.y==tag))) break; __builtin_amdgcn_s_sleep(SLP_GUN);
      }
      FENCE();
      STAMP(7);
      float l0=__uint_as_float(v0.x), l1=__uint_as_float(v1.x), m0=l0, m1=l1;
      m0=fmaxf(m0,dpp<DXOR1>(m0)); m1=fmaxf(m1,dpp<DXOR1>(m1)); m0=fmaxf(m0,dpp<DXOR2>(m0)); m1=fmaxf(m1,dpp<DXOR2>(m1));
      m0=fmaxf(m0,dpp<DHMIR>(m0)); m1=fmaxf(m1,dpp<DHMIR>(m1)); m0=fmaxf(m0,dpp<DROR8>(m0)); m1=fmaxf(m1,dpp<DROR8>(m1)); m0=sw16max(m0); m1=sw16max(m1);
      const float e0=__builtin_amdgcn_exp2f(l0-m0), e1=__builtin_amdgcn_exp2f(l1-m1);
      float s0=e0, s1=e1;
      s0+=dpp<DXOR1>(s0); s1+=dpp<DXOR1>(s1); s0+=dpp<DXOR2>(s0); s1+=dpp<DXOR2>(s1); s0+=dpp<DHMIR>(s0); s1+=dpp<DHMIR>(s1); s0+=dpp<DROR8>(s0); s1+=dpp<DROR8>(s1);
      s0=sw16add(s0,s0); s1=sw16add(s1,s1);
      *(LAS float*)(L+L_WG+hh*128+(l31&7)*16+(l31>>3)*4)=e0*__builtin_amdgcn_rcpf(s0);
      *(LAS float*)(L+L_WG+256+hh*128+(l31&7)*16+(l31>>3)*4)=e1*__builtin_amdgcn_rcpf(s1);
    } else {
    }
    BAR();
    {
      const int b=w>>1, half=w&1, slo=lane>>3, q=lane&7;
      v4u po[4];
      #pragma unroll
      for(int i=0;i<4;i++) po[i]=ld128<AUX_SC1>(rws,WS_PO+(fi*128+b*32+slo+8*i)*256+half*128+q*16);
      const f32x4 e=*(LAS f32x4*)(L+L_WG+b*128+slo*16);
      STAMP(8);
      float a[8];
      #pragma unroll
      for(int k=0;k<8;k++) a[k]=0.f;
      #pragma unroll
      for(int i=0;i<4;i++){
        a[0]+=e[i]*blo(po[i].x); a[1]+=e[i]*bhi(po[i].x); a[2]+=e[i]*blo(po[i].y); a[3]+=e[i]*bhi(po[i].y);
        a[4]+=e[i]*blo(po[i].z); a[5]+=e[i]*bhi(po[i].z); a[6]+=e[i]*blo(po[i].w); a[7]+=e[i]*bhi(po[i].w);
      }
      float c4[4], c2[2];
      #pragma unroll
      for(int k=0;k<4;k++) c4[k]=sw32add(a[k],a[k+4]);
      c2[0]=sw16add(c4[0],c4[2]); c2[1]=sw16add(c4[1],c4[3]);
      const bool b3=(lane>>3)&1;
      const float keep=b3?c2[1]:c2[0], send=b3?c2[0]:c2[1];
      const float r=keep+dpp<DROR8>(send);
      const int col=8*q+4*hh+2*((lane>>4)&1)+(b3?1:0);
      *(LAS u16*)(L+L_CL+b*320+(half*64+col)*2)=(u16)pk2(r,0.f);
    }
    BAR();
    STAMP(9);
    {
      v4u bc[4];
      #pragma unroll
      for(int k=0;k<4;k++) bc[k]=lrd128(L+L_CL+p4*320+(32*k+8*g)*2);
      SB();
      f32x4 acc={0,0,0,0};
      #pragma unroll
      for(int k=0;k<4;k++) acc=mfma16(auv[k],bc[k],acc);
      if((lane&15)<4) st128(rws,WS_CX+fi*2048+w*256+g*64+(lane&3)*16,v4u{pk2(acc[0],acc[1]),tag,pk2(acc[2],acc[3]),tag});
    }
    STAMP(10);
    __builtin_amdgcn_s_sleep(D_E3B);
    {
      v4u c0,c1;
      for(int sp=0;sp<SPIN_CAP;++sp){ FENCE();
        c0=ld128<AUX_SC1>(rws,WS_CX+(th*4+q4)*2048+(2*tq)*256+g*64+p4*16);
        c1=ld128<AUX_SC1>(rws,WS_CX+(th*4+q4)*2048+(2*tq+1)*256+g*64+p4*16);
        if(__all((c0.y==tag)&(c0.w==tag)&(c1.y==tag)&(c1.w==tag))) break;
        __builtin_amdgcn_s_sleep(SLP_E1);
      }
      STAMP(11);
      const v4u bc={c0.x,c0.z,c1.x,c1.z};
      #pragma unroll
      for(int t=0;t<4;t++) oacc[t]=mfma16(awo[t],bc,oacc[t]);
      const bool b3=(lane>>3)&1, b2_=(lane>>2)&1;
      f32x4 u[2], v;
      #pragma unroll
      for(int t=0;t<2;t++)
        #pragma unroll
        for(int r=0;r<4;r++){ const float keep=b3?oacc[t+2][r]:oacc[t][r], send=b3?oacc[t][r]:oacc[t+2][r]; u[t][r]=keep+dpp<DROR8>(send); }
      #pragma unroll
      for(int r=0;r<4;r++){ const float keep=b2_?u[1][r]:u[0][r], send=b2_?u[0][r]:u[1][r];
        v[r]=keep+__builtin_bit_cast(float,__builtin_amdgcn_ds_swizzle(__builtin_bit_cast(int,send),0x101F)); }
      st128(rws,WS_OUT+(16*w+4*q4+g)*16384+fi*128+p4*16,v4u{__float_as_uint(v[0]),tag,__float_as_uint(v[1]),tag});
      st128(rws,WS_OUT+(16*w+4*q4+g)*16384+fi*128+64+p4*16,v4u{__float_as_uint(v[2]),tag,__float_as_uint(v[3]),tag});
    }
    STAMP(12);
    { const u64 t1=rt(); while(rt()-t1<D_E4) __builtin_amdgcn_s_sleep(1); }
    v4u pv[2];
    #pragma unroll
    for(int i=0;i<2;i++) pv[i]=ld128<AUX_SC1>(rws,WS_OUT+fi*16384+i*8192+tid*16);
    for(int sp=0;sp<SPIN_CAP;++sp){
      const bool k0=(pv[0].y==tag)&(pv[0].w==tag), k1=(pv[1].y==tag)&(pv[1].w==tag);
      if(__all(k0&k1)) break;
      __builtin_amdgcn_s_sleep(SLP_E4); FENCE();
      if(!k0) pv[0]=ld128<AUX_SC1>(rws,WS_OUT+fi*16384+tid*16);
      if(!k1) pv[1]=ld128<AUX_SC1>(rws,WS_OUT+fi*16384+8192+tid*16);
    }
    STAMP(13);
    float v=sw32add(__uint_as_float(pv[0].x)+__uint_as_float(pv[1].x),__uint_as_float(pv[0].z)+__uint_as_float(pv[1].z));
    v=sw16add(v,v); v+=dpp<DROR8>(v);
    if((lane&24)==0) *(LAS float*)(L+L_RED+w*64+((2*((lane>>2)&1)+hh)*4+p4)*4)=v;
    BAR();
    if(tid<16){
      float a=0.f;
      #pragma unroll
      for(int ww=0;ww<8;ww++) a+=*(LAS float*)(L+L_RED+ww*64+tid*4);
      out[(tid&3)*DM+4*fi+(tid>>2)]=(u16)pk2(xb_res+a,0.f);
    }
    if(wg==0 && tid==0) __hip_atomic_store((gu64*)(ws+WS_EPOCH),want,RLX_AGENT);
  } else {
    const int ab=fi>>5, as=fi&31;
    const int fl=fsw(l31);
    const int blk=(lane>>4)&1, sgm=w>>1;
    v4u vf[8][2];
#define LOAD_VF() do{ _Pragma("unroll") for(int ks=0;ks<8;ks++) _Pragma("unroll") for(int t=0;t<2;t++){ \
        const int ch=8*(w&1)+4*t+2*blk+(p4>>1); \
        lds8* a0=L+L_CKV+(16*ks+8*hh+q4)*1024+256*sgm+16*(ch^((q4<<2)|(2*hh)))+8*(p4&1); \
        lds8* a1=L+L_CKV+(16*ks+8*hh+4+q4)*1024+256*sgm+16*(ch^((q4<<2)|(2*hh+1)))+8*(p4&1); \
        vf[ks][t]=trd2(a0,a1); } SB(); }while(0)
    float mx;
    u64 want=0;
    if(w>=4 || w==0){ const u64 e=*(const GAS u64*)(ws+WS_EPOCH); want=(((u64)__builtin_amdgcn_readfirstlane((u32)(e>>32))<<32)|(u32)__builtin_amdgcn_readfirstlane((u32)e))+1; }
    if(tid<4) *(volatile LAS u32*)(L+L_QAS+tid*8192)=0;
    BAR();
    if(w<4){
      const u16* gb=ckv+((size_t)ab*SK+as*128+32*w)*DC;
      while(rt()-t_start<T_CKV) __builtin_amdgcn_s_sleep(1);
      #pragma unroll
      for(int r=0;r<32;r++){
        if(r>=CKV_INFL) VMW(CKV_INFL-1);
        dma16(gb+r*DC,(u32)((lane*16)^(16*fsw(r))),Lb+L_CKV+(32*w+r)*1024);
        if(r==R_GO && w==0 && lane<4) *(volatile LAS u32*)(L+L_QAS+lane*8192)=1;
      }
      VMW(0);
      v4u af[32];
      #pragma unroll
      for(int k=0;k<32;k++) af[k]=lrd128(L+L_CKV+(32*w+l31)*1024+256*(k>>3)+16*((2*(k&7)+hh)^fl));
      BAR();
      f32x16 sacc;
      #pragma unroll
      for(int i=0;i<16;i++) sacc[i]=0.f;
      v4u bf[12];
      #pragma unroll
      for(int k=0;k<12;k++) bf[k]=lrd128(L+L_QAS+l31*1024+256*(k>>3)+16*((2*(k&7)+hh)^fl));
      SB();
      #pragma unroll
      for(int k=0;k<32;k++){
        sacc=mfma32(af[k],bf[k%12],sacc);
        if(k+12<32) bf[k%12]=lrd128(L+L_QAS+l31*1024+256*((k+12)>>3)+16*((2*((k+12)&7)+hh)^fl));
        SB();
      }
      float mloc=sacc[0];
      #pragma unroll
      for(int i=1;i<16;i++) mloc=fmaxf(mloc,sacc[i]);
      mloc=sw32max(mloc);
      BAR();
      STAMP(4);
      if(lane<32) *(LAS float*)(L+L_MX+w*128+lane*4)=mloc;
      BAR();
      mx=fmaxf(fmaxf(*(LAS float*)(L+L_MX+l31*4),*(LAS float*)(L+L_MX+128+l31*4)),fmaxf(*(LAS float*)(L+L_MX+256+l31*4),*(LAS float*)(L+L_MX+384+l31*4)));
      float ls=0.f;
      #pragma unroll
      for(int a=0;a<4;a++){
        float p0=__builtin_amdgcn_exp2f(sacc[4*a]-mx), p1=__builtin_amdgcn_exp2f(sacc[4*a+1]-mx), p2=__builtin_amdgcn_exp2f(sacc[4*a+2]-mx), p3=__builtin_amdgcn_exp2f(sacc[4*a+3]-mx);
        const u32 u0=pk2(p0,p1), u1=pk2(p2,p3);
        ls+=(blo(u0)+bhi(u0))+(blo(u1)+bhi(u1));
        *(LAS v2u*)(L+L_P+l31*256+16*((4*w+a)^fl)+8*hh)=v2u{u0,u1};
      }
      ls=sw32add(ls,ls);
      if(lane<32) *(LAS float*)(L+L_LS+w*128+lane*4)=ls;
      BAR();
      STAMP(5);
      LOAD_VF();
    } else {
      while(rt()-t_start<T_FLAG1) __builtin_amdgcn_s_sleep(1);
      for(int sp=0;sp<SPIN_CAP;++sp){ FENCE();
        v4u f=ld128<AUX_SC1>(rws,WS_FLAG1+xc*1024+(w-4)*256+(lane&15)*16);
        if(__all((f.x==(u32)want)&(f.y==(u32)(want>>32))&(f.z==(u32)want)&(f.w==(u32)(want>>32)))) break; __builtin_amdgcn_s_sleep(SLP);
      }
      FENCE();
      for(int sp=0;sp<SPIN_CAP && *(volatile LAS u32*)(L+L_QAS+(w-4)*8192)==0;++sp) __builtin_amdgcn_s_sleep(1);
      const int wv=w-4;
      v4u qa[8];
      #pragma unroll
      for(int i=0;i<8;i++) qa[i]=ld128<AUX_SC1>(rws,WS_QA+ab*32768+(8*wv+i)*1024+16*lane);
      for(int sp=0;sp<SPIN_CAP;++sp){
        u32 bad=0;
        #pragma unroll
        for(int i=0;i<8;i++) bad|=(__all(ok4(qa[i]))?0u:1u)<<i;
        if(!bad) break;
        __builtin_amdgcn_s_sleep(SLP_QA); FENCE();
        #pragma unroll
        for(int i=0;i<8;i++) if((bad>>i)&1) qa[i]=ld128<AUX_SC1>(rws,WS_QA+ab*32768+(8*wv+i)*1024+16*lane);
      }
      #pragma unroll
      for(int i=0;i<8;i++){ const int head=8*wv+i; *(LAS v4u*)(L+L_QAS+head*1024+((lane*16)^(16*fsw(head&15))))=qa[i]; }
      BAR();
      LOAD_VF();
      BAR();
      BAR();
      mx=0.f;
      BAR();
    }
    {
      v4u pb[8];
      #pragma unroll
      for(int ks=0;ks<8;ks++) pb[ks]=lrd128(L+L_P+l31*256+16*((2*ks+hh)^fl));
      const float lsum=(*(LAS float*)(L+L_LS+l31*4)+*(LAS float*)(L+L_LS+128+l31*4))+(*(LAS float*)(L+L_LS+256+l31*4)+*(LAS float*)(L+L_LS+384+l31*4));
      SB();
      f32x16 o[2];
      #pragma unroll
      for(int i=0;i<16;i++){ o[0][i]=0.f; o[1][i]=0.f; }
      #pragma unroll
      for(int t=0;t<2;t++)
        #pragma unroll
        for(int ks=0;ks<8;ks++) o[t]=mfma32(vf[ks][t],pb[ks],o[t]);
      const float inv=__builtin_amdgcn_rcpf(lsum);
      const float lse=mx+__builtin_amdgcn_logf(lsum);
      STAMP(6);
      #pragma unroll
      for(int t=0;t<2;t++)
        #pragma unroll
        for(int a=0;a<4;a++)
          *(LAS v2u*)(L+L_CKV+l31*1024+256*sgm+16*((8*(w&1)+4*t+a)^fl)+8*hh)=v2u{pk2(o[t][4*a]*inv,o[t][4*a+1]*inv),pk2(o[t][4*a+2]*inv,o[t][4*a+3]*inv)};
      v4u ov[4];
      #pragma unroll
      for(int i=0;i<4;i++){ const int head=8*i+(lane>>3); ov[i]=lrd128(L+L_CKV+head*1024+256*sgm+16*((8*(w&1)+(lane&7))^fsw(head))); }
      SB();
      #pragma unroll
      for(int i=0;i<4;i++){ const int head=8*i+(lane>>3); st128(rws,WS_PO+((head*4+sgm)*128+ab*32+as)*256+(w&1)*128+(lane&7)*16,ov[i]); }
      STAMP(7);
      VMW(0);
      BAR();
      if(w==0 && lane<32) __builtin_amdgcn_raw_buffer_store_b64(v2u{__float_as_uint(lse),(u32)want},rws,WS_LSE+(lane>>2)*4096+fi*32+(lane&3)*8,0,AUX_SC1);
      STAMP(8);
    }
  }
  STAMP(15);
}

extern "C" void kernel_launch(void* const* d_in,const int* in_sizes,int n_in,void* d_out,int out_size,void* d_ws,size_t ws_size,hipStream_t stream){
  (void)hipFuncSetAttribute((const void*)mla_mlp_kernel,hipFuncAttributeMaxDynamicSharedMemorySize,LDS_BYTES);
  mla_mlp_kernel<<<256,512,LDS_BYTES,stream>>>((const u16*)d_in[0],(const u16*)d_in[1],(const u16*)d_in[2],(const u16*)d_in[3],(const u16*)d_in[4],(const u16*)d_in[5],
     (const u16*)d_in[7],(const u16*)d_in[8],(const u16*)d_in[9],(const u16*)d_in[10],(u16*)d_out,(unsigned char*)d_ws);
}
```

```cpp
#include <hip/hip_runtime.h>
#include <cstring>
typedef unsigned short u16; typedef unsigned u32; typedef unsigned long long u64;
#define GAS __attribute__((address_space(1)))
#define LAS __attribute__((address_space(3)))
typedef u32 v4u __attribute__((ext_vector_type(4)));
typedef u32 v2u __attribute__((ext_vector_type(2)));
typedef float f32x2 __attribute__((ext_vector_type(2)));
typedef float f32x4 __attribute__((ext_vector_type(4)));
typedef float f32x16 __attribute__((ext_vector_type(16)));
typedef __bf16 bf16x8 __attribute__((ext_vector_type(8)));
typedef __bf16 bf16x2 __attribute__((ext_vector_type(2)));
typedef short s16x4 __attribute__((ext_vector_type(4)));
typedef GAS u64 gu64;
typedef LAS unsigned char lds8;
#define RLX_AGENT __ATOMIC_RELAXED, __HIP_MEMORY_SCOPE_AGENT
#define DI __device__ __forceinline__

constexpr int NB=4, NH=32, SK=4096, HD=128, DM=512, DC=512, FF=1792;
constexpr int WS_EPOCH=0, WS_FLAG1=8192, WS_FLAG2=16384, WS_Q1=32768, WS_CX=WS_Q1+262144, WS_HM=WS_CX+262144, WS_QA=WS_HM+262144, WS_LSE=WS_QA+262144, WS_PO=WS_LSE+32768,
              WS_OUT=WS_PO+(4<<20), WS_END=WS_OUT+(2<<20), WS_STAMP=WS_END;
constexpr int L_CKV=0, L_QAS=131072, L_P=L_QAS, L_MX=L_QAS+8192, L_LS=L_QAS+8704, L_AWQ=L_QAS, L_AXN=126*1024, L_AEP=L_AXN+1536, L_AWO=0, L_ABC=32768;
constexpr int L_WUK=0, L_WO=0, L_W2=81920, L_WUV=98304, L_W1=131072, L_XN=147456, L_QS=148736, L_CL=149760, L_H2=151040, L_RED=151552, L_EP=152064, L_QL=152576, L_WG=156672, L_CX=157184, L_ZERO=158464, L_STF=158720;
constexpr int LDS_BYTES=163840;
constexpr u32 SENT=0xFFFFFFFFu;
constexpr int SPIN_CAP=1<<20;
constexpr int AUX_NT=2, AUX_SC1=16;
#define KNOB(n,v) constexpr int n##_=v
#define CKV_INFL 12
#define NCLR 4
#define T_TAIL 460
#define T_CKV0 120
#define SLP_G 1
#define QA_COPY (xc&1)
#define WO_1 4
#define T_WO 0
#define RW_W 6
#define T_E1 150
#define T_FLAG1 240
#define T_GATHER 430
#define T_CLR 420
#define T_GUN 620
#define T_CX 780
#define SLP 4
#define SLP_S 1
#define D_E4 50
#define D_MG 60
static_assert(CKV_INFL<=30,"ring");
#define STAMP(k) do{}while(0)
#define ESTAMP(i) do{}while(0)

DI u64 rt(){ return __builtin_amdgcn_s_memrealtime(); }
DI u64 ct(){ u64 t; asm volatile("s_memtime %0\n\ts_waitcnt lgkmcnt(0)" : "=s"(t) :: "memory"); return t; }
DI __amdgpu_buffer_rsrc_t mkrs(const void* p, int bytes){ return __builtin_amdgcn_make_buffer_rsrc((void*)p,0,bytes,0x00020000); }
template<int AUX> DI v4u ld128(__amdgpu_buffer_rsrc_t rs,int off){ return __builtin_amdgcn_raw_buffer_load_b128(rs,off,0,AUX); }
template<int AUX> DI u32 ld32(__amdgpu_buffer_rsrc_t rs,int off){ return __builtin_amdgcn_raw_buffer_load_b32(rs,off,0,AUX); }
DI void st128(__amdgpu_buffer_rsrc_t rs,int off,v4u v){ __builtin_amdgcn_raw_buffer_store_b128(v,rs,off,0,AUX_SC1); }
DI void st32(__amdgpu_buffer_rsrc_t rs,int off,u32 v){ __builtin_amdgcn_raw_buffer_store_b32(v,rs,off,0,AUX_SC1); }
DI f32x4 mfma16(v4u a,v4u b,f32x4 c){ return __builtin_amdgcn_mfma_f32_16x16x32_bf16(__builtin_bit_cast(bf16x8,a),__builtin_bit_cast(bf16x8,b),c,0,0,0); }
DI f32x16 mfma32(v4u a,v4u b,f32x16 c){ return __builtin_amdgcn_mfma_f32_32x32x16_bf16(__builtin_bit_cast(bf16x8,a),__builtin_bit_cast(bf16x8,b),c,0,0,0); }
DI v2u trd(lds8* p){ return __builtin_bit_cast(v2u,__builtin_amdgcn_ds_read_tr16_b64_v4i16((LAS s16x4*)p)); }
DI v4u trd2(lds8* p0,lds8* p1){ v2u a=trd(p0), b=trd(p1); return v4u{a.x,a.y,b.x,b.y}; }
DI v4u lrd128(lds8* p){ return *(LAS v4u*)p; }
DI u32 pk2(float lo,float hi){ f32x2 f={lo,hi}; return __builtin_bit_cast(u32,__builtin_convertvector(f,bf16x2)); }
DI float rbf(float x){ return __uint_as_float(pk2(x,0.f)<<16); }
DI float vmax(float a,float b){ return fmaxf(a,b); }
DI float vmax3(float a,float b,float c){ return fmaxf(fmaxf(a,b),c); }
DI float blo(u32 w){ return __uint_as_float(w<<16); }
DI float bhi(u32 w){ return __uint_as_float(w&0xFFFF0000u); }
DI u32 nos(u32 w){ return w<0xFFFFFFFEu? w:0xFFFFFFFEu; }
DI v4u nos4(v4u v){ return v4u{nos(v.x),nos(v.y),nos(v.z),nos(v.w)}; }
DI bool ok4(v4u v){ return (v.x!=SENT)&(v.y!=SENT)&(v.z!=SENT)&(v.w!=SENT); }
#define BAR() asm volatile("s_waitcnt lgkmcnt(0)\n\ts_barrier" ::: "memory")
#define VMW(n) asm volatile("s_waitcnt vmcnt(%0)" :: "i"(n) : "memory")
#define FENCE() asm volatile("" ::: "memory")
#define SB() __builtin_amdgcn_sched_barrier(0)
template<int CTRL> DI float dpp(float x){ return __builtin_bit_cast(float,__builtin_amdgcn_mov_dpp(__builtin_bit_cast(int,x),CTRL,0xf,0xf,true)); }
constexpr int DXOR1=0xB1, DXOR2=0x4E, DHMIR=0x141, DROR8=0x128, DROR4=0x124;
DI float sw32add(float a,float b){ auto s=__builtin_amdgcn_permlane32_swap(__float_as_uint(a),__float_as_uint(b),false,false); return __uint_as_float(s[0])+__uint_as_float(s[1]); }
DI float sw16add(float a,float b){ auto s=__builtin_amdgcn_permlane16_swap(__float_as_uint(a),__float_as_uint(b),false,false); return __uint_as_float(s[0])+__uint_as_float(s[1]); }
DI float sw32max(float a){ auto s=__builtin_amdgcn_permlane32_swap(__float_as_uint(a),__float_as_uint(a),false,false); return vmax(__uint_as_float(s[0]),__uint_as_float(s[1])); }
DI float sw16max(float a){ auto s=__builtin_amdgcn_permlane16_swap(__float_as_uint(a),__float_as_uint(a),false,false); return vmax(__uint_as_float(s[0]),__uint_as_float(s[1])); }
DI float wave_sum(float v){ v+=dpp<DXOR1>(v); v+=dpp<DXOR2>(v); v+=dpp<DHMIR>(v); v+=dpp<DROR8>(v); v=sw16add(v,v); v=sw32add(v,v); return v; }
DI void dma16(const void* gbase,u32 voff,u32 ldsb){
  u32 keep;
  asm volatile("s_mov_b32 %0, m0\n\ts_mov_b32 m0, %3\n\ts_nop 0\n\tglobal_load_lds_dwordx4 %1, %2 nt\n\ts_mov_b32 m0, %0" : "=&s"(keep) : "v"(voff),"s"(gbase),"s"(ldsb) : "memory");
}
DI constexpr int fsw(int row){ return ((row&3)<<2)|((row>>2)&3); }
DI bool flag_ok(v4u f,u64 want){ return (f.x==(u32)want)&(f.y==(u32)(want>>32))&(f.z==(u32)want)&(f.w==(u32)(want>>32)); }
DI void wait_t(u64 t0,int ticks){ while(rt()-t0<(u64)ticks) __builtin_amdgcn_s_sleep(1); }
DI void ln_row(v4u xr,int lane,int tq,lds8* dst){
  float v[8]={blo(xr.x),bhi(xr.x),blo(xr.y),bhi(xr.y),blo(xr.z),bhi(xr.z),blo(xr.w),bhi(xr.w)};
  const float mean=wave_sum(((v[0]+v[1])+(v[2]+v[3]))+((v[4]+v[5])+(v[6]+v[7])))*(1.f/DM);
  float qq=0.f;
  #pragma unroll
  for(int i=0;i<8;i++){ const float d=v[i]-mean; qq+=d*d; }
  const float var=wave_sum(qq)*(1.f/DM);
  const float mb=rbf(mean), isd=__builtin_amdgcn_rcpf(rbf(__builtin_amdgcn_sqrtf(rbf(rbf(var)+1e-5f))));
  float y[8];
  #pragma unroll
  for(int i=0;i<8;i++) y[i]=rbf(v[i]-mb)*isd;
  if((lane>>4)==tq) *(LAS v4u*)(dst+16*(lane&15))=v4u{pk2(y[0],y[1]),pk2(y[2],y[3]),pk2(y[4],y[5]),pk2(y[6],y[7])};
}

__global__ void __launch_bounds__(512,2) mla_mlp_kernel(const u16* __restrict__ x,const u16* __restrict__ ckv,const u16* __restrict__ wq,const u16* __restrict__ wuk,
    const u16* __restrict__ wuv,const u16* __restrict__ wo,const u16* __restrict__ w1,const u16* __restrict__ b1,const u16* __restrict__ w2,const u16* __restrict__ b2,
    u16* __restrict__ out,unsigned char* __restrict__ ws){
  extern __shared__ __attribute__((aligned(16))) unsigned char smem[];
  lds8* L=(lds8*)smem;
  const u32 Lb=(u32)(size_t)L;
  const int tid=threadIdx.x, lane=tid&63, w=__builtin_amdgcn_readfirstlane(tid>>6);
  const int wg=blockIdx.x, xc=wg&7, j=wg>>3, jj=j&15;
  const bool isF=j<16;
  const int fi=xc*16+jj, th=fi>>2, tq=fi&3;
  const int g=lane>>4, q4=(lane&15)>>2, p4=lane&3, hh=lane>>5, l31=lane&31;
  const __amdgpu_buffer_rsrc_t rws=mkrs(ws,WS_END);
  const u64 t_start=rt();
  asm volatile("" :: "s"(x),"s"(ckv),"s"(wq),"s"(wuk),"s"(ws));
  const u32 rowoff=(u32)((128*tq+16*w+g)*8192+th*256);
#define DMA_T(p,dst,i) dma16(p,rowoff+(u32)(4*(i)*8192)+16*((lane&15)^(2*(g|(((i)>>1)<<2)))),Lb+(dst)+(16*w+4*(i))*256)
  const int troff=(8*g+q4)*256+(w^(q4|((g&1)<<2)))*32+p4*8;
  const int r0=4*g+q4;
  if(isF){
    const bool mlp = fi<FF/16;
    if(w==7){
      v2u e2;
      st128(rws,WS_QA+g*32768+th*1024+tq*256+(lane&15)*16,v4u{SENT,SENT,SENT,SENT});
      st128(rws,WS_QA+131072+g*32768+th*1024+tq*256+(lane&15)*16,v4u{SENT,SENT,SENT,SENT});
      asm volatile("global_load_dwordx2 %0, %1, %2" : "=v"(e2) : "v"(0), "s"(ws) : "memory");
      #pragma unroll
      for(int i=0;i<4;i++) dma16(wuk,rowoff+(u32)(4*i*8192)+16*((lane&15)^(4*i+g)),Lb+L_WUK+(16*w+4*i)*256);
      asm volatile("s_waitcnt vmcnt(4)" : "+v"(e2) :: "memory");
      const u64 wn=(((u64)e2.y<<32)|e2.x)+1; if(lane<8) __hip_atomic_store((gu64*)(ws+WS_FLAG1)+lane*128+fi,wn,RLX_AGENT);
      if(lane==0) *(LAS u64*)(L+L_EP)=wn;
      VMW(1);
    } else {
      #pragma unroll
      for(int i=0;i<4;i++) dma16(wuk,rowoff+(u32)(4*i*8192)+16*((lane&15)^(4*i+g)),Lb+L_WUK+(16*w+4*i)*256);
      VMW(0);
    }
    v4u auk[4];
    #pragma unroll
    for(int k=0;k<4;k++){
      lds8* a=L+L_WUK+(16*w+(lane&15))*256+8*(g&1);
      const v2u lo=*(LAS v2u*)(a+16*((4*k+(g>>1))^(lane&15))), hi=*(LAS v2u*)(a+16*((4*k+2+(g>>1))^(lane&15)));
      auk[k]=v4u{lo.x,lo.y,hi.x,hi.y};
    }
    BAR();
    STAMP(1);
    const v2u ep=*(LAS v2u*)(L+L_EP);
    const u32 tag=__builtin_amdgcn_readfirstlane(ep.x);
    const u64 want=((u64)__builtin_amdgcn_readfirstlane(ep.y)<<32)|tag;
    wait_t(t_start,T_E1);
    {
      v4u qf;
      _Pragma("unroll 1") for(int sp=0;sp<SPIN_CAP;++sp){ FENCE();
        qf=ld128<AUX_SC1>(rws,WS_Q1+th*8192+w*1024+lane*16);
        if(__all((qf.y==tag)&(qf.w==tag))) break;
        __builtin_amdgcn_s_sleep(SLP_S);
      }
      const int t=4*(w&1)+g;
      *(LAS v2u*)(L+L_QL+(w>>1)*1024+(t>>1)*256+(lane&15)*16+(t&1)*8)=v2u{qf.x,qf.z};
    }
    BAR();
    STAMP(2);
    {
      v4u qb[4];
      #pragma unroll
      for(int k=0;k<4;k++) qb[k]=lrd128(L+L_QL+q4*1024+k*256+g*64+p4*16);
      SB();
      f32x4 acc={0,0,0,0};
      #pragma unroll
      for(int k=0;k<4;k++) acc=mfma16(auk[k],qb[k],acc);
      #pragma unroll
      for(int r=0;r<4;r++){ acc[r]+=dpp<DROR4>(acc[r]); acc[r]+=dpp<DROR8>(acc[r]); }
      const float SC=0.08838834764831845f*1.4426950408889634f;
      if((lane&15)<4) *(LAS v2u*)(L+L_QS+(lane&15)*256+(16*w+4*g)*2)=v2u{nos(pk2(acc[0]*SC,acc[1]*SC)),nos(pk2(acc[2]*SC,acc[3]*SC))};
    }
    BAR();
    if(w<2){ v4u v=lrd128(L+L_QS+lane*16); st128(rws,WS_QA+w*131072+g*32768+th*1024+tq*256+(lane&15)*16,v); }
    STAMP(3);
    u32 b1raw[4];
    #pragma unroll
    for(int r=0;r<4;r++) b1raw[r]=__builtin_amdgcn_raw_buffer_load_b16(mkrs(b1,FF*2),(16*fi+4*g+r)*2,0,0);
    u32 xraw=__builtin_amdgcn_raw_buffer_load_b16(mkrs(x,NB*DM*2),(p4*DM+4*fi+q4)*2,0,0);
    u32 b2raw=__builtin_amdgcn_raw_buffer_load_b16(mkrs(b2,DM*2),(4*fi+q4)*2,0,0);
    SB();
#define DMA_WO(i) dma16(wo,(u32)((th*128+8*(i)+w)*1024+16*(lane^(2*((w&3)|(((i)&1)<<2))))),Lb+L_WO+(8*(i)+w)*1024)
    wait_t(t_start,T_WO);
    #pragma unroll
    for(int i=0;i<10;i++){ if(i>=WO_1) VMW(WO_1-1); DMA_WO(i); }
    wait_t(t_start,T_TAIL);
    v4u xr;
    asm volatile("global_load_dwordx4 %0, %1, %2" : "=v"(xr) : "v"((w&3)*1024+16*lane), "s"(x) : "memory");
    const int thm=mlp?th:0, fim=mlp?fi:0;
    #pragma unroll
    for(int i=0;i<2;i++){ const int r=8*i+(lane>>3); dma16(w1,(u32)((128*tq+16*w+r)*(FF*2)+thm*128+16*((lane&7)^(2*(((r>>1)&1)|(i<<1))))),Lb+L_W1+(16*w+8*i)*128); }
    #pragma unroll
    for(int i=0;i<4;i++) DMA_T(wuv,L_WUV,i);
    #pragma unroll
    for(int i=0;i<2;i++){ const int r=8*i+w; dma16(w2,(u32)((16*fim+r)*1024+16*(lane^(2*(r&7)))),Lb+L_W2+r*1024); }
    v4u rwo[6];
    #pragma unroll
    for(int i=0;i<6;i++) asm volatile("global_load_dwordx4 %0, %1, %2 nt" : "=v"(rwo[i]) : "v"((th*128+80+8*i+w)*1024+16*lane), "s"(wo) : "memory");
    asm volatile("s_waitcnt vmcnt(12)" : "+v"(xr) :: "memory");
    STAMP(4);
    if(w<4) ln_row(xr,lane,tq,L+L_XN+w*320);
    BAR();
    if(mlp && w<4){
      v4u a1[4], bq[4];
      #pragma unroll
      for(int ks=0;ks<4;ks++){ lds8* a=L+L_W1+(32*ks+8*g+q4)*128+32*(w^((q4>>1)|((g&1)<<1)))+p4*8; a1[ks]=trd2(a,a+512); bq[ks]=lrd128(L+L_XN+p4*320+(32*ks+8*g)*2); }
      SB();
      f32x4 acc={0,0,0,0};
      #pragma unroll
      for(int ks=0;ks<4;ks++) acc=mfma16(a1[ks],bq[ks],acc);
      if((lane&15)<4){
        st128(rws,WS_HM+fi*2048+w*512+g*128+p4*16,v4u{__float_as_uint(acc[0]),tag,__float_as_uint(acc[1]),tag});
        st128(rws,WS_HM+fi*2048+w*512+g*128+64+p4*16,v4u{__float_as_uint(acc[2]),tag,__float_as_uint(acc[3]),tag}); }
    }
    VMW(RW_W);
    BAR();
    v4u auv[4]; v2u aw2[4];
    #pragma unroll
    for(int k=0;k<4;k++){ lds8* a=L+L_WUV+k*8192+troff; auv[k]=trd2(a,a+1024); }
    #pragma unroll
    for(int t=0;t<4;t++) aw2[t]=trd(L+L_W2+r0*1024+(w>>1)*256+32*((4*(w&1)+t)^(r0&7))+p4*8);
    BAR();
    if(mlp && w==4){
      v4u h0,h1;
      _Pragma("unroll 1") for(int sp=0;sp<SPIN_CAP;++sp){ FENCE();
        h0=ld128<AUX_SC1>(rws,WS_HM+(th*4+q4)*2048+tq*512+g*128+p4*16);
        h1=ld128<AUX_SC1>(rws,WS_HM+(th*4+q4)*2048+tq*512+g*128+64+p4*16);
        if(__all((h0.y==tag)&(h0.w==tag)&(h1.y==tag)&(h1.w==tag))) break;
        __builtin_amdgcn_s_sleep(SLP);
      }
      float hv[4]={__uint_as_float(h0.x),__uint_as_float(h0.z),__uint_as_float(h1.x),__uint_as_float(h1.z)};
      #pragma unroll
      for(int r=0;r<4;r++){ float sv=hv[r]; sv+=dpp<DROR4>(sv); sv+=dpp<DROR8>(sv); float h=rbf(rbf(sv)+blo(b1raw[r])); h=h>0.f?h:0.f; hv[r]=h*h; }
      *(LAS v2u*)(L+L_H2+lane*8)=v2u{pk2(hv[0],hv[1]),pk2(hv[2],hv[3])};
    }
    asm volatile("s_waitcnt vmcnt(0)" : "+v"(rwo[0]),"+v"(rwo[1]),"+v"(rwo[2]),"+v"(rwo[3]),"+v"(rwo[4]),"+v"(rwo[5]) :: "memory");
    #pragma unroll
    for(int i=0;i<6;i++) *(LAS v4u*)(L+L_WO+(80+8*i+w)*1024+16*(lane^(2*((w&3)|((i&1)<<2)))))=rwo[i];
    v4u awo[4][4];
#define LOAD_AWO(ks) _Pragma("unroll") for(int t=0;t<4;t++){ lds8* a=L+L_WO+(32*(ks)+8*g+q4)*1024+(w>>1)*256+p4*8+32*((4*(w&1)+t)^(q4|((g&1)<<2))); awo[ks][t]=trd2(a,a+4096); }
    LOAD_AWO(0); LOAD_AWO(1);
    SB();
    STAMP(5);
    BAR();
    asm volatile("" : "+v"(xraw), "+v"(b2raw));
    const float xb_res=blo(xraw)+blo(b2raw);
    LOAD_AWO(2); LOAD_AWO(3);
    f32x4 oacc[4];
    #pragma unroll
    for(int t=0;t<4;t++) oacc[t]=f32x4{0,0,0,0};
    if(mlp){
      const v2u h2=*(LAS v2u*)(L+L_H2+lane*8);
      #pragma unroll
      for(int t=0;t<4;t++) oacc[t]=mfma16(v4u{aw2[t].x,aw2[t].y,0,0},v4u{h2.x,h2.y,0,0},oacc[t]);
    }
    SB();
    STAMP(6);
    if(w==0){
      wait_t(t_start,T_GUN);
      v2u v0,v1;
      _Pragma("unroll 1") for(int sp=0;sp<SPIN_CAP;++sp){ FENCE();
        v0=__builtin_amdgcn_raw_buffer_load_b64(rws,WS_LSE+(th>>2)*4096+lane*32+(th&3)*8,0,AUX_SC1);
        v1=__builtin_amdgcn_raw_buffer_load_b64(rws,WS_LSE+(th>>2)*4096+2048+lane*32+(th&3)*8,0,AUX_SC1);
        if(__all((v0.y==tag)&(v1.y==tag))) break; __builtin_amdgcn_s_sleep(SLP_G);
      }
      STAMP(7);
      *(LAS u32*)(L+L_WG+hh*128+(l31&7)*16+(l31>>3)*4)=v0.x;
      *(LAS u32*)(L+L_WG+256+hh*128+(l31&7)*16+(l31>>3)*4)=v1.x;
    }
    BAR();
    {
      const int b=w>>1, half=w&1, slo=lane>>3, q=lane&7;
      const int po_off=WS_PO+(fi*128+b*32+slo)*256+half*128+q*16;
      v4u po[4];
      #pragma unroll
      for(int i=0;i<4;i++) po[i]=ld128<AUX_SC1>(rws,po_off+i*2048);
      SB();
      const f32x4 lf=*(LAS f32x4*)(L+L_WG+b*128+slo*16);
      float M=vmax(vmax3(lf[0],lf[1],lf[2]),lf[3]);
      M=vmax(M,dpp<DROR8>(M)); M=sw16max(M); M=sw32max(M);
      f32x4 e;
      #pragma unroll
      for(int i=0;i<4;i++) e[i]=__builtin_amdgcn_exp2f(lf[i]-M);
      float Ls=(e[0]+e[1])+(e[2]+e[3]);
      Ls+=dpp<DROR8>(Ls); Ls=sw16add(Ls,Ls); Ls=sw32add(Ls,Ls);
      e*=__builtin_amdgcn_rcpf(Ls);
      SB();
      STAMP(8);
      float a[8];
      #pragma unroll
      for(int k=0;k<8;k++) a[k]=0.f;
      #pragma unroll
      for(int i=0;i<4;i++){
        a[0]+=e[i]*blo(po[i].x); a[1]+=e[i]*bhi(po[i].x); a[2]+=e[i]*blo(po[i].y); a[3]+=e[i]*bhi(po[i].y);
        a[4]+=e[i]*blo(po[i].z); a[5]+=e[i]*bhi(po[i].z); a[6]+=e[i]*blo(po[i].w); a[7]+=e[i]*bhi(po[i].w);
      }
      float c4[4], c2[2];
      #pragma unroll
      for(int k=0;k<4;k++) c4[k]=sw32add(a[k],a[k+4]);
      c2[0]=sw16add(c4[0],c4[2]); c2[1]=sw16add(c4[1],c4[3]);
      const bool b3=(lane>>3)&1;
      const float keep=b3?c2[1]:c2[0], send=b3?c2[0]:c2[1];
      const float r=keep+dpp<DROR8>(send);
      const int col=8*q+4*hh+2*((lane>>4)&1)+(b3?1:0);
      *(LAS u16*)(L+L_CL+b*320+(half*64+col)*2)=(u16)pk2(r,0.f);
    }
    BAR();
    STAMP(9);
    {
      v4u bc[4];
      #pragma unroll
      for(int k=0;k<4;k++) bc[k]=lrd128(L+L_CL+p4*320+(32*k+8*g)*2);
      SB();
      f32x4 acc={0,0,0,0};
      #pragma unroll
      for(int k=0;k<4;k++) acc=mfma16(auv[k],bc[k],acc);
      if((lane&15)<4) *(LAS v2u*)(L+L_CX+p4*320+(16*w+4*g)*2)=v2u{pk2(acc[0],acc[1]),pk2(acc[2],acc[3])};
    }
    BAR();
    {
      v4u bc[4];
      #pragma unroll
      for(int ks=0;ks<4;ks++) bc[ks]=lrd128(L+L_CX+p4*320+(32*ks+8*g)*2);
      SB();
      #pragma unroll
      for(int ks=0;ks<4;ks++)
        #pragma unroll
        for(int t=0;t<4;t++) oacc[t]=mfma16(awo[ks][t],bc[ks],oacc[t]);
      f32x4 v;
      #pragma unroll
      for(int r=0;r<4;r++){ const float lo=(q4&1)?oacc[1][r]:oacc[0][r], hi=(q4&1)?oacc[3][r]:oacc[2][r]; v[r]=(q4&2)?hi:lo; }
      st128(rws,WS_OUT+(16*w+4*q4+g)*8192+fi*64+p4*16,v4u{pk2(v[0],v[1]),tag,pk2(v[2],v[3]),tag});
    }
    STAMP(10);
    { const u64 t1=rt(); wait_t(t1,D_E4); }
    v4u pv;
    _Pragma("unroll 1") for(int sp=0;sp<SPIN_CAP;++sp){ FENCE();
      pv=ld128<AUX_SC1>(rws,WS_OUT+fi*8192+tid*16);
      if(__all((pv.y==tag)&(pv.w==tag))) break;
      __builtin_amdgcn_s_sleep(SLP_S);
    }
    STAMP(11);
    const float u0=sw32add(blo(pv.x),blo(pv.z)), u1=sw32add(bhi(pv.x),bhi(pv.z));
    float v=sw16add(u0,u1);
    v+=dpp<DROR4>(v); v+=dpp<DROR8>(v);
    if((lane&12)==0) *(LAS float*)(L+L_RED+w*64+(g*4+p4)*4)=v;
    BAR();
    if(tid<16){
      float a=0.f;
      #pragma unroll
      for(int ww=0;ww<8;ww++) a+=*(LAS float*)(L+L_RED+ww*64+tid*4);
      out[(tid&3)*DM+4*fi+(tid>>2)]=(u16)pk2(xb_res+a,0.f);
    }
    if(wg==0 && tid==0) __hip_atomic_store((gu64*)(ws+WS_EPOCH),want,RLX_AGENT);
  } else {
    const int ab=fi>>5, as=fi&31;
    const int fl=fsw(l31);
    const int blk=(lane>>4)&1, sgm=w>>1;
    const u16* gb=ckv+((size_t)ab*SK+as*128+32*(w&3))*DC;
#define DMA_CKV(r) dma16(gb+(r)*DC,(u32)((lane*16)^(16*fsw(r))),Lb+L_CKV+(32*w+(r))*1024)
    #pragma unroll
    for(int i=0;i<4;i++) DMA_T(wq,L_AWQ,i);
    if(w<4){
      v2u e2;
      asm volatile("global_load_dwordx2 %0, %1, %2" : "=v"(e2) : "v"(0), "s"(ws) : "memory");
      wait_t(t_start,T_CKV0);
      #pragma unroll
      for(int r=0;r<CKV_INFL;r++) DMA_CKV(r);
      asm volatile("s_waitcnt vmcnt(%1)" : "+v"(e2) : "i"(CKV_INFL) : "memory");
      if(w==0 && lane==0) *(LAS u64*)(L+L_AEP)=(((u64)e2.y<<32)|e2.x)+1;
    } else {
      v4u xr;
      asm volatile("global_load_dwordx4 %0, %1, %2\n\ts_waitcnt vmcnt(0)" : "=v"(xr) : "v"((w&3)*1024+16*lane), "s"(x) : "memory");
      ln_row(xr,lane,tq,L+L_AXN+(w-4)*320);
    }
    BAR();
    ESTAMP(2);
    u32 tag; u64 want;
    {
      v4u aq[4], bq[4];
      #pragma unroll
      for(int ks=0;ks<4;ks++){ lds8* a=L+L_AWQ+ks*8192+troff; aq[ks]=trd2(a,a+1024); bq[ks]=lrd128(L+L_AXN+p4*320+(32*ks+8*g)*2); }
      const v2u e=*(LAS v2u*)(L+L_AEP);
      SB();
      f32x4 acc={0,0,0,0};
      #pragma unroll
      for(int ks=0;ks<4;ks++) acc=mfma16(aq[ks],bq[ks],acc);
      tag=__builtin_amdgcn_readfirstlane(e.x);
      if((lane&15)<4) st128(rws,WS_Q1+fi*2048+w*256+g*64+p4*16,v4u{pk2(acc[0],acc[1]),tag,pk2(acc[2],acc[3]),tag});
      want=((u64)__builtin_amdgcn_readfirstlane(e.y)<<32)|tag;
    }
    ESTAMP(3);
    BAR();
    v4u vf[8][2];
#define LOAD_VF() do{ _Pragma("unroll") for(int ks=0;ks<8;ks++) _Pragma("unroll") for(int t=0;t<2;t++){ \
        const int ch=8*(w&1)+4*t+2*blk+(p4>>1); \
        lds8* a0=L+L_CKV+(16*ks+8*hh+q4)*1024+256*sgm+16*(ch^((q4<<2)|(2*hh)))+8*(p4&1); \
        lds8* a1=L+L_CKV+(16*ks+8*hh+4+q4)*1024+256*sgm+16*(ch^((q4<<2)|(2*hh+1)))+8*(p4&1); \
        vf[ks][t]=trd2(a0,a1); } SB(); }while(0)
    float mx, lse;
    if(w<4){
      #pragma unroll
      for(int r=CKV_INFL;r<32;r++){
        VMW(CKV_INFL-1);
        DMA_CKV(r);
      }
      VMW(0);
      ESTAMP(0);
      v4u af[32];
      #pragma unroll
      for(int k=0;k<32;k++) af[k]=lrd128(L+L_CKV+(32*w+l31)*1024+256*(k>>3)+16*((2*(k&7)+hh)^fl));
      BAR();
      ESTAMP(1);
      f32x16 sacc;
      #pragma unroll
      for(int i=0;i<16;i++) sacc[i]=0.f;
      v4u bf[12];
      #pragma unroll
      for(int k=0;k<12;k++) bf[k]=lrd128(L+L_QAS+l31*1024+256*(k>>3)+16*((2*(k&7)+hh)^fl));
      SB();
      #pragma unroll
      for(int k=0;k<32;k++){
        sacc=mfma32(af[k],bf[k%12],sacc);
        if(k+12<32) bf[k%12]=lrd128(L+L_QAS+l31*1024+256*((k+12)>>3)+16*((2*((k+12)&7)+hh)^fl));
        SB();
      }
      float mloc=vmax3(vmax3(vmax3(sacc[0],sacc[1],sacc[2]),vmax3(sacc[3],sacc[4],sacc[5]),vmax3(sacc[6],sacc[7],sacc[8])),
                       vmax3(vmax3(sacc[9],sacc[10],sacc[11]),vmax3(sacc[12],sacc[13],sacc[14]),sacc[15]),sacc[15]);
      mloc=sw32max(mloc);
      BAR();
      STAMP(4);
      if(lane<32) *(LAS float*)(L+L_MX+w*128+lane*4)=mloc;
      BAR();
      mx=vmax(vmax3(*(LAS float*)(L+L_MX+l31*4),*(LAS float*)(L+L_MX+128+l31*4),*(LAS float*)(L+L_MX+256+l31*4)),*(LAS float*)(L+L_MX+384+l31*4));
      float ls=0.f;
      #pragma unroll
      for(int a=0;a<4;a++){
        float p0=__builtin_amdgcn_exp2f(sacc[4*a]-mx), p1=__builtin_amdgcn_exp2f(sacc[4*a+1]-mx), p2=__builtin_amdgcn_exp2f(sacc[4*a+2]-mx), p3=__builtin_amdgcn_exp2f(sacc[4*a+3]-mx);
        const u32 u0=pk2(p0,p1), u1=pk2(p2,p3);
        ls+=(p0+p1)+(p2+p3);
        *(LAS v2u*)(L+L_P+l31*256+16*((4*w+a)^fl)+8*hh)=v2u{u0,u1};
      }
      ls=sw32add(ls,ls);
      if(lane<32) *(LAS float*)(L+L_LS+w*128+lane*4)=ls;
      BAR();
      STAMP(5);
      LOAD_VF();
    } else {
      const int wv=w-4;
      wait_t(t_start,T_FLAG1);
      _Pragma("unroll 1") for(int sp=0;sp<SPIN_CAP;++sp){ FENCE();
        if(__all(flag_ok(ld128<AUX_SC1>(rws,WS_FLAG1+xc*1024+wv*256+(lane&15)*16),want))) break;
        __builtin_amdgcn_s_sleep(SLP);
      }
      ESTAMP(0);
      wait_t(t_start,T_GATHER);
      v4u qa[8];
      #pragma unroll
      for(int i=0;i<8;i++) qa[i]=ld128<AUX_SC1>(rws,WS_QA+QA_COPY*131072+ab*32768+(8*wv+i)*1024+16*lane);
      _Pragma("unroll 1") for(int sp=0;sp<SPIN_CAP;++sp){
        u32 bad=0;
        #pragma unroll
        for(int i=0;i<8;i++) bad|=(__all(ok4(qa[i]))?0u:1u)<<i;
        if(!bad) break;
        __builtin_amdgcn_s_sleep(SLP_S); FENCE();
        #pragma unroll
        for(int i=0;i<8;i++) if((bad>>i)&1) qa[i]=ld128<AUX_SC1>(rws,WS_QA+QA_COPY*131072+ab*32768+(8*wv+i)*1024+16*lane);
      }
      ESTAMP(1);
      #pragma unroll
      for(int i=0;i<8;i++){ const int head=8*wv+i; *(LAS v4u*)(L+L_QAS+head*1024+((lane*16)^(16*fsw(head&15))))=qa[i]; }
      BAR();
      LOAD_VF();
      BAR();
      BAR();
      mx=0.f;
      BAR();
    }
    {
      v4u pb[8];
      #pragma unroll
      for(int ks=0;ks<8;ks++) pb[ks]=lrd128(L+L_P+l31*256+16*((2*ks+hh)^fl));
      const float lsum=(*(LAS float*)(L+L_LS+l31*4)+*(LAS float*)(L+L_LS+128+l31*4))+(*(LAS float*)(L+L_LS+256+l31*4)+*(LAS float*)(L+L_LS+384+l31*4));
      SB();
      f32x16 o[2];
      #pragma unroll
      for(int i=0;i<16;i++){ o[0][i]=0.f; o[1][i]=0.f; }
      #pragma unroll
      for(int t=0;t<2;t++)
        #pragma unroll
        for(int ks=0;ks<8;ks++) o[t]=mfma32(vf[ks][t],pb[ks],o[t]);
      const float inv=__builtin_amdgcn_rcpf(lsum);
      lse=mx+__builtin_amdgcn_logf(lsum);
      STAMP(6);
      #pragma unroll
      for(int t=0;t<2;t++)
        #pragma unroll
        for(int a=0;a<4;a++)
          *(LAS v2u*)(L+L_CKV+l31*1024+256*sgm+16*((8*(w&1)+4*t+a)^fl)+8*hh)=v2u{pk2(o[t][4*a]*inv,o[t][4*a+1]*inv),pk2(o[t][4*a+2]*inv,o[t][4*a+3]*inv)};
      v4u ov[4];
      #pragma unroll
      for(int i=0;i<4;i++){ const int head=8*i+(lane>>3); ov[i]=lrd128(L+L_CKV+head*1024+256*sgm+16*((8*(w&1)+(lane&7))^fsw(head))); }
      SB();
      #pragma unroll
      for(int i=0;i<4;i++){ const int head=8*i+(lane>>3); st128(rws,WS_PO+((head*4+sgm)*128+ab*32+as)*256+(w&1)*128+(lane&7)*16,ov[i]); }
    }
    STAMP(7);
    VMW(0);
    BAR();
    if(w==0 && lane<32) __builtin_amdgcn_raw_buffer_store_b64(v2u{__float_as_uint(lse),tag},rws,WS_LSE+(lane>>2)*4096+fi*32+(lane&3)*8,0,AUX_SC1);
    STAMP(11);
  }
  STAMP(15);
}

extern "C" void kernel_launch(void* const* d_in,const int* in_sizes,int n_in,void* d_out,int out_size,void* d_ws,size_t ws_size,hipStream_t stream){
  (void)hipFuncSetAttribute((const void*)mla_mlp_kernel,hipFuncAttributeMaxDynamicSharedMemorySize,LDS_BYTES);
  mla_mlp_kernel<<<256,512,LDS_BYTES,stream>>>((const u16*)d_in[0],(const u16*)d_in[1],(const u16*)d_in[2],(const u16*)d_in[3],(const u16*)d_in[4],(const u16*)d_in[5],
     (const u16*)d_in[7],(const u16*)d_in[8],(const u16*)d_in[9],(const u16*)d_in[10],(u16*)d_out,(unsigned char*)d_ws);
}
```

```cpp
#include <hip/hip_runtime.h>
#include <cstring>
#define CODEPF 21504
typedef unsigned short u16; typedef unsigned u32; typedef unsigned long long u64;
#define GAS __attribute__((address_space(1)))
#define LAS __attribute__((address_space(3)))
typedef u32 v4u __attribute__((ext_vector_type(4)));
typedef u32 v2u __attribute__((ext_vector_type(2)));
typedef float f32x2 __attribute__((ext_vector_type(2)));
typedef float f32x4 __attribute__((ext_vector_type(4)));
typedef float f32x16 __attribute__((ext_vector_type(16)));
typedef __bf16 bf16x8 __attribute__((ext_vector_type(8)));
typedef __bf16 bf16x2 __attribute__((ext_vector_type(2)));
typedef short s16x4 __attribute__((ext_vector_type(4)));
typedef GAS u64 gu64;
typedef LAS unsigned char lds8;
#define RLX_AGENT __ATOMIC_RELAXED, __HIP_MEMORY_SCOPE_AGENT
#define DI __device__ __forceinline__

constexpr int NB=4, NH=32, SK=4096, HD=128, DM=512, DC=512, FF=1792;
constexpr int WS_EPOCH=0, WS_FLAG1=8192, WS_FLAG2=16384, WS_Q1=32768, WS_CX=WS_Q1+262144, WS_HM=WS_CX+262144, WS_QA=WS_HM+262144, WS_LSE=WS_QA+262144, WS_PO=WS_LSE+32768,
              WS_OUT=WS_PO+(4<<20), WS_END=WS_OUT+(2<<20), WS_STAMP=WS_END;
constexpr int L_CKV=0, L_QAS=131072, L_P=L_QAS, L_MX=L_QAS+8192, L_LS=L_QAS+8704, L_AWQ=L_QAS, L_AXN=126*1024, L_AEP=L_AXN+1536, L_AWO=0, L_ABC=32768;
constexpr int L_WUK=0, L_WO=0, L_W2=81920, L_WUV=98304, L_W1=131072, L_XN=147456, L_QS=148736, L_CL=149760, L_H2=151040, L_RED=151552, L_EP=152064, L_QL=152576, L_WG=156672, L_CX=157184, L_ZERO=158464, L_STF=158720;
constexpr int LDS_BYTES=163840;
constexpr u32 SENT=0xFFFFFFFFu;
constexpr int SPIN_CAP=1<<20;
constexpr int AUX_NT=2, AUX_SC1=16;
#define KNOB(n,v) constexpr int n##_=v
#define CKV_INFL 12
#define NCLR 4
#define T_TAIL 460
#define T_CKV0 120
#define SLP_G 1
#define QA_COPY (xc&1)
#define WO_1 4
#define T_WO 0
#define RW_W 6
#define T_WUK 0
#define INFL0 12
#define T_E1 150
#define T_FLAG1 240
#define T_GATHER 400
#define T_CLR 420
#define T_GUN 620
#define T_CX 780
#define SLP 4
#define SLP_S 1
#define D_E4 50
#define D_MG 60
static_assert(CKV_INFL<=30,"ring");
#define STAMP(k) do{}while(0)
#define ESTAMP(i) do{}while(0)

DI u64 rt(){ return __builtin_amdgcn_s_memrealtime(); }
DI u64 ct(){ u64 t; asm volatile("s_memtime %0\n\ts_waitcnt lgkmcnt(0)" : "=s"(t) :: "memory"); return t; }
DI __amdgpu_buffer_rsrc_t mkrs(const void* p, int bytes){ return __builtin_amdgcn_make_buffer_rsrc((void*)p,0,bytes,0x00020000); }
template<int AUX> DI v4u ld128(__amdgpu_buffer_rsrc_t rs,int off){ return __builtin_amdgcn_raw_buffer_load_b128(rs,off,0,AUX); }
template<int AUX> DI u32 ld32(__amdgpu_buffer_rsrc_t rs,int off){ return __builtin_amdgcn_raw_buffer_load_b32(rs,off,0,AUX); }
DI void st128(__amdgpu_buffer_rsrc_t rs,int off,v4u v){ __builtin_amdgcn_raw_buffer_store_b128(v,rs,off,0,AUX_SC1); }
DI void st32(__amdgpu_buffer_rsrc_t rs,int off,u32 v){ __builtin_amdgcn_raw_buffer_store_b32(v,rs,off,0,AUX_SC1); }
DI f32x4 mfma16(v4u a,v4u b,f32x4 c){ return __builtin_amdgcn_mfma_f32_16x16x32_bf16(__builtin_bit_cast(bf16x8,a),__builtin_bit_cast(bf16x8,b),c,0,0,0); }
DI f32x16 mfma32(v4u a,v4u b,f32x16 c){ return __builtin_amdgcn_mfma_f32_32x32x16_bf16(__builtin_bit_cast(bf16x8,a),__builtin_bit_cast(bf16x8,b),c,0,0,0); }
DI v2u trd(lds8* p){ return __builtin_bit_cast(v2u,__builtin_amdgcn_ds_read_tr16_b64_v4i16((LAS s16x4*)p)); }
DI v4u trd2(lds8* p0,lds8* p1){ v2u a=trd(p0), b=trd(p1); return v4u{a.x,a.y,b.x,b.y}; }
DI v4u lrd128(lds8* p){ return *(LAS v4u*)p; }
DI u32 pk2(float lo,float hi){ f32x2 f={lo,hi}; return __builtin_bit_cast(u32,__builtin_convertvector(f,bf16x2)); }
DI float rbf(float x){ return __uint_as_float(pk2(x,0.f)<<16); }
DI float vmax(float a,float b){ return fmaxf(a,b); }
DI float vmax3(float a,float b,float c){ return fmaxf(fmaxf(a,b),c); }
DI float blo(u32 w){ return __uint_as_float(w<<16); }
DI float bhi(u32 w){ return __uint_as_float(w&0xFFFF0000u); }
DI u32 nos(u32 w){ return w<0xFFFFFFFEu? w:0xFFFFFFFEu; }
DI v4u nos4(v4u v){ return v4u{nos(v.x),nos(v.y),nos(v.z),nos(v.w)}; }
DI bool ok4(v4u v){ return (v.x!=SENT)&(v.y!=SENT)&(v.z!=SENT)&(v.w!=SENT); }
#define BAR() asm volatile("s_waitcnt lgkmcnt(0)\n\ts_barrier" ::: "memory")
#define VMW(n) asm volatile("s_waitcnt vmcnt(%0)" :: "i"(n) : "memory")
#define FENCE() asm volatile("" ::: "memory")
#define SB() __builtin_amdgcn_sched_barrier(0)
template<int CTRL> DI float dpp(float x){ return __builtin_bit_cast(float,__builtin_amdgcn_mov_dpp(__builtin_bit_cast(int,x),CTRL,0xf,0xf,true)); }
constexpr int DXOR1=0xB1, DXOR2=0x4E, DHMIR=0x141, DROR8=0x128, DROR4=0x124;
DI float sw32add(float a,float b){ auto s=__builtin_amdgcn_permlane32_swap(__float_as_uint(a),__float_as_uint(b),false,false); return __uint_as_float(s[0])+__uint_as_float(s[1]); }
DI float sw16add(float a,float b){ auto s=__builtin_amdgcn_permlane16_swap(__float_as_uint(a),__float_as_uint(b),false,false); return __uint_as_float(s[0])+__uint_as_float(s[1]); }
DI float sw32max(float a){ auto s=__builtin_amdgcn_permlane32_swap(__float_as_uint(a),__float_as_uint(a),false,false); return vmax(__uint_as_float(s[0]),__uint_as_float(s[1])); }
DI float sw16max(float a){ auto s=__builtin_amdgcn_permlane16_swap(__float_as_uint(a),__float_as_uint(a),false,false); return vmax(__uint_as_float(s[0]),__uint_as_float(s[1])); }
DI float wave_sum(float v){ v+=dpp<DXOR1>(v); v+=dpp<DXOR2>(v); v+=dpp<DHMIR>(v); v+=dpp<DROR8>(v); v=sw16add(v,v); v=sw32add(v,v); return v; }
DI void dma16(const void* gbase,u32 voff,u32 ldsb){
  u32 keep;
  asm volatile("s_mov_b32 %0, m0\n\ts_mov_b32 m0, %3\n\ts_nop 0\n\tglobal_load_lds_dwordx4 %1, %2 nt\n\ts_mov_b32 m0, %0" : "=&s"(keep) : "v"(voff),"s"(gbase),"s"(ldsb) : "memory");
}
DI constexpr int fsw(int row){ return ((row&3)<<2)|((row>>2)&3); }
DI bool flag_ok(v4u f,u64 want){ return (f.x==(u32)want)&(f.y==(u32)(want>>32))&(f.z==(u32)want)&(f.w==(u32)(want>>32)); }
DI void wait_t(u64 t0,int ticks){ while(rt()-t0<(u64)ticks) __builtin_amdgcn_s_sleep(1); }
DI void ln_row(v4u xr,int lane,int tq,lds8* dst){
  float v[8]={blo(xr.x),bhi(xr.x),blo(xr.y),bhi(xr.y),blo(xr.z),bhi(xr.z),blo(xr.w),bhi(xr.w)};
  const float mean=wave_sum(((v[0]+v[1])+(v[2]+v[3]))+((v[4]+v[5])+(v[6]+v[7])))*(1.f/DM);
  float qq=0.f;
  #pragma unroll
  for(int i=0;i<8;i++){ const float d=v[i]-mean; qq+=d*d; }
  const float var=wave_sum(qq)*(1.f/DM);
  const float mb=rbf(mean), isd=__builtin_amdgcn_rcpf(rbf(__builtin_amdgcn_sqrtf(rbf(rbf(var)+1e-5f))));
  float y[8];
  #pragma unroll
  for(int i=0;i<8;i++) y[i]=rbf(v[i]-mb)*isd;
  if((lane>>4)==tq) *(LAS v4u*)(dst+16*(lane&15))=v4u{pk2(y[0],y[1]),pk2(y[2],y[3]),pk2(y[4],y[5]),pk2(y[6],y[7])};
}

__global__ void __launch_bounds__(512,2) mla_mlp_kernel(const u16* __restrict__ x,const u16* __restrict__ ckv,const u16* __restrict__ wq,const u16* __restrict__ wuk,
    const u16* __restrict__ wuv,const u16* __restrict__ wo,const u16* __restrict__ w1,const u16* __restrict__ b1,const u16* __restrict__ w2,const u16* __restrict__ b2,
    u16* __restrict__ out,unsigned char* __restrict__ ws){
  extern __shared__ __attribute__((aligned(16))) unsigned char smem[];
  lds8* L=(lds8*)smem;
  const u64 pc0=__builtin_amdgcn_s_getpc()&~(u64)255;
  const u32 Lb=(u32)(size_t)L;
  const int tid=threadIdx.x, lane=tid&63, w=__builtin_amdgcn_readfirstlane(tid>>6);
  const int wg=blockIdx.x, xc=wg&7, j=wg>>3, jj=j&15;
  const bool isF=j<16;
  const int fi=xc*16+jj, th=fi>>2, tq=fi&3;
  const int g=lane>>4, q4=(lane&15)>>2, p4=lane&3, hh=lane>>5, l31=lane&31;
  const __amdgpu_buffer_rsrc_t rws=mkrs(ws,WS_END);
  const u64 t_start=rt();
  asm volatile("" :: "s"(x),"s"(ckv),"s"(wq),"s"(wuk),"s"(ws));
  const u32 rowoff=(u32)((128*tq+16*w+g)*8192+th*256);
#define DMA_T(p,dst,i) dma16(p,rowoff+(u32)(4*(i)*8192)+16*((lane&15)^(2*(g|(((i)>>1)<<2)))),Lb+(dst)+(16*w+4*(i))*256)
  const int troff=(8*g+q4)*256+(w^(q4|((g&1)<<2)))*32+p4*8;
  const int r0=4*g+q4;
  if(isF){
    const bool mlp = fi<FF/16;
    if(w==7){
      v2u e2;
      st128(rws,WS_QA+g*32768+th*1024+tq*256+(lane&15)*16,v4u{SENT,SENT,SENT,SENT});
      st128(rws,WS_QA+131072+g*32768+th*1024+tq*256+(lane&15)*16,v4u{SENT,SENT,SENT,SENT});
      asm volatile("global_load_dwordx2 %0, %1, %2" : "=v"(e2) : "v"(0), "s"(ws) : "memory");
      wait_t(t_start,T_WUK);
      #pragma unroll
      for(int i=0;i<4;i++) dma16(wuk,rowoff+(u32)(4*i*8192)+16*((lane&15)^(4*i+g)),Lb+L_WUK+(16*w+4*i)*256);
      asm volatile("s_waitcnt vmcnt(4)" : "+v"(e2) :: "memory");
      const u64 wn=(((u64)e2.y<<32)|e2.x)+1; if(lane<8) __hip_atomic_store((gu64*)(ws+WS_FLAG1)+lane*128+fi,wn,RLX_AGENT);
      if(lane==0) *(LAS u64*)(L+L_EP)=wn;
      VMW(1);
    } else {
      wait_t(t_start,T_WUK);
      #pragma unroll
      for(int i=0;i<4;i++) dma16(wuk,rowoff+(u32)(4*i*8192)+16*((lane&15)^(4*i+g)),Lb+L_WUK+(16*w+4*i)*256);
      if(w>=5 && (j+16*(6-w))*1024<CODEPF){ v4u t_; asm volatile("global_load_dwordx4 %0, %1, %2\n\ts_waitcnt vmcnt(0)" : "=v"(t_) : "v"((j+16*(6-w))*1024+16*lane), "s"(pc0) : "memory"); }
      VMW(0);
    }
    v4u auk[4];
    #pragma unroll
    for(int k=0;k<4;k++){
      lds8* a=L+L_WUK+(16*w+(lane&15))*256+8*(g&1);
      const v2u lo=*(LAS v2u*)(a+16*((4*k+(g>>1))^(lane&15))), hi=*(LAS v2u*)(a+16*((4*k+2+(g>>1))^(lane&15)));
      auk[k]=v4u{lo.x,lo.y,hi.x,hi.y};
    }
    BAR();
    STAMP(1);
    const v2u ep=*(LAS v2u*)(L+L_EP);
    const u32 tag=__builtin_amdgcn_readfirstlane(ep.x);
    const u64 want=((u64)__builtin_amdgcn_readfirstlane(ep.y)<<32)|tag;
    wait_t(t_start,T_E1);
    {
      v4u qf;
      _Pragma("unroll 1") for(int sp=0;sp<SPIN_CAP;++sp){ FENCE();
        qf=ld128<AUX_SC1>(rws,WS_Q1+th*8192+w*1024+lane*16);
        if(__all((qf.y==tag)&(qf.w==tag))) break;
        __builtin_amdgcn_s_sleep(SLP_S);
      }
      const int t=4*(w&1)+g;
      *(LAS v2u*)(L+L_QL+(w>>1)*1024+(t>>1)*256+(lane&15)*16+(t&1)*8)=v2u{qf.x,qf.z};
    }
    BAR();
    STAMP(2);
    {
      v4u qb[4];
      #pragma unroll
      for(int k=0;k<4;k++) qb[k]=lrd128(L+L_QL+q4*1024+k*256+g*64+p4*16);
      SB();
      f32x4 acc={0,0,0,0};
      #pragma unroll
      for(int k=0;k<4;k++) acc=mfma16(auk[k],qb[k],acc);
      #pragma unroll
      for(int r=0;r<4;r++){ acc[r]+=dpp<DROR4>(acc[r]); acc[r]+=dpp<DROR8>(acc[r]); }
      const float SC=0.08838834764831845f*1.4426950408889634f;
      if((lane&15)<4) *(LAS v2u*)(L+L_QS+(lane&15)*256+(16*w+4*g)*2)=v2u{nos(pk2(acc[0]*SC,acc[1]*SC)),nos(pk2(acc[2]*SC,acc[3]*SC))};
    }
    BAR();
    if(w<2){ v4u v=lrd128(L+L_QS+lane*16); st128(rws,WS_QA+w*131072+g*32768+th*1024+tq*256+(lane&15)*16,v); }
    STAMP(3);
    u32 b1raw[4];
    #pragma unroll
    for(int r=0;r<4;r++) b1raw[r]=__builtin_amdgcn_raw_buffer_load_b16(mkrs(b1,FF*2),(16*fi+4*g+r)*2,0,0);
    u32 xraw=__builtin_amdgcn_raw_buffer_load_b16(mkrs(x,NB*DM*2),(p4*DM+4*fi+q4)*2,0,0);
    u32 b2raw=__builtin_amdgcn_raw_buffer_load_b16(mkrs(b2,DM*2),(4*fi+q4)*2,0,0);
    SB();
#define DMA_WO(i) dma16(wo,(u32)((th*128+8*(i)+w)*1024+16*(lane^(2*((w&3)|(((i)&1)<<2))))),Lb+L_WO+(8*(i)+w)*1024)
    wait_t(t_start,T_WO);
    #pragma unroll
    for(int i=0;i<10;i++){ if(i>=WO_1) VMW(WO_1-1); DMA_WO(i); }
    wait_t(t_start,T_TAIL);
    v4u xr;
    asm volatile("global_load_dwordx4 %0, %1, %2" : "=v"(xr) : "v"((w&3)*1024+16*lane), "s"(x) : "memory");
    const int thm=mlp?th:0, fim=mlp?fi:0;
    #pragma unroll
    for(int i=0;i<2;i++){ const int r=8*i+(lane>>3); dma16(w1,(u32)((128*tq+16*w+r)*(FF*2)+thm*128+16*((lane&7)^(2*(((r>>1)&1)|(i<<1))))),Lb+L_W1+(16*w+8*i)*128); }
    #pragma unroll
    for(int i=0;i<4;i++) DMA_T(wuv,L_WUV,i);
    #pragma unroll
    for(int i=0;i<2;i++){ const int r=8*i+w; dma16(w2,(u32)((16*fim+r)*1024+16*(lane^(2*(r&7)))),Lb+L_W2+r*1024); }
    v4u rwo[6];
    #pragma unroll
    for(int i=0;i<6;i++) asm volatile("global_load_dwordx4 %0, %1, %2 nt" : "=v"(rwo[i]) : "v"((th*128+80+8*i+w)*1024+16*lane), "s"(wo) : "memory");
    asm volatile("s_waitcnt vmcnt(12)" : "+v"(xr) :: "memory");
    STAMP(4);
    if(w<4) ln_row(xr,lane,tq,L+L_XN+w*320);
    BAR();
    if(mlp && w<4){
      v4u a1[4], bq[4];
      #pragma unroll
      for(int ks=0;ks<4;ks++){ lds8* a=L+L_W1+(32*ks+8*g+q4)*128+32*(w^((q4>>1)|((g&1)<<1)))+p4*8; a1[ks]=trd2(a,a+512); bq[ks]=lrd128(L+L_XN+p4*320+(32*ks+8*g)*2); }
      SB();
      f32x4 acc={0,0,0,0};
      #pragma unroll
      for(int ks=0;ks<4;ks++) acc=mfma16(a1[ks],bq[ks],acc);
      if((lane&15)<4){
        st128(rws,WS_HM+fi*2048+w*512+g*128+p4*16,v4u{__float_as_uint(acc[0]),tag,__float_as_uint(acc[1]),tag});
        st128(rws,WS_HM+fi*2048+w*512+g*128+64+p4*16,v4u{__float_as_uint(acc[2]),tag,__float_as_uint(acc[3]),tag}); }
    }
    VMW(RW_W);
    BAR();
    v4u auv[4]; v2u aw2[4];
    #pragma unroll
    for(int k=0;k<4;k++){ lds8* a=L+L_WUV+k*8192+troff; auv[k]=trd2(a,a+1024); }
    #pragma unroll
    for(int t=0;t<4;t++) aw2[t]=trd(L+L_W2+r0*1024+(w>>1)*256+32*((4*(w&1)+t)^(r0&7))+p4*8);
    BAR();
    if(mlp && w==4){
      v4u h0,h1;
      _Pragma("unroll 1") for(int sp=0;sp<SPIN_CAP;++sp){ FENCE();
        h0=ld128<AUX_SC1>(rws,WS_HM+(th*4+q4)*2048+tq*512+g*128+p4*16);
        h1=ld128<AUX_SC1>(rws,WS_HM+(th*4+q4)*2048+tq*512+g*128+64+p4*16);
        if(__all((h0.y==tag)&(h0.w==tag)&(h1.y==tag)&(h1.w==tag))) break;
        __builtin_amdgcn_s_sleep(SLP);
      }
      float hv[4]={__uint_as_float(h0.x),__uint_as_float(h0.z),__uint_as_float(h1.x),__uint_as_float(h1.z)};
      #pragma unroll
      for(int r=0;r<4;r++){ float sv=hv[r]; sv+=dpp<DROR4>(sv); sv+=dpp<DROR8>(sv); float h=rbf(rbf(sv)+blo(b1raw[r])); h=h>0.f?h:0.f; hv[r]=h*h; }
      *(LAS v2u*)(L+L_H2+lane*8)=v2u{pk2(hv[0],hv[1]),pk2(hv[2],hv[3])};
    }
    asm volatile("s_waitcnt vmcnt(0)" : "+v"(rwo[0]),"+v"(rwo[1]),"+v"(rwo[2]),"+v"(rwo[3]),"+v"(rwo[4]),"+v"(rwo[5]) :: "memory");
    #pragma unroll
    for(int i=0;i<6;i++) *(LAS v4u*)(L+L_WO+(80+8*i+w)*1024+16*(lane^(2*((w&3)|((i&1)<<2)))))=rwo[i];
    v4u awo[4][4];
#define LOAD_AWO(ks) _Pragma("unroll") for(int t=0;t<4;t++){ lds8* a=L+L_WO+(32*(ks)+8*g+q4)*1024+(w>>1)*256+p4*8+32*((4*(w&1)+t)^(q4|((g&1)<<2))); awo[ks][t]=trd2(a,a+4096); }
    LOAD_AWO(0); LOAD_AWO(1);
    SB();
    STAMP(5);
    BAR();
    asm volatile("" : "+v"(xraw), "+v"(b2raw));
    const float xb_res=blo(xraw)+blo(b2raw);
    LOAD_AWO(2); LOAD_AWO(3);
    f32x4 oacc[4];
    #pragma unroll
    for(int t=0;t<4;t++) oacc[t]=f32x4{0,0,0,0};
    if(mlp){
      const v2u h2=*(LAS v2u*)(L+L_H2+lane*8);
      #pragma unroll
      for(int t=0;t<4;t++) oacc[t]=mfma16(v4u{aw2[t].x,aw2[t].y,0,0},v4u{h2.x,h2.y,0,0},oacc[t]);
    }
    SB();
    STAMP(6);
    if(w==0){
      wait_t(t_start,T_GUN);
      v2u v0,v1;
      _Pragma("unroll 1") for(int sp=0;sp<SPIN_CAP;++sp){ FENCE();
        v0=__builtin_amdgcn_raw_buffer_load_b64(rws,WS_LSE+(th>>2)*4096+lane*32+(th&3)*8,0,AUX_SC1);
        v1=__builtin_amdgcn_raw_buffer_load_b64(rws,WS_LSE+(th>>2)*4096+2048+lane*32+(th&3)*8,0,AUX_SC1);
        if(__all((v0.y==tag)&(v1.y==tag))) break; __builtin_amdgcn_s_sleep(SLP_G);
      }
      STAMP(7);
      *(LAS u32*)(L+L_WG+hh*128+(l31&7)*16+(l31>>3)*4)=v0.x;
      *(LAS u32*)(L+L_WG+256+hh*128+(l31&7)*16+(l31>>3)*4)=v1.x;
    }
    BAR();
    {
      const int b=w>>1, half=w&1, slo=lane>>3, q=lane&7;
      const int po_off=WS_PO+(fi*128+b*32+slo)*256+half*128+q*16;
      v4u po[4];
      #pragma unroll
      for(int i=0;i<4;i++) po[i]=ld128<AUX_SC1>(rws,po_off+i*2048);
      SB();
      const f32x4 lf=*(LAS f32x4*)(L+L_WG+b*128+slo*16);
      float M=vmax(vmax3(lf[0],lf[1],lf[2]),lf[3]);
      M=vmax(M,dpp<DROR8>(M)); M=sw16max(M); M=sw32max(M);
      f32x4 e;
      #pragma unroll
      for(int i=0;i<4;i++) e[i]=__builtin_amdgcn_exp2f(lf[i]-M);
      float Ls=(e[0]+e[1])+(e[2]+e[3]);
      Ls+=dpp<DROR8>(Ls); Ls=sw16add(Ls,Ls); Ls=sw32add(Ls,Ls);
      e*=__builtin_amdgcn_rcpf(Ls);
      SB();
      STAMP(8);
      float a[8];
      #pragma unroll
      for(int k=0;k<8;k++) a[k]=0.f;
      #pragma unroll
      for(int i=0;i<4;i++){
        a[0]+=e[i]*blo(po[i].x); a[1]+=e[i]*bhi(po[i].x); a[2]+=e[i]*blo(po[i].y); a[3]+=e[i]*bhi(po[i].y);
        a[4]+=e[i]*blo(po[i].z); a[5]+=e[i]*bhi(po[i].z); a[6]+=e[i]*blo(po[i].w); a[7]+=e[i]*bhi(po[i].w);
      }
      float c4[4], c2[2];
      #pragma unroll
      for(int k=0;k<4;k++) c4[k]=sw32add(a[k],a[k+4]);
      c2[0]=sw16add(c4[0],c4[2]); c2[1]=sw16add(c4[1],c4[3]);
      const bool b3=(lane>>3)&1;
      const float keep=b3?c2[1]:c2[0], send=b3?c2[0]:c2[1];
      const float r=keep+dpp<DROR8>(send);
      const int col=8*q+4*hh+2*((lane>>4)&1)+(b3?1:0);
      *(LAS u16*)(L+L_CL+b*320+(half*64+col)*2)=(u16)pk2(r,0.f);
    }
    BAR();
    STAMP(9);
    {
      v4u bc[4];
      #pragma unroll
      for(int k=0;k<4;k++) bc[k]=lrd128(L+L_CL+p4*320+(32*k+8*g)*2);
      SB();
      f32x4 acc={0,0,0,0};
      #pragma unroll
      for(int k=0;k<4;k++) acc=mfma16(auv[k],bc[k],acc);
      if((lane&15)<4) *(LAS v2u*)(L+L_CX+p4*320+(16*w+4*g)*2)=v2u{pk2(acc[0],acc[1]),pk2(acc[2],acc[3])};
    }
    BAR();
    {
      v4u bc[4];
      #pragma unroll
      for(int ks=0;ks<4;ks++) bc[ks]=lrd128(L+L_CX+p4*320+(32*ks+8*g)*2);
      SB();
      #pragma unroll
      for(int ks=0;ks<4;ks++)
        #pragma unroll
        for(int t=0;t<4;t++) oacc[t]=mfma16(awo[ks][t],bc[ks],oacc[t]);
      f32x4 v;
      #pragma unroll
      for(int r=0;r<4;r++){ const float lo=(q4&1)?oacc[1][r]:oacc[0][r], hi=(q4&1)?oacc[3][r]:oacc[2][r]; v[r]=(q4&2)?hi:lo; }
      st128(rws,WS_OUT+(16*w+4*q4+g)*8192+fi*64+p4*16,v4u{pk2(v[0],v[1]),tag,pk2(v[2],v[3]),tag});
    }
    STAMP(10);
    { const u64 t1=rt(); wait_t(t1,D_E4); }
    v4u pv;
    _Pragma("unroll 1") for(int sp=0;sp<SPIN_CAP;++sp){ FENCE();
      pv=ld128<AUX_SC1>(rws,WS_OUT+fi*8192+tid*16);
      if(__all((pv.y==tag)&(pv.w==tag))) break;
      __builtin_amdgcn_s_sleep(SLP_S);
    }
    STAMP(11);
    const float u0=sw32add(blo(pv.x),blo(pv.z)), u1=sw32add(bhi(pv.x),bhi(pv.z));
    float v=sw16add(u0,u1);
    v+=dpp<DROR4>(v); v+=dpp<DROR8>(v);
    if((lane&12)==0) *(LAS float*)(L+L_RED+w*64+(g*4+p4)*4)=v;
    BAR();
    if(tid<16){
      float a=0.f;
      #pragma unroll
      for(int ww=0;ww<8;ww++) a+=*(LAS float*)(L+L_RED+ww*64+tid*4);
      out[(tid&3)*DM+4*fi+(tid>>2)]=(u16)pk2(xb_res+a,0.f);
    }
    if(wg==0 && tid==0) __hip_atomic_store((gu64*)(ws+WS_EPOCH),want,RLX_AGENT);
  } else {
    const int ab=fi>>5, as=fi&31;
    const int fl=fsw(l31);
    const int blk=(lane>>4)&1, sgm=w>>1;
    const u16* gb=ckv+((size_t)ab*SK+as*128+32*(w&3))*DC;
#define DMA_CKV(r) dma16(gb+(r)*DC,(u32)((lane*16)^(16*fsw(r))),Lb+L_CKV+(32*w+(r))*1024)
    #pragma unroll
    for(int i=0;i<4;i++) DMA_T(wq,L_AWQ,i);
    if(w<4){
      v2u e2;
      asm volatile("global_load_dwordx2 %0, %1, %2" : "=v"(e2) : "v"(0), "s"(ws) : "memory");
      wait_t(t_start,T_CKV0);
      #pragma unroll
      for(int r=0;r<INFL0;r++) DMA_CKV(r);
      asm volatile("s_waitcnt vmcnt(%1)" : "+v"(e2) : "i"(INFL0) : "memory");
      if(w==0 && lane==0) *(LAS u64*)(L+L_AEP)=(((u64)e2.y<<32)|e2.x)+1;
    } else {
      v4u xr;
      asm volatile("global_load_dwordx4 %0, %1, %2\n\ts_waitcnt vmcnt(0)" : "=v"(xr) : "v"((w&3)*1024+16*lane), "s"(x) : "memory");
      ln_row(xr,lane,tq,L+L_AXN+(w-4)*320);
    }
    BAR();
    ESTAMP(2);
    u32 tag; u64 want;
    {
      v4u aq[4], bq[4];
      #pragma unroll
      for(int ks=0;ks<4;ks++){ lds8* a=L+L_AWQ+ks*8192+troff; aq[ks]=trd2(a,a+1024); bq[ks]=lrd128(L+L_AXN+p4*320+(32*ks+8*g)*2); }
      const v2u e=*(LAS v2u*)(L+L_AEP);
      SB();
      f32x4 acc={0,0,0,0};
      #pragma unroll
      for(int ks=0;ks<4;ks++) acc=mfma16(aq[ks],bq[ks],acc);
      tag=__builtin_amdgcn_readfirstlane(e.x);
      if((lane&15)<4) st128(rws,WS_Q1+fi*2048+w*256+g*64+p4*16,v4u{pk2(acc[0],acc[1]),tag,pk2(acc[2],acc[3]),tag});
      want=((u64)__builtin_amdgcn_readfirstlane(e.y)<<32)|tag;
    }
    ESTAMP(3);
    BAR();
    v4u vf[8][2];
#define LOAD_VF() do{ _Pragma("unroll") for(int ks=0;ks<8;ks++) _Pragma("unroll") for(int t=0;t<2;t++){ \
        const int ch=8*(w&1)+4*t+2*blk+(p4>>1); \
        lds8* a0=L+L_CKV+(16*ks+8*hh+q4)*1024+256*sgm+16*(ch^((q4<<2)|(2*hh)))+8*(p4&1); \
        lds8* a1=L+L_CKV+(16*ks+8*hh+4+q4)*1024+256*sgm+16*(ch^((q4<<2)|(2*hh+1)))+8*(p4&1); \
        vf[ks][t]=trd2(a0,a1); } SB(); }while(0)
#define GATHER_QA() do{ v4u qa[4]; const int qo=WS_QA+QA_COPY*131072+ab*32768+4*w*1024+16*lane; \
      _Pragma("unroll") for(int i=0;i<4;i++) qa[i]=ld128<AUX_SC1>(rws,qo+i*1024); \
      _Pragma("unroll 1") for(int sp=0;sp<SPIN_CAP;++sp){ u32 bad=0; \
        _Pragma("unroll") for(int i=0;i<4;i++) bad|=(__all(ok4(qa[i]))?0u:1u)<<i; \
        if(!bad) break; \
        __builtin_amdgcn_s_sleep(SLP_S); FENCE(); \
        _Pragma("unroll") for(int i=0;i<4;i++) if((bad>>i)&1) qa[i]=ld128<AUX_SC1>(rws,qo+i*1024); } \
      _Pragma("unroll") for(int i=0;i<4;i++) *(LAS v4u*)(L+L_QAS+(4*w+i)*1024+((lane*16)^(16*((i<<2)|(w&3)))))=qa[i]; }while(0)
    float mx, lse;
    if(w<4){
      #pragma unroll
      for(int r=INFL0;r<32;r++){
        if(r>=CKV_INFL) VMW(CKV_INFL-1);
        DMA_CKV(r);
      }
      BAR();
      GATHER_QA();
      VMW(0);
      ESTAMP(0);
      v4u af[32];
      #pragma unroll
      for(int k=0;k<32;k++) af[k]=lrd128(L+L_CKV+(32*w+l31)*1024+256*(k>>3)+16*((2*(k&7)+hh)^fl));
      BAR();
      ESTAMP(1);
      f32x16 sacc;
      #pragma unroll
      for(int i=0;i<16;i++) sacc[i]=0.f;
      v4u bf[12];
      #pragma unroll
      for(int k=0;k<12;k++) bf[k]=lrd128(L+L_QAS+l31*1024+256*(k>>3)+16*((2*(k&7)+hh)^fl));
      SB();
      #pragma unroll
      for(int k=0;k<32;k++){
        sacc=mfma32(af[k],bf[k%12],sacc);
        if(k+12<32) bf[k%12]=lrd128(L+L_QAS+l31*1024+256*((k+12)>>3)+16*((2*((k+12)&7)+hh)^fl));
        SB();
      }
      float mloc=vmax3(vmax3(vmax3(sacc[0],sacc[1],sacc[2]),vmax3(sacc[3],sacc[4],sacc[5]),vmax3(sacc[6],sacc[7],sacc[8])),
                       vmax3(vmax3(sacc[9],sacc[10],sacc[11]),vmax3(sacc[12],sacc[13],sacc[14]),sacc[15]),sacc[15]);
      mloc=sw32max(mloc);
      BAR();
      STAMP(4);
      if(lane<32) *(LAS float*)(L+L_MX+w*128+lane*4)=mloc;
      BAR();
      mx=vmax(vmax3(*(LAS float*)(L+L_MX+l31*4),*(LAS float*)(L+L_MX+128+l31*4),*(LAS float*)(L+L_MX+256+l31*4)),*(LAS float*)(L+L_MX+384+l31*4));
      float ls=0.f;
      #pragma unroll
      for(int a=0;a<4;a++){
        float p0=__builtin_amdgcn_exp2f(sacc[4*a]-mx), p1=__builtin_amdgcn_exp2f(sacc[4*a+1]-mx), p2=__builtin_amdgcn_exp2f(sacc[4*a+2]-mx), p3=__builtin_amdgcn_exp2f(sacc[4*a+3]-mx);
        const u32 u0=pk2(p0,p1), u1=pk2(p2,p3);
        ls+=(p0+p1)+(p2+p3);
        *(LAS v2u*)(L+L_P+l31*256+16*((4*w+a)^fl)+8*hh)=v2u{u0,u1};
      }
      ls=sw32add(ls,ls);
      if(lane<32) *(LAS float*)(L+L_LS+w*128+lane*4)=ls;
      BAR();
      STAMP(5);
      LOAD_VF();
    } else {
      const int wv=w-4;
      wait_t(t_start,T_FLAG1);
      _Pragma("unroll 1") for(int sp=0;sp<SPIN_CAP;++sp){ FENCE();
        if(__all(flag_ok(ld128<AUX_SC1>(rws,WS_FLAG1+xc*1024+wv*256+(lane&15)*16),want))) break;
        __builtin_amdgcn_s_sleep(SLP);
      }
      ESTAMP(0);
      wait_t(t_start,T_GATHER);
      BAR();
      GATHER_QA();
      ESTAMP(1);
      BAR();
      LOAD_VF();
      BAR();
      BAR();
      mx=0.f;
      BAR();
    }
    {
      v4u pb[8];
      #pragma unroll
      for(int ks=0;ks<8;ks++) pb[ks]=lrd128(L+L_P+l31*256+16*((2*ks+hh)^fl));
      const float lsum=(*(LAS float*)(L+L_LS+l31*4)+*(LAS float*)(L+L_LS+128+l31*4))+(*(LAS float*)(L+L_LS+256+l31*4)+*(LAS float*)(L+L_LS+384+l31*4));
      SB();
      f32x16 o[2];
      #pragma unroll
      for(int i=0;i<16;i++){ o[0][i]=0.f; o[1][i]=0.f; }
      #pragma unroll
      for(int t=0;t<2;t++)
        #pragma unroll
        for(int ks=0;ks<8;ks++) o[t]=mfma32(vf[ks][t],pb[ks],o[t]);
      const float inv=__builtin_amdgcn_rcpf(lsum);
      lse=mx+__builtin_amdgcn_logf(lsum);
      STAMP(6);
      #pragma unroll
      for(int t=0;t<2;t++)
        #pragma unroll
        for(int a=0;a<4;a++)
          *(LAS v2u*)(L+L_CKV+l31*1024+256*sgm+16*((8*(w&1)+4*t+a)^fl)+8*hh)=v2u{pk2(o[t][4*a]*inv,o[t][4*a+1]*inv),pk2(o[t][4*a+2]*inv,o[t][4*a+3]*inv)};
      v4u ov[4];
      #pragma unroll
      for(int i=0;i<4;i++){ const int head=8*i+(lane>>3); ov[i]=lrd128(L+L_CKV+head*1024+256*sgm+16*((8*(w&1)+(lane&7))^fsw(head))); }
      SB();
      #pragma unroll
      for(int i=0;i<4;i++){ const int head=8*i+(lane>>3); st128(rws,WS_PO+((head*4+sgm)*128+ab*32+as)*256+(w&1)*128+(lane&7)*16,ov[i]); }
    }
    STAMP(7);
    VMW(0);
    BAR();
    if(w==0 && lane<32) __builtin_amdgcn_raw_buffer_store_b64(v2u{__float_as_uint(lse),tag},rws,WS_LSE+(lane>>2)*4096+fi*32+(lane&3)*8,0,AUX_SC1);
    STAMP(11);
  }
  STAMP(15);
}

extern "C" void kernel_launch(void* const* d_in,const int* in_sizes,int n_in,void* d_out,int out_size,void* d_ws,size_t ws_size,hipStream_t stream){
  (void)hipFuncSetAttribute((const void*)mla_mlp_kernel,hipFuncAttributeMaxDynamicSharedMemorySize,LDS_BYTES);
  mla_mlp_kernel<<<256,512,LDS_BYTES,stream>>>((const u16*)d_in[0],(const u16*)d_in[1],(const u16*)d_in[2],(const u16*)d_in[3],(const u16*)d_in[4],(const u16*)d_in[5],
     (const u16*)d_in[7],(const u16*)d_in[8],(const u16*)d_in[9],(const u16*)d_in[10],(u16*)d_out,(unsigned char*)d_ws);
}
```

```cpp
#include <hip/hip_runtime.h>
#include <cstring>
typedef unsigned short u16; typedef unsigned u32; typedef unsigned long long u64;
#define GAS __attribute__((address_space(1)))
#define LAS __attribute__((address_space(3)))
typedef u32 v4u __attribute__((ext_vector_type(4)));
typedef u32 v2u __attribute__((ext_vector_type(2)));
typedef float f32x2 __attribute__((ext_vector_type(2)));
typedef float f32x4 __attribute__((ext_vector_type(4)));
typedef float f32x16 __attribute__((ext_vector_type(16)));
typedef __bf16 bf16x8 __attribute__((ext_vector_type(8)));
typedef __bf16 bf16x2 __attribute__((ext_vector_type(2)));
typedef short s16x4 __attribute__((ext_vector_type(4)));
typedef GAS u64 gu64;
typedef LAS unsigned char lds8;
#define RLX_AGENT __ATOMIC_RELAXED, __HIP_MEMORY_SCOPE_AGENT
#define DI __device__ __forceinline__

constexpr int NB=4, NH=32, SK=4096, HD=128, DM=512, DC=512, FF=1792;
constexpr int WS_EPOCH=0, WS_FLAG1=8192, WS_Q1=32768, WS_HM=WS_Q1+262144, WS_QA=WS_HM+262144, WS_LSE=WS_QA+262144, WS_OUT=WS_LSE+65536,
              WS_PO=WS_OUT+(1<<20), WS_END=WS_PO+(4<<20), WS_STAMP=WS_END;
constexpr int L_CKV=0, L_QAS=131072, L_P=L_QAS, L_MX=L_QAS+8192, L_LS=L_QAS+8704, L_AWQ=L_QAS, L_AXN=126*1024, L_AEP=L_AXN+1536;
constexpr int L_WUK=0, L_WO=0, L_W2=81920, L_WUV=98304, L_W1=131072, L_XN=147456, L_QS=148736, L_CL=149760, L_H2=151040, L_RED=151552, L_EP=152064, L_QL=152576, L_WG=156672, L_CX=157184, L_STF=158720;
constexpr int LDS_BYTES=163840;
constexpr u32 SENT=0xFFFFFFFFu;
constexpr int SPIN_CAP=1<<20;
constexpr int AUX_NT=2, AUX_SC1=16;
#define CKV_INFL 12
#define INFL0 12
#define T_CKV0 120
#define T_E1 150
#define T_FLAG1 240
#define T_GATHER 400
#define WO_1 4
#define T_WO 0
#define T_TAIL 460
#define T_GUN 620
#define D_E4 50
#define SLP 4
#define SLP_S 1
static_assert(CKV_INFL<=30,"ring");
#define STAMP(k) do{}while(0)
#define ESTAMP(i) do{}while(0)

DI u64 rt(){ return __builtin_amdgcn_s_memrealtime(); }
DI u64 ct(){ u64 t; asm volatile("s_memtime %0\n\ts_waitcnt lgkmcnt(0)" : "=s"(t) :: "memory"); return t; }
DI __amdgpu_buffer_rsrc_t mkrs(const void* p, int bytes){ return __builtin_amdgcn_make_buffer_rsrc((void*)p,0,bytes,0x00020000); }
template<int AUX> DI v4u ld128(__amdgpu_buffer_rsrc_t rs,int off){ return __builtin_amdgcn_raw_buffer_load_b128(rs,off,0,AUX); }
template<int AUX> DI u32 ld32(__amdgpu_buffer_rsrc_t rs,int off){ return __builtin_amdgcn_raw_buffer_load_b32(rs,off,0,AUX); }
DI void st128(__amdgpu_buffer_rsrc_t rs,int off,v4u v){ __builtin_amdgcn_raw_buffer_store_b128(v,rs,off,0,AUX_SC1); }
DI void st32(__amdgpu_buffer_rsrc_t rs,int off,u32 v){ __builtin_amdgcn_raw_buffer_store_b32(v,rs,off,0,AUX_SC1); }
DI f32x4 mfma16(v4u a,v4u b,f32x4 c){ return __builtin_amdgcn_mfma_f32_16x16x32_bf16(__builtin_bit_cast(bf16x8,a),__builtin_bit_cast(bf16x8,b),c,0,0,0); }
DI f32x16 mfma32(v4u a,v4u b,f32x16 c){ return __builtin_amdgcn_mfma_f32_32x32x16_bf16(__builtin_bit_cast(bf16x8,a),__builtin_bit_cast(bf16x8,b),c,0,0,0); }
DI v2u trd(lds8* p){ return __builtin_bit_cast(v2u,__builtin_amdgcn_ds_read_tr16_b64_v4i16((LAS s16x4*)p)); }
DI v4u trd2(lds8* p0,lds8* p1){ v2u a=trd(p0), b=trd(p1); return v4u{a.x,a.y,b.x,b.y}; }
DI v4u lrd128(lds8* p){ return *(LAS v4u*)p; }
DI u32 pk2(float lo,float hi){ f32x2 f={lo,hi}; return __builtin_bit_cast(u32,__builtin_convertvector(f,bf16x2)); }
DI float rbf(float x){ return __uint_as_float(pk2(x,0.f)<<16); }
DI float vmax(float a,float b){ return fmaxf(a,b); }
DI float vmax3(float a,float b,float c){ return fmaxf(fmaxf(a,b),c); }
DI float blo(u32 w){ return __uint_as_float(w<<16); }
DI float bhi(u32 w){ return __uint_as_float(w&0xFFFF0000u); }
DI u32 nos(u32 w){ return w<0xFFFFFFFEu? w:0xFFFFFFFEu; }
DI v4u nos4(v4u v){ return v4u{nos(v.x),nos(v.y),nos(v.z),nos(v.w)}; }
DI bool ok4(v4u v){ return (v.x!=SENT)&(v.y!=SENT)&(v.z!=SENT)&(v.w!=SENT); }
#define BAR() asm volatile("s_waitcnt lgkmcnt(0)\n\ts_barrier" ::: "memory")
#define VMW(n) asm volatile("s_waitcnt vmcnt(%0)" :: "i"(n) : "memory")
#define FENCE() asm volatile("" ::: "memory")
#define SB() __builtin_amdgcn_sched_barrier(0)
template<int CTRL> DI float dpp(float x){ return __builtin_bit_cast(float,__builtin_amdgcn_mov_dpp(__builtin_bit_cast(int,x),CTRL,0xf,0xf,true)); }
constexpr int DXOR1=0xB1, DXOR2=0x4E, DHMIR=0x141, DROR8=0x128, DROR4=0x124;
DI float sw32add(float a,float b){ auto s=__builtin_amdgcn_permlane32_swap(__float_as_uint(a),__float_as_uint(b),false,false); return __uint_as_float(s[0])+__uint_as_float(s[1]); }
DI float sw16add(float a,float b){ auto s=__builtin_amdgcn_permlane16_swap(__float_as_uint(a),__float_as_uint(b),false,false); return __uint_as_float(s[0])+__uint_as_float(s[1]); }
DI float sw32max(float a){ auto s=__builtin_amdgcn_permlane32_swap(__float_as_uint(a),__float_as_uint(a),false,false); return vmax(__uint_as_float(s[0]),__uint_as_float(s[1])); }
DI float sw16max(float a){ auto s=__builtin_amdgcn_permlane16_swap(__float_as_uint(a),__float_as_uint(a),false,false); return vmax(__uint_as_float(s[0]),__uint_as_float(s[1])); }
DI float wave_sum(float v){ v+=dpp<DXOR1>(v); v+=dpp<DXOR2>(v); v+=dpp<DHMIR>(v); v+=dpp<DROR8>(v); v=sw16add(v,v); v=sw32add(v,v); return v; }
DI void dma16(const void* gbase,u32 voff,u32 ldsb){
  u32 keep;
  asm volatile("s_mov_b32 %0, m0\n\ts_mov_b32 m0, %3\n\ts_nop 0\n\tglobal_load_lds_dwordx4 %1, %2 nt\n\ts_mov_b32 m0, %0" : "=&s"(keep) : "v"(voff),"s"(gbase),"s"(ldsb) : "memory");
}
DI constexpr int fsw(int row){ return ((row&3)<<2)|((row>>2)&3); }
DI bool flag_ok(v4u f,u64 want){ return (f.x==(u32)want)&(f.y==(u32)(want>>32))&(f.z==(u32)want)&(f.w==(u32)(want>>32)); }
DI void wait_t(u64 t0,int ticks){ while(rt()-t0<(u64)ticks) __builtin_amdgcn_s_sleep(1); }
DI void ln_row(v4u xr,int lane,int tq,lds8* dst){
  float v[8]={blo(xr.x),bhi(xr.x),blo(xr.y),bhi(xr.y),blo(xr.z),bhi(xr.z),blo(xr.w),bhi(xr.w)};
  const float mean=wave_sum(((v[0]+v[1])+(v[2]+v[3]))+((v[4]+v[5])+(v[6]+v[7])))*(1.f/DM);
  float qq=0.f;
  #pragma unroll
  for(int i=0;i<8;i++){ const float d=v[i]-mean; qq+=d*d; }
  const float var=wave_sum(qq)*(1.f/DM);
  const float mb=rbf(mean), isd=__builtin_amdgcn_rcpf(rbf(__builtin_amdgcn_sqrtf(rbf(rbf(var)+1e-5f))));
  float y[8];
  #pragma unroll
  for(int i=0;i<8;i++) y[i]=rbf(v[i]-mb)*isd;
  if((lane>>4)==tq) *(LAS v4u*)(dst+16*(lane&15))=v4u{pk2(y[0],y[1]),pk2(y[2],y[3]),pk2(y[4],y[5]),pk2(y[6],y[7])};
}

extern "C" __global__ void __launch_bounds__(512,2) mla_mlp_kernel(const u16* __restrict__ x,const u16* __restrict__ ckv,const u16* __restrict__ wq,const u16* __restrict__ wuk,
    const u16* __restrict__ wuv,const u16* __restrict__ wo,const u16* __restrict__ w1,const u16* __restrict__ b1,const u16* __restrict__ w2,const u16* __restrict__ b2,
    u16* __restrict__ out,unsigned char* __restrict__ ws){
  extern __shared__ __attribute__((aligned(16))) unsigned char smem[];
  lds8* L=(lds8*)smem;
  const u32 Lb=(u32)(size_t)L;
  const int tid=threadIdx.x, lane=tid&63, w=__builtin_amdgcn_readfirstlane(tid>>6);
  const int wg=blockIdx.x, xc=wg&7, j=wg>>3, jj=j&15;
  const bool isF=j<16;
  const int fi=xc*16+jj, th=fi>>2, tq=fi&3;
  const int g=lane>>4, q4=(lane&15)>>2, p4=lane&3, hh=lane>>5, l31=lane&31;
  const __amdgpu_buffer_rsrc_t rws=mkrs(ws,WS_END);
  const u64 t_start=rt();
  asm volatile("" :: "s"(x),"s"(ckv),"s"(wq),"s"(wuk),"s"(ws));
  const u32 rowoff=(u32)((128*tq+16*w+g)*8192+th*256);
#define DMA_T(p,dst,i) dma16(p,rowoff+(u32)(4*(i)*8192)+16*((lane&15)^(2*(g|(((i)>>1)<<2)))),Lb+(dst)+(16*w+4*(i))*256)
  const int troff=(8*g+q4)*256+(w^(q4|((g&1)<<2)))*32+p4*8;
  const int r0=4*g+q4;
  if(isF){
    const bool mlp = fi<FF/16;
    if(w==7){
      v2u e2;
      st128(rws,WS_QA+g*32768+th*1024+tq*256+(lane&15)*16,v4u{SENT,SENT,SENT,SENT});
      st128(rws,WS_QA+131072+g*32768+th*1024+tq*256+(lane&15)*16,v4u{SENT,SENT,SENT,SENT});
      asm volatile("global_load_dwordx2 %0, %1, %2" : "=v"(e2) : "v"(0), "s"(ws) : "memory");
      #pragma unroll
      for(int i=0;i<4;i++) dma16(wuk,rowoff+(u32)(4*i*8192)+16*((lane&15)^(4*i+g)),Lb+L_WUK+(16*w+4*i)*256);
      asm volatile("s_waitcnt vmcnt(4)" : "+v"(e2) :: "memory");
      const u64 wn=(((u64)e2.y<<32)|e2.x)+1; if(lane<8) __hip_atomic_store((gu64*)(ws+WS_FLAG1)+lane*128+fi,wn,RLX_AGENT);
      if(lane==0) *(LAS u64*)(L+L_EP)=wn;
      VMW(1);
    } else {
      #pragma unroll
      for(int i=0;i<4;i++) dma16(wuk,rowoff+(u32)(4*i*8192)+16*((lane&15)^(4*i+g)),Lb+L_WUK+(16*w+4*i)*256);
      if(w>=5){
        u64 pc; u32 back, size;
        asm volatile("s_getpc_b64 %0\n.Lmla_pc_anchor:\n\ts_mov_b32 %1, .Lmla_pc_anchor-mla_mlp_kernel\n\ts_mov_b32 %2, .Lmla_code_end-mla_mlp_kernel" : "=s"(pc),"=s"(back),"=s"(size));
        const u32 off=(u32)((j+16*(6-w))*1024+16*lane);
        if(off+16<=size){ v4u t_; asm volatile("global_load_dwordx4 %0, %1, %2\n\ts_waitcnt vmcnt(0)" : "=v"(t_) : "v"(off), "s"(pc-back) : "memory"); }
      }
      VMW(0);
    }
    v4u auk[4];
    #pragma unroll
    for(int k=0;k<4;k++){
      lds8* a=L+L_WUK+(16*w+(lane&15))*256+8*(g&1);
      const v2u lo=*(LAS v2u*)(a+16*((4*k+(g>>1))^(lane&15))), hi=*(LAS v2u*)(a+16*((4*k+2+(g>>1))^(lane&15)));
      auk[k]=v4u{lo.x,lo.y,hi.x,hi.y};
    }
    BAR();
    STAMP(1);
    const v2u ep=*(LAS v2u*)(L+L_EP);
    const u32 tag=__builtin_amdgcn_readfirstlane(ep.x);
    const u64 want=((u64)__builtin_amdgcn_readfirstlane(ep.y)<<32)|tag;
    wait_t(t_start,T_E1);
    {
      v4u qf;
      _Pragma("unroll 1") for(int sp=0;sp<SPIN_CAP;++sp){ FENCE();
        qf=ld128<AUX_SC1>(rws,WS_Q1+th*8192+w*1024+lane*16);
        if(__all((qf.y==tag)&(qf.w==tag))) break;
        __builtin_amdgcn_s_sleep(SLP_S);
      }
      const int t=4*(w&1)+g;
      *(LAS v2u*)(L+L_QL+(w>>1)*1024+(t>>1)*256+(lane&15)*16+(t&1)*8)=v2u{qf.x,qf.z};
    }
    BAR();
    STAMP(2);
    {
      v4u qb[4];
      #pragma unroll
      for(int k=0;k<4;k++) qb[k]=lrd128(L+L_QL+q4*1024+k*256+g*64+p4*16);
      SB();
      f32x4 acc={0,0,0,0};
      #pragma unroll
      for(int k=0;k<4;k++) acc=mfma16(auk[k],qb[k],acc);
      #pragma unroll
      for(int r=0;r<4;r++){ acc[r]+=dpp<DROR4>(acc[r]); acc[r]+=dpp<DROR8>(acc[r]); }
      const float SC=0.08838834764831845f*1.4426950408889634f;
      if((lane&15)<4) *(LAS v2u*)(L+L_QS+(lane&15)*256+(16*w+4*g)*2)=v2u{nos(pk2(acc[0]*SC,acc[1]*SC)),nos(pk2(acc[2]*SC,acc[3]*SC))};
    }
    BAR();
    if(w<2){ v4u v=lrd128(L+L_QS+lane*16); st128(rws,WS_QA+w*131072+g*32768+th*1024+tq*256+(lane&15)*16,v); }
    STAMP(3);
    u32 b1raw[4];
    #pragma unroll
    for(int r=0;r<4;r++) b1raw[r]=__builtin_amdgcn_raw_buffer_load_b16(mkrs(b1,FF*2),(16*fi+4*g+r)*2,0,0);
    u32 xraw=__builtin_amdgcn_raw_buffer_load_b16(mkrs(x,NB*DM*2),(p4*DM+4*fi+q4)*2,0,0);
    u32 b2raw=__builtin_amdgcn_raw_buffer_load_b16(mkrs(b2,DM*2),(4*fi+q4)*2,0,0);
    SB();
#define DMA_WO(i) dma16(wo,(u32)((th*128+8*(i)+w)*1024+16*(lane^(2*((w&3)|(((i)&1)<<2))))),Lb+L_WO+(8*(i)+w)*1024)
    wait_t(t_start,T_WO);
    #pragma unroll
    for(int i=0;i<10;i++){ if(i>=WO_1) VMW(WO_1-1); DMA_WO(i); }
    wait_t(t_start,T_TAIL);
    v4u xr;
    asm volatile("global_load_dwordx4 %0, %1, %2" : "=v"(xr) : "v"((w&3)*1024+16*lane), "s"(x) : "memory");
    const int thm=mlp?th:0, fim=mlp?fi:0;
    #pragma unroll
    for(int i=0;i<2;i++){ const int r=8*i+(lane>>3); dma16(w1,(u32)((128*tq+16*w+r)*(FF*2)+thm*128+16*((lane&7)^(2*(((r>>1)&1)|(i<<1))))),Lb+L_W1+(16*w+8*i)*128); }
    #pragma unroll
    for(int i=0;i<4;i++) DMA_T(wuv,L_WUV,i);
    #pragma unroll
    for(int i=0;i<2;i++){ const int r=8*i+w; dma16(w2,(u32)((16*fim+r)*1024+16*(lane^(2*(r&7)))),Lb+L_W2+r*1024); }
    v4u rwo[6];
    #pragma unroll
    for(int i=0;i<6;i++) asm volatile("global_load_dwordx4 %0, %1, %2 nt" : "=v"(rwo[i]) : "v"((th*128+80+8*i+w)*1024+16*lane), "s"(wo) : "memory");
    asm volatile("s_waitcnt vmcnt(12)" : "+v"(xr) :: "memory");
    STAMP(4);
    if(w<4) ln_row(xr,lane,tq,L+L_XN+w*320);
    BAR();
    if(mlp && w<4){
      v4u a1[4], bq[4];
      #pragma unroll
      for(int ks=0;ks<4;ks++){ lds8* a=L+L_W1+(32*ks+8*g+q4)*128+32*(w^((q4>>1)|((g&1)<<1)))+p4*8; a1[ks]=trd2(a,a+512); bq[ks]=lrd128(L+L_XN+p4*320+(32*ks+8*g)*2); }
      SB();
      f32x4 acc={0,0,0,0};
      #pragma unroll
      for(int ks=0;ks<4;ks++) acc=mfma16(a1[ks],bq[ks],acc);
      if((lane&15)<4){
        st128(rws,WS_HM+fi*2048+w*512+g*128+p4*16,v4u{__float_as_uint(acc[0]),tag,__float_as_uint(acc[1]),tag});
        st128(rws,WS_HM+fi*2048+w*512+g*128+64+p4*16,v4u{__float_as_uint(acc[2]),tag,__float_as_uint(acc[3]),tag}); }
    }
    VMW(6);
    BAR();
    v4u auv[4]; v2u aw2[4];
    #pragma unroll
    for(int k=0;k<4;k++){ lds8* a=L+L_WUV+k*8192+troff; auv[k]=trd2(a,a+1024); }
    #pragma unroll
    for(int t=0;t<4;t++) aw2[t]=trd(L+L_W2+r0*1024+(w>>1)*256+32*((4*(w&1)+t)^(r0&7))+p4*8);
    BAR();
    if(mlp && w==4){
      v4u h0,h1;
      _Pragma("unroll 1") for(int sp=0;sp<SPIN_CAP;++sp){ FENCE();
        h0=ld128<AUX_SC1>(rws,WS_HM+(th*4+q4)*2048+tq*512+g*128+p4*16);
        h1=ld128<AUX_SC1>(rws,WS_HM+(th*4+q4)*2048+tq*512+g*128+64+p4*16);
        if(__all((h0.y==tag)&(h0.w==tag)&(h1.y==tag)&(h1.w==tag))) break;
        __builtin_amdgcn_s_sleep(SLP);
      }
      float hv[4]={__uint_as_float(h0.x),__uint_as_float(h0.z),__uint_as_float(h1.x),__uint_as_float(h1.z)};
      #pragma unroll
      for(int r=0;r<4;r++){ float sv=hv[r]; sv+=dpp<DROR4>(sv); sv+=dpp<DROR8>(sv); float h=rbf(rbf(sv)+blo(b1raw[r])); h=h>0.f?h:0.f; hv[r]=h*h; }
      *(LAS v2u*)(L+L_H2+lane*8)=v2u{pk2(hv[0],hv[1]),pk2(hv[2],hv[3])};
    }
    asm volatile("s_waitcnt vmcnt(0)" : "+v"(rwo[0]),"+v"(rwo[1]),"+v"(rwo[2]),"+v"(rwo[3]),"+v"(rwo[4]),"+v"(rwo[5]) :: "memory");
    #pragma unroll
    for(int i=0;i<6;i++) *(LAS v4u*)(L+L_WO+(80+8*i+w)*1024+16*(lane^(2*((w&3)|((i&1)<<2)))))=rwo[i];
    v4u awo[4][4];
#define LOAD_AWO(ks) _Pragma("unroll") for(int t=0;t<4;t++){ lds8* a=L+L_WO+(32*(ks)+8*g+q4)*1024+(w>>1)*256+p4*8+32*((4*(w&1)+t)^(q4|((g&1)<<2))); awo[ks][t]=trd2(a,a+4096); }
    LOAD_AWO(0); LOAD_AWO(1);
    SB();
    STAMP(5);
    BAR();
    asm volatile("" : "+v"(xraw), "+v"(b2raw));
    const float xb_res=blo(xraw)+blo(b2raw);
    LOAD_AWO(2); LOAD_AWO(3);
    f32x4 oacc[4];
    #pragma unroll
    for(int t=0;t<4;t++) oacc[t]=f32x4{0,0,0,0};
    if(mlp){
      const v2u h2=*(LAS v2u*)(L+L_H2+lane*8);
      #pragma unroll
      for(int t=0;t<4;t++) oacc[t]=mfma16(v4u{aw2[t].x,aw2[t].y,0,0},v4u{h2.x,h2.y,0,0},oacc[t]);
    }
    SB();
    STAMP(6);
    if(w==0){
      wait_t(t_start,T_GUN);
      v4u v0,v1;
      const u32 tagh=(u32)(want>>32)^0x5A5A5A5Au;
      _Pragma("unroll 1") for(int sp=0;sp<SPIN_CAP;++sp){ FENCE();
        v0=ld128<AUX_SC1>(rws,WS_LSE+(th>>2)*8192+lane*64+(th&3)*16);
        v1=ld128<AUX_SC1>(rws,WS_LSE+(th>>2)*8192+4096+lane*64+(th&3)*16);
        if(__all((v0.y==tag)&(v1.y==tag)&(v0.w==tagh)&(v1.w==tagh)&(v0.x==v0.z)&(v1.x==v1.z))) break; __builtin_amdgcn_s_sleep(SLP_S);
      }
      STAMP(7);
      *(LAS u32*)(L+L_WG+hh*128+(l31&7)*16+(l31>>3)*4)=v0.x;
      *(LAS u32*)(L+L_WG+256+hh*128+(l31&7)*16+(l31>>3)*4)=v1.x;
    }
    BAR();
    {
      const int b=w>>1, half=w&1, slo=lane>>3, q=lane&7;
      const int po_off=WS_PO+(fi*128+b*32+slo)*256+half*128+q*16;
      v4u po[4];
      #pragma unroll
      for(int i=0;i<4;i++) po[i]=ld128<AUX_SC1>(rws,po_off+i*2048);
      SB();
      const f32x4 lf=*(LAS f32x4*)(L+L_WG+b*128+slo*16);
      float M=vmax(vmax3(lf[0],lf[1],lf[2]),lf[3]);
      M=vmax(M,dpp<DROR8>(M)); M=sw16max(M); M=sw32max(M);
      f32x4 e;
      #pragma unroll
      for(int i=0;i<4;i++) e[i]=__builtin_amdgcn_exp2f(lf[i]-M);
      float Ls=(e[0]+e[1])+(e[2]+e[3]);
      Ls+=dpp<DROR8>(Ls); Ls=sw16add(Ls,Ls); Ls=sw32add(Ls,Ls);
      e*=__builtin_amdgcn_rcpf(Ls);
      SB();
      STAMP(8);
      float a[8];
      #pragma unroll
      for(int k=0;k<8;k++) a[k]=0.f;
      #pragma unroll
      for(int i=0;i<4;i++){
        a[0]+=e[i]*blo(po[i].x); a[1]+=e[i]*bhi(po[i].x); a[2]+=e[i]*blo(po[i].y); a[3]+=e[i]*bhi(po[i].y);
        a[4]+=e[i]*blo(po[i].z); a[5]+=e[i]*bhi(po[i].z); a[6]+=e[i]*blo(po[i].w); a[7]+=e[i]*bhi(po[i].w);
      }
      float c4[4], c2[2];
      #pragma unroll
      for(int k=0;k<4;k++) c4[k]=sw32add(a[k],a[k+4]);
      c2[0]=sw16add(c4[0],c4[2]); c2[1]=sw16add(c4[1],c4[3]);
      const bool b3=(lane>>3)&1;
      const float keep=b3?c2[1]:c2[0], send=b3?c2[0]:c2[1];
      const float r=keep+dpp<DROR8>(send);
      const int col=8*q+4*hh+2*((lane>>4)&1)+(b3?1:0);
      *(LAS u16*)(L+L_CL+b*320+(half*64+col)*2)=(u16)pk2(r,0.f);
    }
    BAR();
    STAMP(9);
    {
      v4u bc[4];
      #pragma unroll
      for(int k=0;k<4;k++) bc[k]=lrd128(L+L_CL+p4*320+(32*k+8*g)*2);
      SB();
      f32x4 acc={0,0,0,0};
      #pragma unroll
      for(int k=0;k<4;k++) acc=mfma16(auv[k],bc[k],acc);
      if((lane&15)<4) *(LAS v2u*)(L+L_CX+p4*320+(16*w+4*g)*2)=v2u{pk2(acc[0],acc[1]),pk2(acc[2],acc[3])};
    }
    BAR();
    {
      v4u bc[4];
      #pragma unroll
      for(int ks=0;ks<4;ks++) bc[ks]=lrd128(L+L_CX+p4*320+(32*ks+8*g)*2);
      SB();
      #pragma unroll
      for(int ks=0;ks<4;ks++)
        #pragma unroll
        for(int t=0;t<4;t++) oacc[t]=mfma16(awo[ks][t],bc[ks],oacc[t]);
      f32x4 v;
      #pragma unroll
      for(int r=0;r<4;r++){ const float lo=(q4&1)?oacc[1][r]:oacc[0][r], hi=(q4&1)?oacc[3][r]:oacc[2][r]; v[r]=(q4&2)?hi:lo; }
      st128(rws,WS_OUT+(16*w+4*q4+g)*8192+fi*64+p4*16,v4u{pk2(v[0],v[1]),tag,pk2(v[2],v[3]),tag});
    }
    STAMP(10);
    { const u64 t1=rt(); wait_t(t1,D_E4); }
    v4u pv;
    _Pragma("unroll 1") for(int sp=0;sp<SPIN_CAP;++sp){ FENCE();
      pv=ld128<AUX_SC1>(rws,WS_OUT+fi*8192+tid*16);
      if(__all((pv.y==tag)&(pv.w==tag))) break;
      __builtin_amdgcn_s_sleep(SLP_S);
    }
    STAMP(11);
    const float u0=sw32add(blo(pv.x),blo(pv.z)), u1=sw32add(bhi(pv.x),bhi(pv.z));
    float v=sw16add(u0,u1);
    v+=dpp<DROR4>(v); v+=dpp<DROR8>(v);
    if((lane&12)==0) *(LAS float*)(L+L_RED+w*64+(g*4+p4)*4)=v;
    BAR();
    if(tid<16){
      float a=0.f;
      #pragma unroll
      for(int ww=0;ww<8;ww++) a+=*(LAS float*)(L+L_RED+ww*64+tid*4);
      out[(tid&3)*DM+4*fi+(tid>>2)]=(u16)pk2(xb_res+a,0.f);
    }
    if(wg==0 && tid==0) __hip_atomic_store((gu64*)(ws+WS_EPOCH),want,RLX_AGENT);
    asm volatile(".Lmla_code_end:");
  } else {
    const int ab=fi>>5, as=fi&31;
    const int fl=fsw(l31);
    const int blk=(lane>>4)&1, sgm=w>>1;
    const u16* gb=ckv+((size_t)ab*SK+as*128+32*(w&3))*DC;
#define DMA_CKV(r) dma16(gb+(r)*DC,(u32)((lane*16)^(16*fsw(r))),Lb+L_CKV+(32*w+(r))*1024)
    #pragma unroll
    for(int i=0;i<4;i++) DMA_T(wq,L_AWQ,i);
    if(w<4){
      v2u e2;
      asm volatile("global_load_dwordx2 %0, %1, %2" : "=v"(e2) : "v"(0), "s"(ws) : "memory");
      wait_t(t_start,T_CKV0);
      #pragma unroll
      for(int r=0;r<INFL0;r++) DMA_CKV(r);
      asm volatile("s_waitcnt vmcnt(%1)" : "+v"(e2) : "i"(INFL0) : "memory");
      if(w==0 && lane==0) *(LAS u64*)(L+L_AEP)=(((u64)e2.y<<32)|e2.x)+1;
    } else {
      v4u xr;
      asm volatile("global_load_dwordx4 %0, %1, %2\n\ts_waitcnt vmcnt(0)" : "=v"(xr) : "v"((w&3)*1024+16*lane), "s"(x) : "memory");
      ln_row(xr,lane,tq,L+L_AXN+(w-4)*320);
    }
    BAR();
    ESTAMP(2);
    u32 tag; u64 want;
    {
      v4u aq[4], bq[4];
      #pragma unroll
      for(int ks=0;ks<4;ks++){ lds8* a=L+L_AWQ+ks*8192+troff; aq[ks]=trd2(a,a+1024); bq[ks]=lrd128(L+L_AXN+p4*320+(32*ks+8*g)*2); }
      const v2u e=*(LAS v2u*)(L+L_AEP);
      SB();
      f32x4 acc={0,0,0,0};
      #pragma unroll
      for(int ks=0;ks<4;ks++) acc=mfma16(aq[ks],bq[ks],acc);
      tag=__builtin_amdgcn_readfirstlane(e.x);
      if((lane&15)<4) st128(rws,WS_Q1+fi*2048+w*256+g*64+p4*16,v4u{pk2(acc[0],acc[1]),tag,pk2(acc[2],acc[3]),tag});
      want=((u64)__builtin_amdgcn_readfirstlane(e.y)<<32)|tag;
    }
    ESTAMP(3);
    BAR();
    v4u vf[8][2];
#define LOAD_VF() do{ _Pragma("unroll") for(int ks=0;ks<8;ks++) _Pragma("unroll") for(int t=0;t<2;t++){ \
        const int ch=8*(w&1)+4*t+2*blk+(p4>>1); \
        lds8* a0=L+L_CKV+(16*ks+8*hh+q4)*1024+256*sgm+16*(ch^((q4<<2)|(2*hh)))+8*(p4&1); \
        lds8* a1=L+L_CKV+(16*ks+8*hh+4+q4)*1024+256*sgm+16*(ch^((q4<<2)|(2*hh+1)))+8*(p4&1); \
        vf[ks][t]=trd2(a0,a1); } SB(); }while(0)
#define GATHER_QA() do{ v4u qa[4]; const int qo=WS_QA+(xc&1)*131072+ab*32768+4*w*1024+16*lane; \
      _Pragma("unroll") for(int i=0;i<4;i++) qa[i]=ld128<AUX_SC1>(rws,qo+i*1024); \
      _Pragma("unroll 1") for(int sp=0;sp<SPIN_CAP;++sp){ u32 bad=0; \
        _Pragma("unroll") for(int i=0;i<4;i++) bad|=(__all(ok4(qa[i]))?0u:1u)<<i; \
        if(!bad) break; \
        __builtin_amdgcn_s_sleep(SLP_S); FENCE(); \
        _Pragma("unroll") for(int i=0;i<4;i++) if((bad>>i)&1) qa[i]=ld128<AUX_SC1>(rws,qo+i*1024); } \
      _Pragma("unroll") for(int i=0;i<4;i++) *(LAS v4u*)(L+L_QAS+(4*w+i)*1024+((lane*16)^(16*((i<<2)|(w&3)))))=qa[i]; }while(0)
    float mx, lse;
    if(w<4){
      #pragma unroll
      for(int r=INFL0;r<32;r++){
        if(r>=CKV_INFL) VMW(CKV_INFL-1);
        DMA_CKV(r);
      }
      BAR();
      GATHER_QA();
      VMW(0);
      ESTAMP(0);
      v4u af[32];
      #pragma unroll
      for(int k=0;k<32;k++) af[k]=lrd128(L+L_CKV+(32*w+l31)*1024+256*(k>>3)+16*((2*(k&7)+hh)^fl));
      BAR();
      ESTAMP(1);
      f32x16 sacc;
      #pragma unroll
      for(int i=0;i<16;i++) sacc[i]=0.f;
      v4u bf[12];
      #pragma unroll
      for(int k=0;k<12;k++) bf[k]=lrd128(L+L_QAS+l31*1024+256*(k>>3)+16*((2*(k&7)+hh)^fl));
      SB();
      #pragma unroll
      for(int k=0;k<32;k++){
        sacc=mfma32(af[k],bf[k%12],sacc);
        if(k+12<32) bf[k%12]=lrd128(L+L_QAS+l31*1024+256*((k+12)>>3)+16*((2*((k+12)&7)+hh)^fl));
        SB();
      }
      float mloc=vmax3(vmax3(vmax3(sacc[0],sacc[1],sacc[2]),vmax3(sacc[3],sacc[4],sacc[5]),vmax3(sacc[6],sacc[7],sacc[8])),
                       vmax3(vmax3(sacc[9],sacc[10],sacc[11]),vmax3(sacc[12],sacc[13],sacc[14]),sacc[15]),sacc[15]);
      mloc=sw32max(mloc);
      BAR();
      STAMP(4);
      if(lane<32) *(LAS float*)(L+L_MX+w*128+lane*4)=mloc;
      BAR();
      mx=vmax(vmax3(*(LAS float*)(L+L_MX+l31*4),*(LAS float*)(L+L_MX+128+l31*4),*(LAS float*)(L+L_MX+256+l31*4)),*(LAS float*)(L+L_MX+384+l31*4));
      float ls=0.f;
      #pragma unroll
      for(int a=0;a<4;a++){
        float p0=__builtin_amdgcn_exp2f(sacc[4*a]-mx), p1=__builtin_amdgcn_exp2f(sacc[4*a+1]-mx), p2=__builtin_amdgcn_exp2f(sacc[4*a+2]-mx), p3=__builtin_amdgcn_exp2f(sacc[4*a+3]-mx);
        const u32 u0=pk2(p0,p1), u1=pk2(p2,p3);
        ls+=(p0+p1)+(p2+p3);
        *(LAS v2u*)(L+L_P+l31*256+16*((4*w+a)^fl)+8*hh)=v2u{u0,u1};
      }
      ls=sw32add(ls,ls);
      if(lane<32) *(LAS float*)(L+L_LS+w*128+lane*4)=ls;
      BAR();
      STAMP(5);
      LOAD_VF();
    } else {
      const int wv=w-4;
      wait_t(t_start,T_FLAG1);
      _Pragma("unroll 1") for(int sp=0;sp<SPIN_CAP;++sp){ FENCE();
        if(__all(flag_ok(ld128<AUX_SC1>(rws,WS_FLAG1+xc*1024+wv*256+(lane&15)*16),want))) break;
        __builtin_amdgcn_s_sleep(SLP);
      }
      ESTAMP(0);
      wait_t(t_start,T_GATHER);
      BAR();
      GATHER_QA();
      ESTAMP(1);
      BAR();
      LOAD_VF();
      BAR();
      BAR();
      mx=0.f;
      BAR();
    }
    {
      v4u pb[8];
      #pragma unroll
      for(int ks=0;ks<8;ks++) pb[ks]=lrd128(L+L_P+l31*256+16*((2*ks+hh)^fl));
      const float lsum=(*(LAS float*)(L+L_LS+l31*4)+*(LAS float*)(L+L_LS+128+l31*4))+(*(LAS float*)(L+L_LS+256+l31*4)+*(LAS float*)(L+L_LS+384+l31*4));
      SB();
      f32x16 o[2];
      #pragma unroll
      for(int i=0;i<16;i++){ o[0][i]=0.f; o[1][i]=0.f; }
      #pragma unroll
      for(int t=0;t<2;t++)
        #pragma unroll
        for(int ks=0;ks<8;ks++) o[t]=mfma32(vf[ks][t],pb[ks],o[t]);
      const float inv=__builtin_amdgcn_rcpf(lsum);
      lse=mx+__builtin_amdgcn_logf(lsum);
      STAMP(6);
      #pragma unroll
      for(int t=0;t<2;t++)
        #pragma unroll
        for(int a=0;a<4;a++)
          *(LAS v2u*)(L+L_CKV+l31*1024+256*sgm+16*((8*(w&1)+4*t+a)^fl)+8*hh)=v2u{pk2(o[t][4*a]*inv,o[t][4*a+1]*inv),pk2(o[t][4*a+2]*inv,o[t][4*a+3]*inv)};
      v4u ov[4];
      #pragma unroll
      for(int i=0;i<4;i++){ const int head=8*i+(lane>>3); ov[i]=lrd128(L+L_CKV+head*1024+256*sgm+16*((8*(w&1)+(lane&7))^fsw(head))); }
      SB();
      #pragma unroll
      for(int i=0;i<4;i++){ const int head=8*i+(lane>>3); st128(rws,WS_PO+((head*4+sgm)*128+ab*32+as)*256+(w&1)*128+(lane&7)*16,ov[i]); }
    }
    STAMP(7);
    VMW(0);
    BAR();
    if(w==0 && lane<32) st128(rws,WS_LSE+(lane>>2)*8192+fi*64+(lane&3)*16,v4u{__float_as_uint(lse),tag,__float_as_uint(lse),(u32)(want>>32)^0x5A5A5A5Au});
    STAMP(11);
  }
  STAMP(15);
}

extern "C" void kernel_launch(void* const* d_in,const int* in_sizes,int n_in,void* d_out,int out_size,void* d_ws,size_t ws_size,hipStream_t stream){
  (void)hipFuncSetAttribute((const void*)mla_mlp_kernel,hipFuncAttributeMaxDynamicSharedMemorySize,LDS_BYTES);
  mla_mlp_kernel<<<256,512,LDS_BYTES,stream>>>((const u16*)d_in[0],(const u16*)d_in[1],(const u16*)d_in[2],(const u16*)d_in[3],(const u16*)d_in[4],(const u16*)d_in[5],
     (const u16*)d_in[7],(const u16*)d_in[8],(const u16*)d_in[9],(const u16*)d_in[10],(u16*)d_out,(unsigned char*)d_ws);
}
```

```cpp
#include <hip/hip_runtime.h>
#include <cstring>
#define PROBE 203
typedef unsigned short u16; typedef unsigned u32; typedef unsigned long long u64;
#define GAS __attribute__((address_space(1)))
#define LAS __attribute__((address_space(3)))
typedef u32 v4u __attribute__((ext_vector_type(4)));
typedef u32 v2u __attribute__((ext_vector_type(2)));
typedef float f32x2 __attribute__((ext_vector_type(2)));
typedef float f32x4 __attribute__((ext_vector_type(4)));
typedef float f32x16 __attribute__((ext_vector_type(16)));
typedef __bf16 bf16x8 __attribute__((ext_vector_type(8)));
typedef __bf16 bf16x2 __attribute__((ext_vector_type(2)));
typedef short s16x4 __attribute__((ext_vector_type(4)));
typedef GAS u64 gu64;
typedef LAS unsigned char lds8;
#define RLX_AGENT __ATOMIC_RELAXED, __HIP_MEMORY_SCOPE_AGENT
#define DI __device__ __forceinline__

constexpr int NB=4, NH=32, SK=4096, HD=128, DM=512, DC=512, FF=1792;
constexpr int WS_EPOCH=0, WS_FLAG1=8192, WS_Q1=32768, WS_HM=WS_Q1+262144, WS_QA=WS_HM+262144, WS_LSE=WS_QA+262144, WS_OUT=WS_LSE+65536,
              WS_PO=WS_OUT+(1<<20), WS_END=WS_PO+(4<<20), WS_STAMP=WS_END;
constexpr int L_CKV=0, L_QAS=131072, L_P=L_QAS, L_MX=L_QAS+8192, L_LS=L_QAS+8704, L_AWQ=L_QAS, L_AXN=126*1024, L_AEP=L_AXN+1536;
constexpr int L_WUK=0, L_WO=0, L_W2=81920, L_WUV=98304, L_W1=131072, L_XN=147456, L_QS=148736, L_CL=149760, L_H2=151040, L_RED=151552, L_EP=152064, L_QL=152576, L_WG=156672, L_CX=157184, L_STF=158720;
constexpr int LDS_BYTES=163840;
constexpr u32 SENT=0xFFFFFFFFu;
constexpr int SPIN_CAP=1<<20;
constexpr int AUX_NT=2, AUX_SC1=16;
#define CKV_INFL 12
#define INFL0 12
#define T_CKV0 120
#define T_E1 150
#define T_FLAG1 240
#define T_GATHER 400
#define WO_1 4
#define T_WO 0
#define T_TAIL 460
#define T_GUN 620
#define D_E4 50
#define SLP 4
#define SLP_S 1
static_assert(CKV_INFL<=30,"ring");
#define STAMP(k) do{ if((isF?0:100)+(k)==PROBE) probe_t=rt()-t_start; }while(0)
#define ESTAMP(i) do{ if(200+(w<4?0:10)+(i)==PROBE) probe_t=rt()-t_start; }while(0)

DI u64 rt(){ return __builtin_amdgcn_s_memrealtime(); }
DI u64 ct(){ u64 t; asm volatile("s_memtime %0\n\ts_waitcnt lgkmcnt(0)" : "=s"(t) :: "memory"); return t; }
DI __amdgpu_buffer_rsrc_t mkrs(const void* p, int bytes){ return __builtin_amdgcn_make_buffer_rsrc((void*)p,0,bytes,0x00020000); }
template<int AUX> DI v4u ld128(__amdgpu_buffer_rsrc_t rs,int off){ return __builtin_amdgcn_raw_buffer_load_b128(rs,off,0,AUX); }
template<int AUX> DI u32 ld32(__amdgpu_buffer_rsrc_t rs,int off){ return __builtin_amdgcn_raw_buffer_load_b32(rs,off,0,AUX); }
DI void st128(__amdgpu_buffer_rsrc_t rs,int off,v4u v){ __builtin_amdgcn_raw_buffer_store_b128(v,rs,off,0,AUX_SC1); }
DI void st32(__amdgpu_buffer_rsrc_t rs,int off,u32 v){ __builtin_amdgcn_raw_buffer_store_b32(v,rs,off,0,AUX_SC1); }
DI f32x4 mfma16(v4u a,v4u b,f32x4 c){ return __builtin_amdgcn_mfma_f32_16x16x32_bf16(__builtin_bit_cast(bf16x8,a),__builtin_bit_cast(bf16x8,b),c,0,0,0); }
DI f32x16 mfma32(v4u a,v4u b,f32x16 c){ return __builtin_amdgcn_mfma_f32_32x32x16_bf16(__builtin_bit_cast(bf16x8,a),__builtin_bit_cast(bf16x8,b),c,0,0,0); }
DI v2u trd(lds8* p){ return __builtin_bit_cast(v2u,__builtin_amdgcn_ds_read_tr16_b64_v4i16((LAS s16x4*)p)); }
DI v4u trd2(lds8* p0,lds8* p1){ v2u a=trd(p0), b=trd(p1); return v4u{a.x,a.y,b.x,b.y}; }
DI v4u lrd128(lds8* p){ return *(LAS v4u*)p; }
DI u32 pk2(float lo,float hi){ f32x2 f={lo,hi}; return __builtin_bit_cast(u32,__builtin_convertvector(f,bf16x2)); }
DI float rbf(float x){ return __uint_as_float(pk2(x,0.f)<<16); }
DI float vmax(float a,float b){ return fmaxf(a,b); }
DI float vmax3(float a,float b,float c){ return fmaxf(fmaxf(a,b),c); }
DI float blo(u32 w){ return __uint_as_float(w<<16); }
DI float bhi(u32 w){ return __uint_as_float(w&0xFFFF0000u); }
DI u32 nos(u32 w){ return w<0xFFFFFFFEu? w:0xFFFFFFFEu; }
DI v4u nos4(v4u v){ return v4u{nos(v.x),nos(v.y),nos(v.z),nos(v.w)}; }
DI bool ok4(v4u v){ return (v.x!=SENT)&(v.y!=SENT)&(v.z!=SENT)&(v.w!=SENT); }
#define BAR() asm volatile("s_waitcnt lgkmcnt(0)\n\ts_barrier" ::: "memory")
#define VMW(n) asm volatile("s_waitcnt vmcnt(%0)" :: "i"(n) : "memory")
#define FENCE() asm volatile("" ::: "memory")
#define SB() __builtin_amdgcn_sched_barrier(0)
template<int CTRL> DI float dpp(float x){ return __builtin_bit_cast(float,__builtin_amdgcn_mov_dpp(__builtin_bit_cast(int,x),CTRL,0xf,0xf,true)); }
constexpr int DXOR1=0xB1, DXOR2=0x4E, DHMIR=0x141, DROR8=0x128, DROR4=0x124;
DI float sw32add(float a,float b){ auto s=__builtin_amdgcn_permlane32_swap(__float_as_uint(a),__float_as_uint(b),false,false); return __uint_as_float(s[0])+__uint_as_float(s[1]); }
DI float sw16add(float a,float b){ auto s=__builtin_amdgcn_permlane16_swap(__float_as_uint(a),__float_as_uint(b),false,false); return __uint_as_float(s[0])+__uint_as_float(s[1]); }
DI float sw32max(float a){ auto s=__builtin_amdgcn_permlane32_swap(__float_as_uint(a),__float_as_uint(a),false,false); return vmax(__uint_as_float(s[0]),__uint_as_float(s[1])); }
DI float sw16max(float a){ auto s=__builtin_amdgcn_permlane16_swap(__float_as_uint(a),__float_as_uint(a),false,false); return vmax(__uint_as_float(s[0]),__uint_as_float(s[1])); }
DI float wave_sum(float v){ v+=dpp<DXOR1>(v); v+=dpp<DXOR2>(v); v+=dpp<DHMIR>(v); v+=dpp<DROR8>(v); v=sw16add(v,v); v=sw32add(v,v); return v; }
DI void dma16(const void* gbase,u32 voff,u32 ldsb){
  u32 keep;
  asm volatile("s_mov_b32 %0, m0\n\ts_mov_b32 m0, %3\n\ts_nop 0\n\tglobal_load_lds_dwordx4 %1, %2 nt\n\ts_mov_b32 m0, %0" : "=&s"(keep) : "v"(voff),"s"(gbase),"s"(ldsb) : "memory");
}
DI constexpr int fsw(int row){ return ((row&3)<<2)|((row>>2)&3); }
DI bool flag_ok(v4u f,u64 want){ return (f.x==(u32)want)&(f.y==(u32)(want>>32))&(f.z==(u32)want)&(f.w==(u32)(want>>32)); }
DI void wait_t(u64 t0,int ticks){ while(rt()-t0<(u64)ticks) __builtin_amdgcn_s_sleep(1); }
DI void ln_row(v4u xr,int lane,int tq,lds8* dst){
  float v[8]={blo(xr.x),bhi(xr.x),blo(xr.y),bhi(xr.y),blo(xr.z),bhi(xr.z),blo(xr.w),bhi(xr.w)};
  const float mean=wave_sum(((v[0]+v[1])+(v[2]+v[3]))+((v[4]+v[5])+(v[6]+v[7])))*(1.f/DM);
  float qq=0.f;
  #pragma unroll
  for(int i=0;i<8;i++){ const float d=v[i]-mean; qq+=d*d; }
  const float var=wave_sum(qq)*(1.f/DM);
  const float mb=rbf(mean), isd=__builtin_amdgcn_rcpf(rbf(__builtin_amdgcn_sqrtf(rbf(rbf(var)+1e-5f))));
  float y[8];
  #pragma unroll
  for(int i=0;i<8;i++) y[i]=rbf(v[i]-mb)*isd;
  if((lane>>4)==tq) *(LAS v4u*)(dst+16*(lane&15))=v4u{pk2(y[0],y[1]),pk2(y[2],y[3]),pk2(y[4],y[5]),pk2(y[6],y[7])};
}

extern "C" __global__ void __launch_bounds__(512,2) mla_mlp_kernel(const u16* __restrict__ x,const u16* __restrict__ ckv,const u16* __restrict__ wq,const u16* __restrict__ wuk,
    const u16* __restrict__ wuv,const u16* __restrict__ wo,const u16* __restrict__ w1,const u16* __restrict__ b1,const u16* __restrict__ w2,const u16* __restrict__ b2,
    u16* __restrict__ out,unsigned char* __restrict__ ws){
  extern __shared__ __attribute__((aligned(16))) unsigned char smem[];
  lds8* L=(lds8*)smem;
  const u32 Lb=(u32)(size_t)L;
  const int tid=threadIdx.x, lane=tid&63, w=__builtin_amdgcn_readfirstlane(tid>>6);
  const int wg=blockIdx.x, xc=wg&7, j=wg>>3, jj=j&15;
  const bool isF=j<16;
  const int fi=xc*16+jj, th=fi>>2, tq=fi&3;
  const int g=lane>>4, q4=(lane&15)>>2, p4=lane&3, hh=lane>>5, l31=lane&31;
  const __amdgpu_buffer_rsrc_t rws=mkrs(ws,WS_END);
  const u64 t_start=rt();
  u64 probe_t=0;
  asm volatile("" :: "s"(x),"s"(ckv),"s"(wq),"s"(wuk),"s"(ws));
  const u32 rowoff=(u32)((128*tq+16*w+g)*8192+th*256);
#define DMA_T(p,dst,i) dma16(p,rowoff+(u32)(4*(i)*8192)+16*((lane&15)^(2*(g|(((i)>>1)<<2)))),Lb+(dst)+(16*w+4*(i))*256)
  const int troff=(8*g+q4)*256+(w^(q4|((g&1)<<2)))*32+p4*8;
  const int r0=4*g+q4;
  if(isF){
    const bool mlp = fi<FF/16;
    if(w==7){
      v2u e2;
      st128(rws,WS_QA+g*32768+th*1024+tq*256+(lane&15)*16,v4u{SENT,SENT,SENT,SENT});
      st128(rws,WS_QA+131072+g*32768+th*1024+tq*256+(lane&15)*16,v4u{SENT,SENT,SENT,SENT});
      asm volatile("global_load_dwordx2 %0, %1, %2" : "=v"(e2) : "v"(0), "s"(ws) : "memory");
      #pragma unroll
      for(int i=0;i<4;i++) dma16(wuk,rowoff+(u32)(4*i*8192)+16*((lane&15)^(4*i+g)),Lb+L_WUK+(16*w+4*i)*256);
      asm volatile("s_waitcnt vmcnt(4)" : "+v"(e2) :: "memory");
      const u64 wn=(((u64)e2.y<<32)|e2.x)+1; if(lane<8) __hip_atomic_store((gu64*)(ws+WS_FLAG1)+lane*128+fi,wn,RLX_AGENT);
      if(lane==0) *(LAS u64*)(L+L_EP)=wn;
      VMW(1);
    } else {
      #pragma unroll
      for(int i=0;i<4;i++) dma16(wuk,rowoff+(u32)(4*i*8192)+16*((lane&15)^(4*i+g)),Lb+L_WUK+(16*w+4*i)*256);
      if(w>=5){
        u64 pc; u32 back, size;
        asm volatile("s_getpc_b64 %0\n.Lmla_pc_anchor:\n\ts_mov_b32 %1, .Lmla_pc_anchor-mla_mlp_kernel\n\ts_mov_b32 %2, .Lmla_code_end-mla_mlp_kernel" : "=s"(pc),"=s"(back),"=s"(size));
        const u32 off=(u32)((j+16*(6-w))*1024+16*lane);
        if(off+16<=size){ v4u t_; asm volatile("global_load_dwordx4 %0, %1, %2\n\ts_waitcnt vmcnt(0)" : "=v"(t_) : "v"(off), "s"(pc-back) : "memory"); }
      }
      VMW(0);
    }
    v4u auk[4];
    #pragma unroll
    for(int k=0;k<4;k++){
      lds8* a=L+L_WUK+(16*w+(lane&15))*256+8*(g&1);
      const v2u lo=*(LAS v2u*)(a+16*((4*k+(g>>1))^(lane&15))), hi=*(LAS v2u*)(a+16*((4*k+2+(g>>1))^(lane&15)));
      auk[k]=v4u{lo.x,lo.y,hi.x,hi.y};
    }
    BAR();
    STAMP(1);
    const v2u ep=*(LAS v2u*)(L+L_EP);
    const u32 tag=__builtin_amdgcn_readfirstlane(ep.x);
    const u64 want=((u64)__builtin_amdgcn_readfirstlane(ep.y)<<32)|tag;
    wait_t(t_start,T_E1);
    {
      v4u qf;
      _Pragma("unroll 1") for(int sp=0;sp<SPIN_CAP;++sp){ FENCE();
        qf=ld128<AUX_SC1>(rws,WS_Q1+th*8192+w*1024+lane*16);
        if(__all((qf.y==tag)&(qf.w==tag))) break;
        __builtin_amdgcn_s_sleep(SLP_S);
      }
      const int t=4*(w&1)+g;
      *(LAS v2u*)(L+L_QL+(w>>1)*1024+(t>>1)*256+(lane&15)*16+(t&1)*8)=v2u{qf.x,qf.z};
    }
    BAR();
    STAMP(2);
    {
      v4u qb[4];
      #pragma unroll
      for(int k=0;k<4;k++) qb[k]=lrd128(L+L_QL+q4*1024+k*256+g*64+p4*16);
      SB();
      f32x4 acc={0,0,0,0};
      #pragma unroll
      for(int k=0;k<4;k++) acc=mfma16(auk[k],qb[k],acc);
      #pragma unroll
      for(int r=0;r<4;r++){ acc[r]+=dpp<DROR4>(acc[r]); acc[r]+=dpp<DROR8>(acc[r]); }
      const float SC=0.08838834764831845f*1.4426950408889634f;
      if((lane&15)<4) *(LAS v2u*)(L+L_QS+(lane&15)*256+(16*w+4*g)*2)=v2u{nos(pk2(acc[0]*SC,acc[1]*SC)),nos(pk2(acc[2]*SC,acc[3]*SC))};
    }
    BAR();
    if(w<2){ v4u v=lrd128(L+L_QS+lane*16); st128(rws,WS_QA+w*131072+g*32768+th*1024+tq*256+(lane&15)*16,v); }
    STAMP(3);
    u32 b1raw[4];
    #pragma unroll
    for(int r=0;r<4;r++) b1raw[r]=__builtin_amdgcn_raw_buffer_load_b16(mkrs(b1,FF*2),(16*fi+4*g+r)*2,0,0);
    u32 xraw=__builtin_amdgcn_raw_buffer_load_b16(mkrs(x,NB*DM*2),(p4*DM+4*fi+q4)*2,0,0);
    u32 b2raw=__builtin_amdgcn_raw_buffer_load_b16(mkrs(b2,DM*2),(4*fi+q4)*2,0,0);
    SB();
#define DMA_WO(i) dma16(wo,(u32)((th*128+8*(i)+w)*1024+16*(lane^(2*((w&3)|(((i)&1)<<2))))),Lb+L_WO+(8*(i)+w)*1024)
    wait_t(t_start,T_WO);
    #pragma unroll
    for(int i=0;i<10;i++){ if(i>=WO_1) VMW(WO_1-1); DMA_WO(i); }
    wait_t(t_start,T_TAIL);
    v4u xr;
    asm volatile("global_load_dwordx4 %0, %1, %2" : "=v"(xr) : "v"((w&3)*1024+16*lane), "s"(x) : "memory");
    const int thm=mlp?th:0, fim=mlp?fi:0;
    #pragma unroll
    for(int i=0;i<2;i++){ const int r=8*i+(lane>>3); dma16(w1,(u32)((128*tq+16*w+r)*(FF*2)+thm*128+16*((lane&7)^(2*(((r>>1)&1)|(i<<1))))),Lb+L_W1+(16*w+8*i)*128); }
    #pragma unroll
    for(int i=0;i<4;i++) DMA_T(wuv,L_WUV,i);
    #pragma unroll
    for(int i=0;i<2;i++){ const int r=8*i+w; dma16(w2,(u32)((16*fim+r)*1024+16*(lane^(2*(r&7)))),Lb+L_W2+r*1024); }
    v4u rwo[6];
    #pragma unroll
    for(int i=0;i<6;i++) asm volatile("global_load_dwordx4 %0, %1, %2 nt" : "=v"(rwo[i]) : "v"((th*128+80+8*i+w)*1024+16*lane), "s"(wo) : "memory");
    asm volatile("s_waitcnt vmcnt(12)" : "+v"(xr) :: "memory");
    STAMP(4);
    if(w<4) ln_row(xr,lane,tq,L+L_XN+w*320);
    BAR();
    if(mlp && w<4){
      v4u a1[4], bq[4];
      #pragma unroll
      for(int ks=0;ks<4;ks++){ lds8* a=L+L_W1+(32*ks+8*g+q4)*128+32*(w^((q4>>1)|((g&1)<<1)))+p4*8; a1[ks]=trd2(a,a+512); bq[ks]=lrd128(L+L_XN+p4*320+(32*ks+8*g)*2); }
      SB();
      f32x4 acc={0,0,0,0};
      #pragma unroll
      for(int ks=0;ks<4;ks++) acc=mfma16(a1[ks],bq[ks],acc);
      if((lane&15)<4){
        st128(rws,WS_HM+fi*2048+w*512+g*128+p4*16,v4u{__float_as_uint(acc[0]),tag,__float_as_uint(acc[1]),tag});
        st128(rws,WS_HM+fi*2048+w*512+g*128+64+p4*16,v4u{__float_as_uint(acc[2]),tag,__float_as_uint(acc[3]),tag}); }
    }
    VMW(6);
    BAR();
    v4u auv[4]; v2u aw2[4];
    #pragma unroll
    for(int k=0;k<4;k++){ lds8* a=L+L_WUV+k*8192+troff; auv[k]=trd2(a,a+1024); }
    #pragma unroll
    for(int t=0;t<4;t++) aw2[t]=trd(L+L_W2+r0*1024+(w>>1)*256+32*((4*(w&1)+t)^(r0&7))+p4*8);
    BAR();
    if(mlp && w==4){
      v4u h0,h1;
      _Pragma("unroll 1") for(int sp=0;sp<SPIN_CAP;++sp){ FENCE();
        h0=ld128<AUX_SC1>(rws,WS_HM+(th*4+q4)*2048+tq*512+g*128+p4*16);
        h1=ld128<AUX_SC1>(rws,WS_HM+(th*4+q4)*2048+tq*512+g*128+64+p4*16);
        if(__all((h0.y==tag)&(h0.w==tag)&(h1.y==tag)&(h1.w==tag))) break;
        __builtin_amdgcn_s_sleep(SLP);
      }
      float hv[4]={__uint_as_float(h0.x),__uint_as_float(h0.z),__uint_as_float(h1.x),__uint_as_float(h1.z)};
      #pragma unroll
      for(int r=0;r<4;r++){ float sv=hv[r]; sv+=dpp<DROR4>(sv); sv+=dpp<DROR8>(sv); float h=rbf(rbf(sv)+blo(b1raw[r])); h=h>0.f?h:0.f; hv[r]=h*h; }
      *(LAS v2u*)(L+L_H2+lane*8)=v2u{pk2(hv[0],hv[1]),pk2(hv[2],hv[3])};
    }
    asm volatile("s_waitcnt vmcnt(0)" : "+v"(rwo[0]),"+v"(rwo[1]),"+v"(rwo[2]),"+v"(rwo[3]),"+v"(rwo[4]),"+v"(rwo[5]) :: "memory");
    #pragma unroll
    for(int i=0;i<6;i++) *(LAS v4u*)(L+L_WO+(80+8*i+w)*1024+16*(lane^(2*((w&3)|((i&1)<<2)))))=rwo[i];
    v4u awo[4][4];
#define LOAD_AWO(ks) _Pragma("unroll") for(int t=0;t<4;t++){ lds8* a=L+L_WO+(32*(ks)+8*g+q4)*1024+(w>>1)*256+p4*8+32*((4*(w&1)+t)^(q4|((g&1)<<2))); awo[ks][t]=trd2(a,a+4096); }
    LOAD_AWO(0); LOAD_AWO(1);
    SB();
    STAMP(5);
    BAR();
    asm volatile("" : "+v"(xraw), "+v"(b2raw));
    const float xb_res=blo(xraw)+blo(b2raw);
    LOAD_AWO(2); LOAD_AWO(3);
    f32x4 oacc[4];
    #pragma unroll
    for(int t=0;t<4;t++) oacc[t]=f32x4{0,0,0,0};
    if(mlp){
      const v2u h2=*(LAS v2u*)(L+L_H2+lane*8);
      #pragma unroll
      for(int t=0;t<4;t++) oacc[t]=mfma16(v4u{aw2[t].x,aw2[t].y,0,0},v4u{h2.x,h2.y,0,0},oacc[t]);
    }
    SB();
    STAMP(6);
    if(w==0){
      wait_t(t_start,T_GUN);
      v4u v0,v1;
      const u32 tagh=(u32)(want>>32)^0x5A5A5A5Au;
      _Pragma("unroll 1") for(int sp=0;sp<SPIN_CAP;++sp){ FENCE();
        v0=ld128<AUX_SC1>(rws,WS_LSE+(th>>2)*8192+lane*64+(th&3)*16);
        v1=ld128<AUX_SC1>(rws,WS_LSE+(th>>2)*8192+4096+lane*64+(th&3)*16);
        if(__all((v0.y==tag)&(v1.y==tag)&(v0.w==tagh)&(v1.w==tagh)&(v0.x==v0.z)&(v1.x==v1.z))) break; __builtin_amdgcn_s_sleep(SLP_S);
      }
      STAMP(7);
      *(LAS u32*)(L+L_WG+hh*128+(l31&7)*16+(l31>>3)*4)=v0.x;
      *(LAS u32*)(L+L_WG+256+hh*128+(l31&7)*16+(l31>>3)*4)=v1.x;
    }
    BAR();
    {
      const int b=w>>1, half=w&1, slo=lane>>3, q=lane&7;
      const int po_off=WS_PO+(fi*128+b*32+slo)*256+half*128+q*16;
      v4u po[4];
      #pragma unroll
      for(int i=0;i<4;i++) po[i]=ld128<AUX_SC1>(rws,po_off+i*2048);
      SB();
      const f32x4 lf=*(LAS f32x4*)(L+L_WG+b*128+slo*16);
      float M=vmax(vmax3(lf[0],lf[1],lf[2]),lf[3]);
      M=vmax(M,dpp<DROR8>(M)); M=sw16max(M); M=sw32max(M);
      f32x4 e;
      #pragma unroll
      for(int i=0;i<4;i++) e[i]=__builtin_amdgcn_exp2f(lf[i]-M);
      float Ls=(e[0]+e[1])+(e[2]+e[3]);
      Ls+=dpp<DROR8>(Ls); Ls=sw16add(Ls,Ls); Ls=sw32add(Ls,Ls);
      e*=__builtin_amdgcn_rcpf(Ls);
      SB();
      STAMP(8);
      float a[8];
      #pragma unroll
      for(int k=0;k<8;k++) a[k]=0.f;
      #pragma unroll
      for(int i=0;i<4;i++){
        a[0]+=e[i]*blo(po[i].x); a[1]+=e[i]*bhi(po[i].x); a[2]+=e[i]*blo(po[i].y); a[3]+=e[i]*bhi(po[i].y);
        a[4]+=e[i]*blo(po[i].z); a[5]+=e[i]*bhi(po[i].z); a[6]+=e[i]*blo(po[i].w); a[7]+=e[i]*bhi(po[i].w);
      }
      float c4[4], c2[2];
      #pragma unroll
      for(int k=0;k<4;k++) c4[k]=sw32add(a[k],a[k+4]);
      c2[0]=sw16add(c4[0],c4[2]); c2[1]=sw16add(c4[1],c4[3]);
      const bool b3=(lane>>3)&1;
      const float keep=b3?c2[1]:c2[0], send=b3?c2[0]:c2[1];
      const float r=keep+dpp<DROR8>(send);
      const int col=8*q+4*hh+2*((lane>>4)&1)+(b3?1:0);
      *(LAS u16*)(L+L_CL+b*320+(half*64+col)*2)=(u16)pk2(r,0.f);
    }
    BAR();
    STAMP(9);
    {
      v4u bc[4];
      #pragma unroll
      for(int k=0;k<4;k++) bc[k]=lrd128(L+L_CL+p4*320+(32*k+8*g)*2);
      SB();
      f32x4 acc={0,0,0,0};
      #pragma unroll
      for(int k=0;k<4;k++) acc=mfma16(auv[k],bc[k],acc);
      if((lane&15)<4) *(LAS v2u*)(L+L_CX+p4*320+(16*w+4*g)*2)=v2u{pk2(acc[0],acc[1]),pk2(acc[2],acc[3])};
    }
    BAR();
    {
      v4u bc[4];
      #pragma unroll
      for(int ks=0;ks<4;ks++) bc[ks]=lrd128(L+L_CX+p4*320+(32*ks+8*g)*2);
      SB();
      #pragma unroll
      for(int ks=0;ks<4;ks++)
        #pragma unroll
        for(int t=0;t<4;t++) oacc[t]=mfma16(awo[ks][t],bc[ks],oacc[t]);
      f32x4 v;
      #pragma unroll
      for(int r=0;r<4;r++){ const float lo=(q4&1)?oacc[1][r]:oacc[0][r], hi=(q4&1)?oacc[3][r]:oacc[2][r]; v[r]=(q4&2)?hi:lo; }
      st128(rws,WS_OUT+(16*w+4*q4+g)*8192+fi*64+p4*16,v4u{pk2(v[0],v[1]),tag,pk2(v[2],v[3]),tag});
    }
    STAMP(10);
    { const u64 t1=rt(); wait_t(t1,D_E4); }
    v4u pv;
    _Pragma("unroll 1") for(int sp=0;sp<SPIN_CAP;++sp){ FENCE();
      pv=ld128<AUX_SC1>(rws,WS_OUT+fi*8192+tid*16);
      if(__all((pv.y==tag)&(pv.w==tag))) break;
      __builtin_amdgcn_s_sleep(SLP_S);
    }
    STAMP(11);
    const float u0=sw32add(blo(pv.x),blo(pv.z)), u1=sw32add(bhi(pv.x),bhi(pv.z));
    float v=sw16add(u0,u1);
    v+=dpp<DROR4>(v); v+=dpp<DROR8>(v);
    if((lane&12)==0) *(LAS float*)(L+L_RED+w*64+(g*4+p4)*4)=v;
    BAR();
    if(tid<16){
      float a=0.f;
      #pragma unroll
      for(int ww=0;ww<8;ww++) a+=*(LAS float*)(L+L_RED+ww*64+tid*4);
      out[(tid&3)*DM+4*fi+(tid>>2)]=(u16)pk2(xb_res+a,0.f);
    }
    if(wg==0 && tid==0) __hip_atomic_store((gu64*)(ws+WS_EPOCH),want,RLX_AGENT);
    asm volatile(".Lmla_code_end:");
  } else {
    const int ab=fi>>5, as=fi&31;
    const int fl=fsw(l31);
    const int blk=(lane>>4)&1, sgm=w>>1;
    const u16* gb=ckv+((size_t)ab*SK+as*128+32*(w&3))*DC;
#define DMA_CKV(r) dma16(gb+(r)*DC,(u32)((lane*16)^(16*fsw(r))),Lb+L_CKV+(32*w+(r))*1024)
    #pragma unroll
    for(int i=0;i<4;i++) DMA_T(wq,L_AWQ,i);
    if(w<4){
      v2u e2;
      asm volatile("global_load_dwordx2 %0, %1, %2" : "=v"(e2) : "v"(0), "s"(ws) : "memory");
      wait_t(t_start,T_CKV0);
      #pragma unroll
      for(int r=0;r<INFL0;r++) DMA_CKV(r);
      asm volatile("s_waitcnt vmcnt(%1)" : "+v"(e2) : "i"(INFL0) : "memory");
      if(w==0 && lane==0) *(LAS u64*)(L+L_AEP)=(((u64)e2.y<<32)|e2.x)+1;
    } else {
      v4u xr;
      asm volatile("global_load_dwordx4 %0, %1, %2\n\ts_waitcnt vmcnt(0)" : "=v"(xr) : "v"((w&3)*1024+16*lane), "s"(x) : "memory");
      ln_row(xr,lane,tq,L+L_AXN+(w-4)*320);
    }
    BAR();
    ESTAMP(2);
    u32 tag; u64 want;
    {
      v4u aq[4], bq[4];
      #pragma unroll
      for(int ks=0;ks<4;ks++){ lds8* a=L+L_AWQ+ks*8192+troff; aq[ks]=trd2(a,a+1024); bq[ks]=lrd128(L+L_AXN+p4*320+(32*ks+8*g)*2); }
      const v2u e=*(LAS v2u*)(L+L_AEP);
      SB();
      f32x4 acc={0,0,0,0};
      #pragma unroll
      for(int ks=0;ks<4;ks++) acc=mfma16(aq[ks],bq[ks],acc);
      tag=__builtin_amdgcn_readfirstlane(e.x);
      if((lane&15)<4) st128(rws,WS_Q1+fi*2048+w*256+g*64+p4*16,v4u{pk2(acc[0],acc[1]),tag,pk2(acc[2],acc[3]),tag});
      want=((u64)__builtin_amdgcn_readfirstlane(e.y)<<32)|tag;
    }
    ESTAMP(3);
    BAR();
    v4u vf[8][2];
#define LOAD_VF() do{ _Pragma("unroll") for(int ks=0;ks<8;ks++) _Pragma("unroll") for(int t=0;t<2;t++){ \
        const int ch=8*(w&1)+4*t+2*blk+(p4>>1); \
        lds8* a0=L+L_CKV+(16*ks+8*hh+q4)*1024+256*sgm+16*(ch^((q4<<2)|(2*hh)))+8*(p4&1); \
        lds8* a1=L+L_CKV+(16*ks+8*hh+4+q4)*1024+256*sgm+16*(ch^((q4<<2)|(2*hh+1)))+8*(p4&1); \
        vf[ks][t]=trd2(a0,a1); } SB(); }while(0)
#define GATHER_QA() do{ v4u qa[4]; const int qo=WS_QA+(xc&1)*131072+ab*32768+4*w*1024+16*lane; \
      _Pragma("unroll") for(int i=0;i<4;i++) qa[i]=ld128<AUX_SC1>(rws,qo+i*1024); \
      _Pragma("unroll 1") for(int sp=0;sp<SPIN_CAP;++sp){ u32 bad=0; \
        _Pragma("unroll") for(int i=0;i<4;i++) bad|=(__all(ok4(qa[i]))?0u:1u)<<i; \
        if(!bad) break; \
        __builtin_amdgcn_s_sleep(SLP_S); FENCE(); \
        _Pragma("unroll") for(int i=0;i<4;i++) if((bad>>i)&1) qa[i]=ld128<AUX_SC1>(rws,qo+i*1024); } \
      _Pragma("unroll") for(int i=0;i<4;i++) *(LAS v4u*)(L+L_QAS+(4*w+i)*1024+((lane*16)^(16*((i<<2)|(w&3)))))=qa[i]; }while(0)
    float mx, lse;
    if(w<4){
      #pragma unroll
      for(int r=INFL0;r<32;r++){
        if(r>=CKV_INFL) VMW(CKV_INFL-1);
        DMA_CKV(r);
      }
      BAR();
      GATHER_QA();
      VMW(0);
      ESTAMP(0);
      v4u af[32];
      #pragma unroll
      for(int k=0;k<32;k++) af[k]=lrd128(L+L_CKV+(32*w+l31)*1024+256*(k>>3)+16*((2*(k&7)+hh)^fl));
      BAR();
      ESTAMP(1);
      f32x16 sacc;
      #pragma unroll
      for(int i=0;i<16;i++) sacc[i]=0.f;
      v4u bf[12];
      #pragma unroll
      for(int k=0;k<12;k++) bf[k]=lrd128(L+L_QAS+l31*1024+256*(k>>3)+16*((2*(k&7)+hh)^fl));
      SB();
      #pragma unroll
      for(int k=0;k<32;k++){
        sacc=mfma32(af[k],bf[k%12],sacc);
        if(k+12<32) bf[k%12]=lrd128(L+L_QAS+l31*1024+256*((k+12)>>3)+16*((2*((k+12)&7)+hh)^fl));
        SB();
      }
      float mloc=vmax3(vmax3(vmax3(sacc[0],sacc[1],sacc[2]),vmax3(sacc[3],sacc[4],sacc[5]),vmax3(sacc[6],sacc[7],sacc[8])),
                       vmax3(vmax3(sacc[9],sacc[10],sacc[11]),vmax3(sacc[12],sacc[13],sacc[14]),sacc[15]),sacc[15]);
      mloc=sw32max(mloc);
      BAR();
      STAMP(4);
      if(lane<32) *(LAS float*)(L+L_MX+w*128+lane*4)=mloc;
      BAR();
      mx=vmax(vmax3(*(LAS float*)(L+L_MX+l31*4),*(LAS float*)(L+L_MX+128+l31*4),*(LAS float*)(L+L_MX+256+l31*4)),*(LAS float*)(L+L_MX+384+l31*4));
      float ls=0.f;
      #pragma unroll
      for(int a=0;a<4;a++){
        float p0=__builtin_amdgcn_exp2f(sacc[4*a]-mx), p1=__builtin_amdgcn_exp2f(sacc[4*a+1]-mx), p2=__builtin_amdgcn_exp2f(sacc[4*a+2]-mx), p3=__builtin_amdgcn_exp2f(sacc[4*a+3]-mx);
        const u32 u0=pk2(p0,p1), u1=pk2(p2,p3);
        ls+=(p0+p1)+(p2+p3);
        *(LAS v2u*)(L+L_P+l31*256+16*((4*w+a)^fl)+8*hh)=v2u{u0,u1};
      }
      ls=sw32add(ls,ls);
      if(lane<32) *(LAS float*)(L+L_LS+w*128+lane*4)=ls;
      BAR();
      STAMP(5);
      LOAD_VF();
    } else {
      const int wv=w-4;
      wait_t(t_start,T_FLAG1);
      _Pragma("unroll 1") for(int sp=0;sp<SPIN_CAP;++sp){ FENCE();
        if(__all(flag_ok(ld128<AUX_SC1>(rws,WS_FLAG1+xc*1024+wv*256+(lane&15)*16),want))) break;
        __builtin_amdgcn_s_sleep(SLP);
      }
      ESTAMP(0);
      wait_t(t_start,T_GATHER);
      BAR();
      GATHER_QA();
      ESTAMP(1);
      BAR();
      LOAD_VF();
      BAR();
      BAR();
      mx=0.f;
      BAR();
    }
    {
      v4u pb[8];
      #pragma unroll
      for(int ks=0;ks<8;ks++) pb[ks]=lrd128(L+L_P+l31*256+16*((2*ks+hh)^fl));
      const float lsum=(*(LAS float*)(L+L_LS+l31*4)+*(LAS float*)(L+L_LS+128+l31*4))+(*(LAS float*)(L+L_LS+256+l31*4)+*(LAS float*)(L+L_LS+384+l31*4));
      SB();
      f32x16 o[2];
      #pragma unroll
      for(int i=0;i<16;i++){ o[0][i]=0.f; o[1][i]=0.f; }
      #pragma unroll
      for(int t=0;t<2;t++)
        #pragma unroll
        for(int ks=0;ks<8;ks++) o[t]=mfma32(vf[ks][t],pb[ks],o[t]);
      const float inv=__builtin_amdgcn_rcpf(lsum);
      lse=mx+__builtin_amdgcn_logf(lsum);
      STAMP(6);
      #pragma unroll
      for(int t=0;t<2;t++)
        #pragma unroll
        for(int a=0;a<4;a++)
          *(LAS v2u*)(L+L_CKV+l31*1024+256*sgm+16*((8*(w&1)+4*t+a)^fl)+8*hh)=v2u{pk2(o[t][4*a]*inv,o[t][4*a+1]*inv),pk2(o[t][4*a+2]*inv,o[t][4*a+3]*inv)};
      v4u ov[4];
      #pragma unroll
      for(int i=0;i<4;i++){ const int head=8*i+(lane>>3); ov[i]=lrd128(L+L_CKV+head*1024+256*sgm+16*((8*(w&1)+(lane&7))^fsw(head))); }
      SB();
      #pragma unroll
      for(int i=0;i<4;i++){ const int head=8*i+(lane>>3); st128(rws,WS_PO+((head*4+sgm)*128+ab*32+as)*256+(w&1)*128+(lane&7)*16,ov[i]); }
    }
    STAMP(7);
    VMW(0);
    BAR();
    if(w==0 && lane<32) st128(rws,WS_LSE+(lane>>2)*8192+fi*64+(lane&3)*16,v4u{__float_as_uint(lse),tag,__float_as_uint(lse),(u32)(want>>32)^0x5A5A5A5Au});
    STAMP(11);
  }
  STAMP(15);
  { const u64 lim=(probe_t>3000?3000:probe_t)*10; while(rt()-t_start<lim) __builtin_amdgcn_s_sleep(2); }
}

extern "C" void kernel_launch(void* const* d_in,const int* in_sizes,int n_in,void* d_out,int out_size,void* d_ws,size_t ws_size,hipStream_t stream){
  (void)hipFuncSetAttribute((const void*)mla_mlp_kernel,hipFuncAttributeMaxDynamicSharedMemorySize,LDS_BYTES);
  mla_mlp_kernel<<<256,512,LDS_BYTES,stream>>>((const u16*)d_in[0],(const u16*)d_in[1],(const u16*)d_in[2],(const u16*)d_in[3],(const u16*)d_in[4],(const u16*)d_in[5],
     (const u16*)d_in[7],(const u16*)d_in[8],(const u16*)d_in[9],(const u16*)d_in[10],(u16*)d_out,(unsigned char*)d_ws);
}
```

```cpp
#include <hip/hip_runtime.h>
#include <cstring>
#define D_G 200
typedef unsigned short u16; typedef unsigned u32; typedef unsigned long long u64;
#define GAS __attribute__((address_space(1)))
#define LAS __attribute__((address_space(3)))
typedef u32 v4u __attribute__((ext_vector_type(4)));
typedef u32 v2u __attribute__((ext_vector_type(2)));
typedef float f32x2 __attribute__((ext_vector_type(2)));
typedef float f32x4 __attribute__((ext_vector_type(4)));
typedef float f32x16 __attribute__((ext_vector_type(16)));
typedef __bf16 bf16x8 __attribute__((ext_vector_type(8)));
typedef __bf16 bf16x2 __attribute__((ext_vector_type(2)));
typedef short s16x4 __attribute__((ext_vector_type(4)));
typedef GAS u64 gu64;
typedef LAS unsigned char lds8;
#define RLX_AGENT __ATOMIC_RELAXED, __HIP_MEMORY_SCOPE_AGENT
#define DI __device__ __forceinline__

constexpr int NB=4, NH=32, SK=4096, HD=128, DM=512, DC=512, FF=1792;
constexpr int WS_EPOCH=0, WS_FLAG1=8192, WS_Q1=32768, WS_HM=WS_Q1+262144, WS_QA=WS_HM+262144, WS_LSE=WS_QA+262144, WS_OUT=WS_LSE+65536,
              WS_PO=WS_OUT+(2<<20), WS_END=WS_PO+(5<<20), WS_STAMP=WS_END;
constexpr int L_CKV=0, L_QAS=131072, L_P=L_QAS, L_MX=L_QAS+8192, L_LS=L_QAS+8704, L_AWQ=L_QAS, L_AXN=126*1024, L_AEP=L_AXN+1536;
constexpr int L_WUK=0, L_WO=0, L_W2=81920, L_WUV=98304, L_W1=131072, L_XN=147456, L_QS=148736, L_CL=149760, L_H2=151040, L_RED=151552, L_EP=152064, L_QL=152576, L_WG=156672, L_CX=157184, L_STF=158720;
constexpr int LDS_BYTES=163840;
constexpr u32 SENT=0xFFFFFFFFu;
constexpr int SPIN_CAP=1<<20;
constexpr int AUX_NT=2, AUX_SC1=16;
#define PO_STRIDE 32768
#define OUT_STRIDE 8192
#define CKV_INFL 12
#define INFL0 12
#define T_CKV0 120
#define T_E1 150
#define T_FLAG1 240
#define T_GATHER 400
#define WO_1 4
#define T_WO 0
#define T_TAIL 460
#define T_GUN 620
#define D_E4 50
#define SLP 4
#define SLP_S 1
static_assert(CKV_INFL<=30,"ring");
#define STAMP(k) do{}while(0)
#define ESTAMP(i) do{}while(0)

DI u64 rt(){ return __builtin_amdgcn_s_memrealtime(); }
DI u64 ct(){ u64 t; asm volatile("s_memtime %0\n\ts_waitcnt lgkmcnt(0)" : "=s"(t) :: "memory"); return t; }
DI __amdgpu_buffer_rsrc_t mkrs(const void* p, int bytes){ return __builtin_amdgcn_make_buffer_rsrc((void*)p,0,bytes,0x00020000); }
template<int AUX> DI v4u ld128(__amdgpu_buffer_rsrc_t rs,int off){ return __builtin_amdgcn_raw_buffer_load_b128(rs,off,0,AUX); }
template<int AUX> DI u32 ld32(__amdgpu_buffer_rsrc_t rs,int off){ return __builtin_amdgcn_raw_buffer_load_b32(rs,off,0,AUX); }
DI void st128(__amdgpu_buffer_rsrc_t rs,int off,v4u v){ __builtin_amdgcn_raw_buffer_store_b128(v,rs,off,0,AUX_SC1); }
DI void st32(__amdgpu_buffer_rsrc_t rs,int off,u32 v){ __builtin_amdgcn_raw_buffer_store_b32(v,rs,off,0,AUX_SC1); }
DI f32x4 mfma16(v4u a,v4u b,f32x4 c){ return __builtin_amdgcn_mfma_f32_16x16x32_bf16(__builtin_bit_cast(bf16x8,a),__builtin_bit_cast(bf16x8,b),c,0,0,0); }
DI f32x16 mfma32(v4u a,v4u b,f32x16 c){ return __builtin_amdgcn_mfma_f32_32x32x16_bf16(__builtin_bit_cast(bf16x8,a),__builtin_bit_cast(bf16x8,b),c,0,0,0); }
DI v2u trd(lds8* p){ return __builtin_bit_cast(v2u,__builtin_amdgcn_ds_read_tr16_b64_v4i16((LAS s16x4*)p)); }
DI v4u trd2(lds8* p0,lds8* p1){ v2u a=trd(p0), b=trd(p1); return v4u{a.x,a.y,b.x,b.y}; }
DI v4u lrd128(lds8* p){ return *(LAS v4u*)p; }
DI u32 pk2(float lo,float hi){ f32x2 f={lo,hi}; return __builtin_bit_cast(u32,__builtin_convertvector(f,bf16x2)); }
DI float rbf(float x){ return __uint_as_float(pk2(x,0.f)<<16); }
DI float vmax(float a,float b){ return fmaxf(a,b); }
DI float vmax3(float a,float b,float c){ return fmaxf(fmaxf(a,b),c); }
DI float blo(u32 w){ return __uint_as_float(w<<16); }
DI float bhi(u32 w){ return __uint_as_float(w&0xFFFF0000u); }
DI u32 nos(u32 w){ return w<0xFFFFFFFEu? w:0xFFFFFFFEu; }
DI v4u nos4(v4u v){ return v4u{nos(v.x),nos(v.y),nos(v.z),nos(v.w)}; }
DI bool ok4(v4u v){ return (v.x!=SENT)&(v.y!=SENT)&(v.z!=SENT)&(v.w!=SENT); }
#define BAR() asm volatile("s_waitcnt lgkmcnt(0)\n\ts_barrier" ::: "memory")
#define VMW(n) asm volatile("s_waitcnt vmcnt(%0)" :: "i"(n) : "memory")
#define FENCE() asm volatile("" ::: "memory")
#define SB() __builtin_amdgcn_sched_barrier(0)
template<int CTRL> DI float dpp(float x){ return __builtin_bit_cast(float,__builtin_amdgcn_mov_dpp(__builtin_bit_cast(int,x),CTRL,0xf,0xf,true)); }
constexpr int DXOR1=0xB1, DXOR2=0x4E, DHMIR=0x141, DROR8=0x128, DROR4=0x124;
DI float sw32add(float a,float b){ auto s=__builtin_amdgcn_permlane32_swap(__float_as_uint(a),__float_as_uint(b),false,false); return __uint_as_float(s[0])+__uint_as_float(s[1]); }
DI float sw16add(float a,float b){ auto s=__builtin_amdgcn_permlane16_swap(__float_as_uint(a),__float_as_uint(b),false,false); return __uint_as_float(s[0])+__uint_as_float(s[1]); }
DI float sw32max(float a){ auto s=__builtin_amdgcn_permlane32_swap(__float_as_uint(a),__float_as_uint(a),false,false); return vmax(__uint_as_float(s[0]),__uint_as_float(s[1])); }
DI float sw16max(float a){ auto s=__builtin_amdgcn_permlane16_swap(__float_as_uint(a),__float_as_uint(a),false,false); return vmax(__uint_as_float(s[0]),__uint_as_float(s[1])); }
DI float wave_sum(float v){ v+=dpp<DXOR1>(v); v+=dpp<DXOR2>(v); v+=dpp<DHMIR>(v); v+=dpp<DROR8>(v); v=sw16add(v,v); v=sw32add(v,v); return v; }
DI void dma16(const void* gbase,u32 voff,u32 ldsb){
  u32 keep;
  asm volatile("s_mov_b32 %0, m0\n\ts_mov_b32 m0, %3\n\ts_nop 0\n\tglobal_load_lds_dwordx4 %1, %2 nt\n\ts_mov_b32 m0, %0" : "=&s"(keep) : "v"(voff),"s"(gbase),"s"(ldsb) : "memory");
}
DI void dma16c(const void* gbase,u32 voff,u32 ldsb){
  u32 keep;
  asm volatile("s_mov_b32 %0, m0\n\ts_mov_b32 m0, %3\n\ts_nop 0\n\tglobal_load_lds_dwordx4 %1, %2\n\ts_mov_b32 m0, %0" : "=&s"(keep) : "v"(voff),"s"(gbase),"s"(ldsb) : "memory");
}
DI constexpr int fsw(int row){ return ((row&3)<<2)|((row>>2)&3); }
DI bool flag_ok(v4u f,u64 want){ return (f.x==(u32)want)&(f.y==(u32)(want>>32))&(f.z==(u32)want)&(f.w==(u32)(want>>32)); }
DI void wait_t(u64 t0,int ticks){ while(rt()-t0<(u64)ticks) __builtin_amdgcn_s_sleep(1); }
DI void ln_row(v4u xr,int lane,int tq,lds8* dst){
  float v[8]={blo(xr.x),bhi(xr.x),blo(xr.y),bhi(xr.y),blo(xr.z),bhi(xr.z),blo(xr.w),bhi(xr.w)};
  const float mean=wave_sum(((v[0]+v[1])+(v[2]+v[3]))+((v[4]+v[5])+(v[6]+v[7])))*(1.f/DM);
  float qq=0.f;
  #pragma unroll
  for(int i=0;i<8;i++){ const float d=v[i]-mean; qq+=d*d; }
  const float var=wave_sum(qq)*(1.f/DM);
  const float mb=rbf(mean), isd=__builtin_amdgcn_rcpf(rbf(__builtin_amdgcn_sqrtf(rbf(rbf(var)+1e-5f))));
  float y[8];
  #pragma unroll
  for(int i=0;i<8;i++) y[i]=rbf(v[i]-mb)*isd;
  if((lane>>4)==tq) *(LAS v4u*)(dst+16*(lane&15))=v4u{pk2(y[0],y[1]),pk2(y[2],y[3]),pk2(y[4],y[5]),pk2(y[6],y[7])};
}

extern "C" __global__ void __launch_bounds__(512,2) mla_mlp_kernel(const u16* __restrict__ x,const u16* __restrict__ ckv,const u16* __restrict__ wq,const u16* __restrict__ wuk,
    const u16* __restrict__ wuv,const u16* __restrict__ wo,const u16* __restrict__ w1,const u16* __restrict__ b1,const u16* __restrict__ w2,const u16* __restrict__ b2,
    u16* __restrict__ out,unsigned char* __restrict__ ws){
  extern __shared__ __attribute__((aligned(16))) unsigned char smem[];
  lds8* L=(lds8*)smem;
  const u32 Lb=(u32)(size_t)L;
  const int tid=threadIdx.x, lane=tid&63, w=__builtin_amdgcn_readfirstlane(tid>>6);
  const int wg=blockIdx.x, xc=wg&7, j=wg>>3, jj=j&15;
  const bool isF=j<16;
  const int fi=xc*16+jj, th=fi>>2, tq=fi&3;
  const int g=lane>>4, q4=(lane&15)>>2, p4=lane&3, hh=lane>>5, l31=lane&31;
  const __amdgpu_buffer_rsrc_t rws=mkrs(ws,WS_END);
  const u64 t_start=rt();
  asm volatile("" :: "s"(x),"s"(ckv),"s"(wq),"s"(wuk),"s"(ws));
  const u32 rowoff=(u32)((128*tq+16*w+g)*8192+th*256);
#define DMA_T(p,dst,i) dma16(p,rowoff+(u32)(4*(i)*8192)+16*((lane&15)^(2*(g|(((i)>>1)<<2)))),Lb+(dst)+(16*w+4*(i))*256)
  const int troff=(8*g+q4)*256+(w^(q4|((g&1)<<2)))*32+p4*8;
  const int r0=4*g+q4;
  if(isF){
    const bool mlp = fi<FF/16;
    if(w==7){
      v2u e2;
      st128(rws,WS_QA+g*32768+th*1024+tq*256+(lane&15)*16,v4u{SENT,SENT,SENT,SENT});
      st128(rws,WS_QA+131072+g*32768+th*1024+tq*256+(lane&15)*16,v4u{SENT,SENT,SENT,SENT});
      asm volatile("global_load_dwordx2 %0, %1, %2" : "=v"(e2) : "v"(0), "s"(ws) : "memory");
      #pragma unroll
      for(int i=0;i<4;i++) dma16(wuk,rowoff+(u32)(4*i*8192)+16*((lane&15)^(4*i+g)),Lb+L_WUK+(16*w+4*i)*256);
      asm volatile("s_waitcnt vmcnt(4)" : "+v"(e2) :: "memory");
      const u64 wn=(((u64)e2.y<<32)|e2.x)+1; if(lane<8) __hip_atomic_store((gu64*)(ws+WS_FLAG1)+lane*128+fi,wn,RLX_AGENT);
      if(lane==0) *(LAS u64*)(L+L_EP)=wn;
      VMW(1);
    } else {
      #pragma unroll
      for(int i=0;i<4;i++) dma16(wuk,rowoff+(u32)(4*i*8192)+16*((lane&15)^(4*i+g)),Lb+L_WUK+(16*w+4*i)*256);
      if(w>=5){
        u64 pc; u32 back, size;
        asm volatile("s_getpc_b64 %0\n.Lmla_pc_anchor:\n\ts_mov_b32 %1, .Lmla_pc_anchor-mla_mlp_kernel\n\ts_mov_b32 %2, .Lmla_code_end-mla_mlp_kernel" : "=s"(pc),"=s"(back),"=s"(size));
        const u32 off=(u32)((j+16*(6-w))*1024+16*lane);
        if(off+16<=size){ v4u t_; asm volatile("global_load_dwordx4 %0, %1, %2\n\ts_waitcnt vmcnt(0)" : "=v"(t_) : "v"(off), "s"(pc-back) : "memory"); }
      }
      VMW(0);
    }
    v4u auk[4];
    #pragma unroll
    for(int k=0;k<4;k++){
      lds8* a=L+L_WUK+(16*w+(lane&15))*256+8*(g&1);
      const v2u lo=*(LAS v2u*)(a+16*((4*k+(g>>1))^(lane&15))), hi=*(LAS v2u*)(a+16*((4*k+2+(g>>1))^(lane&15)));
      auk[k]=v4u{lo.x,lo.y,hi.x,hi.y};
    }
    BAR();
    STAMP(1);
    const v2u ep=*(LAS v2u*)(L+L_EP);
    const u32 tag=__builtin_amdgcn_readfirstlane(ep.x);
    const u64 want=((u64)__builtin_amdgcn_readfirstlane(ep.y)<<32)|tag;
    wait_t(t_start,T_E1);
    {
      v4u qf;
      _Pragma("unroll 1") for(int sp=0;sp<SPIN_CAP;++sp){ FENCE();
        qf=ld128<AUX_SC1>(rws,WS_Q1+th*8192+w*1024+lane*16);
        if(__all((qf.y==tag)&(qf.w==tag))) break;
        __builtin_amdgcn_s_sleep(SLP_S);
      }
      const int t=4*(w&1)+g;
      *(LAS v2u*)(L+L_QL+(w>>1)*1024+(t>>1)*256+(lane&15)*16+(t&1)*8)=v2u{qf.x,qf.z};
    }
    BAR();
    STAMP(2);
    {
      v4u qb[4];
      #pragma unroll
      for(int k=0;k<4;k++) qb[k]=lrd128(L+L_QL+q4*1024+k*256+g*64+p4*16);
      SB();
      f32x4 acc={0,0,0,0};
      #pragma unroll
      for(int k=0;k<4;k++) acc=mfma16(auk[k],qb[k],acc);
      #pragma unroll
      for(int r=0;r<4;r++){ acc[r]+=dpp<DROR4>(acc[r]); acc[r]+=dpp<DROR8>(acc[r]); }
      const float SC=0.08838834764831845f*1.4426950408889634f;
      if((lane&15)<4) *(LAS v2u*)(L+L_QS+(lane&15)*256+(16*w+4*g)*2)=v2u{nos(pk2(acc[0]*SC,acc[1]*SC)),nos(pk2(acc[2]*SC,acc[3]*SC))};
    }
    BAR();
    if(w<2){ v4u v=lrd128(L+L_QS+lane*16); st128(rws,WS_QA+w*131072+g*32768+th*1024+tq*256+(lane&15)*16,v); }
    STAMP(3);
    u32 b1raw[4];
    #pragma unroll
    for(int r=0;r<4;r++) b1raw[r]=__builtin_amdgcn_raw_buffer_load_b16(mkrs(b1,FF*2),(16*fi+4*g+r)*2,0,0);
    u32 xraw=__builtin_amdgcn_raw_buffer_load_b16(mkrs(x,NB*DM*2),(p4*DM+4*fi+q4)*2,0,0);
    u32 b2raw=__builtin_amdgcn_raw_buffer_load_b16(mkrs(b2,DM*2),(4*fi+q4)*2,0,0);
    SB();
#define dma_wo dma16
#define WO_NT " nt"
#define DMA_WO(i) dma_wo(wo,(u32)((th*128+8*(i)+w)*1024+16*(lane^(2*((w&3)|(((i)&1)<<2))))),Lb+L_WO+(8*(i)+w)*1024)
    wait_t(t_start,T_WO);
    #pragma unroll
    for(int i=0;i<10;i++){ if(i>=WO_1) VMW(WO_1-1); DMA_WO(i); }
    wait_t(t_start,T_TAIL);
    v4u xr;
    asm volatile("global_load_dwordx4 %0, %1, %2" : "=v"(xr) : "v"((w&3)*1024+16*lane), "s"(x) : "memory");
    const int thm=mlp?th:0, fim=mlp?fi:0;
    #pragma unroll
    for(int i=0;i<2;i++){ const int r=8*i+(lane>>3); dma16(w1,(u32)((128*tq+16*w+r)*(FF*2)+thm*128+16*((lane&7)^(2*(((r>>1)&1)|(i<<1))))),Lb+L_W1+(16*w+8*i)*128); }
    #pragma unroll
    for(int i=0;i<4;i++) DMA_T(wuv,L_WUV,i);
    #pragma unroll
    for(int i=0;i<2;i++){ const int r=8*i+w; dma16(w2,(u32)((16*fim+r)*1024+16*(lane^(2*(r&7)))),Lb+L_W2+r*1024); }
    v4u rwo[6];
    #pragma unroll
    for(int i=0;i<6;i++) asm volatile("global_load_dwordx4 %0, %1, %2" WO_NT : "=v"(rwo[i]) : "v"((th*128+80+8*i+w)*1024+16*lane), "s"(wo) : "memory");
    asm volatile("s_waitcnt vmcnt(12)" : "+v"(xr) :: "memory");
    STAMP(4);
    if(w<4) ln_row(xr,lane,tq,L+L_XN+w*320);
    BAR();
    if(mlp && w<4){
      v4u a1[4], bq[4];
      #pragma unroll
      for(int ks=0;ks<4;ks++){ lds8* a=L+L_W1+(32*ks+8*g+q4)*128+32*(w^((q4>>1)|((g&1)<<1)))+p4*8; a1[ks]=trd2(a,a+512); bq[ks]=lrd128(L+L_XN+p4*320+(32*ks+8*g)*2); }
      SB();
      f32x4 acc={0,0,0,0};
      #pragma unroll
      for(int ks=0;ks<4;ks++) acc=mfma16(a1[ks],bq[ks],acc);
      if((lane&15)<4){
        st128(rws,WS_HM+fi*2048+w*512+g*128+p4*16,v4u{__float_as_uint(acc[0]),tag,__float_as_uint(acc[1]),tag});
        st128(rws,WS_HM+fi*2048+w*512+g*128+64+p4*16,v4u{__float_as_uint(acc[2]),tag,__float_as_uint(acc[3]),tag}); }
    }
    VMW(6);
    BAR();
    v4u auv[4]; v2u aw2[4];
    #pragma unroll
    for(int k=0;k<4;k++){ lds8* a=L+L_WUV+k*8192+troff; auv[k]=trd2(a,a+1024); }
    #pragma unroll
    for(int t=0;t<4;t++) aw2[t]=trd(L+L_W2+r0*1024+(w>>1)*256+32*((4*(w&1)+t)^(r0&7))+p4*8);
    BAR();
    if(mlp && w==4){
      v4u h0,h1;
      _Pragma("unroll 1") for(int sp=0;sp<SPIN_CAP;++sp){ FENCE();
        h0=ld128<AUX_SC1>(rws,WS_HM+(th*4+q4)*2048+tq*512+g*128+p4*16);
        h1=ld128<AUX_SC1>(rws,WS_HM+(th*4+q4)*2048+tq*512+g*128+64+p4*16);
        if(__all((h0.y==tag)&(h0.w==tag)&(h1.y==tag)&(h1.w==tag))) break;
        __builtin_amdgcn_s_sleep(SLP);
      }
      float hv[4]={__uint_as_float(h0.x),__uint_as_float(h0.z),__uint_as_float(h1.x),__uint_as_float(h1.z)};
      #pragma unroll
      for(int r=0;r<4;r++){ float sv=hv[r]; sv+=dpp<DROR4>(sv); sv+=dpp<DROR8>(sv); float h=rbf(rbf(sv)+blo(b1raw[r])); h=h>0.f?h:0.f; hv[r]=h*h; }
      *(LAS v2u*)(L+L_H2+lane*8)=v2u{pk2(hv[0],hv[1]),pk2(hv[2],hv[3])};
    }
    asm volatile("s_waitcnt vmcnt(0)" : "+v"(rwo[0]),"+v"(rwo[1]),"+v"(rwo[2]),"+v"(rwo[3]),"+v"(rwo[4]),"+v"(rwo[5]) :: "memory");
    #pragma unroll
    for(int i=0;i<6;i++) *(LAS v4u*)(L+L_WO+(80+8*i+w)*1024+16*(lane^(2*((w&3)|((i&1)<<2)))))=rwo[i];
    v4u awo[4][4];
#define LOAD_AWO(ks) _Pragma("unroll") for(int t=0;t<4;t++){ lds8* a=L+L_WO+(32*(ks)+8*g+q4)*1024+(w>>1)*256+p4*8+32*((4*(w&1)+t)^(q4|((g&1)<<2))); awo[ks][t]=trd2(a,a+4096); }
    LOAD_AWO(0); LOAD_AWO(1);
    SB();
    STAMP(5);
    BAR();
    asm volatile("" : "+v"(xraw), "+v"(b2raw));
    const float xb_res=blo(xraw)+blo(b2raw);
    LOAD_AWO(2); LOAD_AWO(3);
    f32x4 oacc[4];
    #pragma unroll
    for(int t=0;t<4;t++) oacc[t]=f32x4{0,0,0,0};
    if(mlp){
      const v2u h2=*(LAS v2u*)(L+L_H2+lane*8);
      #pragma unroll
      for(int t=0;t<4;t++) oacc[t]=mfma16(v4u{aw2[t].x,aw2[t].y,0,0},v4u{h2.x,h2.y,0,0},oacc[t]);
    }
    SB();
    STAMP(6);
    if(w==0){
      wait_t(t_start,T_GUN);
      v4u v0,v1;
      const u32 tagh=(u32)(want>>32)^0x5A5A5A5Au;
      _Pragma("unroll 1") for(int sp=0;sp<SPIN_CAP;++sp){ FENCE();
        v0=ld128<AUX_SC1>(rws,WS_LSE+(th>>2)*8192+lane*64+(th&3)*16);
        v1=ld128<AUX_SC1>(rws,WS_LSE+(th>>2)*8192+4096+lane*64+(th&3)*16);
        if(__all((v0.y==tag)&(v1.y==tag)&(v0.w==tagh)&(v1.w==tagh)&(v0.x==v0.z)&(v1.x==v1.z))) break; __builtin_amdgcn_s_sleep(SLP_S);
      }
      STAMP(7);
      *(LAS u32*)(L+L_WG+hh*128+(l31&7)*16+(l31>>3)*4)=v0.x;
      *(LAS u32*)(L+L_WG+256+hh*128+(l31&7)*16+(l31>>3)*4)=v1.x;
    }
    BAR();
    {
      const int b=w>>1, half=w&1, slo=lane>>3, q=lane&7;
      const int po_off=WS_PO+fi*PO_STRIDE+(b*32+slo)*256+half*128+q*16;
      v4u po[4];
      #pragma unroll
      for(int i=0;i<4;i++) po[i]=ld128<AUX_SC1>(rws,po_off+i*2048);
      SB();
      const f32x4 lf=*(LAS f32x4*)(L+L_WG+b*128+slo*16);
      float M=vmax(vmax3(lf[0],lf[1],lf[2]),lf[3]);
      M=vmax(M,dpp<DROR8>(M)); M=sw16max(M); M=sw32max(M);
      f32x4 e;
      #pragma unroll
      for(int i=0;i<4;i++) e[i]=__builtin_amdgcn_exp2f(lf[i]-M);
      float Ls=(e[0]+e[1])+(e[2]+e[3]);
      Ls+=dpp<DROR8>(Ls); Ls=sw16add(Ls,Ls); Ls=sw32add(Ls,Ls);
      e*=__builtin_amdgcn_rcpf(Ls);
      SB();
      STAMP(8);
      float a[8];
      #pragma unroll
      for(int k=0;k<8;k++) a[k]=0.f;
      #pragma unroll
      for(int i=0;i<4;i++){
        a[0]+=e[i]*blo(po[i].x); a[1]+=e[i]*bhi(po[i].x); a[2]+=e[i]*blo(po[i].y); a[3]+=e[i]*bhi(po[i].y);
        a[4]+=e[i]*blo(po[i].z); a[5]+=e[i]*bhi(po[i].z); a[6]+=e[i]*blo(po[i].w); a[7]+=e[i]*bhi(po[i].w);
      }
      float c4[4], c2[2];
      #pragma unroll
      for(int k=0;k<4;k++) c4[k]=sw32add(a[k],a[k+4]);
      c2[0]=sw16add(c4[0],c4[2]); c2[1]=sw16add(c4[1],c4[3]);
      const bool b3=(lane>>3)&1;
      const float keep=b3?c2[1]:c2[0], send=b3?c2[0]:c2[1];
      const float r=keep+dpp<DROR8>(send);
      const int col=8*q+4*hh+2*((lane>>4)&1)+(b3?1:0);
      *(LAS u16*)(L+L_CL+b*320+(half*64+col)*2)=(u16)pk2(r,0.f);
    }
    BAR();
    STAMP(9);
    {
      v4u bc[4];
      #pragma unroll
      for(int k=0;k<4;k++) bc[k]=lrd128(L+L_CL+p4*320+(32*k+8*g)*2);
      SB();
      f32x4 acc={0,0,0,0};
      #pragma unroll
      for(int k=0;k<4;k++) acc=mfma16(auv[k],bc[k],acc);
      if((lane&15)<4) *(LAS v2u*)(L+L_CX+p4*320+(16*w+4*g)*2)=v2u{pk2(acc[0],acc[1]),pk2(acc[2],acc[3])};
    }
    BAR();
    {
      v4u bc[4];
      #pragma unroll
      for(int ks=0;ks<4;ks++) bc[ks]=lrd128(L+L_CX+p4*320+(32*ks+8*g)*2);
      SB();
      #pragma unroll
      for(int ks=0;ks<4;ks++)
        #pragma unroll
        for(int t=0;t<4;t++) oacc[t]=mfma16(awo[ks][t],bc[ks],oacc[t]);
      f32x4 v;
      #pragma unroll
      for(int r=0;r<4;r++){ const float lo=(q4&1)?oacc[1][r]:oacc[0][r], hi=(q4&1)?oacc[3][r]:oacc[2][r]; v[r]=(q4&2)?hi:lo; }
      st128(rws,WS_OUT+(16*w+4*q4+g)*OUT_STRIDE+fi*64+p4*16,v4u{pk2(v[0],v[1]),tag,pk2(v[2],v[3]),tag});
    }
    STAMP(10);
    { const u64 t1=rt(); wait_t(t1,D_E4); }
    v4u pv;
    _Pragma("unroll 1") for(int sp=0;sp<SPIN_CAP;++sp){ FENCE();
      pv=ld128<AUX_SC1>(rws,WS_OUT+fi*OUT_STRIDE+tid*16);
      if(__all((pv.y==tag)&(pv.w==tag))) break;
      __builtin_amdgcn_s_sleep(SLP_S);
    }
    STAMP(11);
    const float u0=sw32add(blo(pv.x),blo(pv.z)), u1=sw32add(bhi(pv.x),bhi(pv.z));
    float v=sw16add(u0,u1);
    v+=dpp<DROR4>(v); v+=dpp<DROR8>(v);
    if((lane&12)==0) *(LAS float*)(L+L_RED+w*64+(g*4+p4)*4)=v;
    BAR();
    if(tid<16){
      float a=0.f;
      #pragma unroll
      for(int ww=0;ww<8;ww++) a+=*(LAS float*)(L+L_RED+ww*64+tid*4);
      out[(tid&3)*DM+4*fi+(tid>>2)]=(u16)pk2(xb_res+a,0.f);
    }
    if(wg==0 && tid==0) __hip_atomic_store((gu64*)(ws+WS_EPOCH),want,RLX_AGENT);
    asm volatile(".Lmla_code_end:");
  } else {
    const int ab=fi>>5, as=fi&31;
    const int fl=fsw(l31);
    const int blk=(lane>>4)&1, sgm=w>>1;
    const u16* gb=ckv+((size_t)ab*SK+as*128+32*(w&3))*DC;
#define DMA_CKV(r) dma16(gb+(r)*DC,(u32)((lane*16)^(16*fsw(r))),Lb+L_CKV+(32*w+(r))*1024)
    #pragma unroll
    for(int i=0;i<4;i++) DMA_T(wq,L_AWQ,i);
    if(w<4){
      v2u e2;
      asm volatile("global_load_dwordx2 %0, %1, %2" : "=v"(e2) : "v"(0), "s"(ws) : "memory");
      wait_t(t_start,T_CKV0);
      #pragma unroll
      for(int r=0;r<INFL0;r++) DMA_CKV(r);
      asm volatile("s_waitcnt vmcnt(%1)" : "+v"(e2) : "i"(INFL0) : "memory");
      if(w==0 && lane==0) *(LAS u64*)(L+L_AEP)=(((u64)e2.y<<32)|e2.x)+1;
    } else {
      v4u xr;
      asm volatile("global_load_dwordx4 %0, %1, %2\n\ts_waitcnt vmcnt(0)" : "=v"(xr) : "v"((w&3)*1024+16*lane), "s"(x) : "memory");
      ln_row(xr,lane,tq,L+L_AXN+(w-4)*320);
    }
    BAR();
    ESTAMP(2);
    u32 tag; u64 want;
    {
      v4u aq[4], bq[4];
      #pragma unroll
      for(int ks=0;ks<4;ks++){ lds8* a=L+L_AWQ+ks*8192+troff; aq[ks]=trd2(a,a+1024); bq[ks]=lrd128(L+L_AXN+p4*320+(32*ks+8*g)*2); }
      const v2u e=*(LAS v2u*)(L+L_AEP);
      SB();
      f32x4 acc={0,0,0,0};
      #pragma unroll
      for(int ks=0;ks<4;ks++) acc=mfma16(aq[ks],bq[ks],acc);
      tag=__builtin_amdgcn_readfirstlane(e.x);
      if((lane&15)<4) st128(rws,WS_Q1+fi*2048+w*256+g*64+p4*16,v4u{pk2(acc[0],acc[1]),tag,pk2(acc[2],acc[3]),tag});
      want=((u64)__builtin_amdgcn_readfirstlane(e.y)<<32)|tag;
    }
    ESTAMP(3);
    BAR();
    v4u vf[8][2];
#define LOAD_VF() do{ _Pragma("unroll") for(int ks=0;ks<8;ks++) _Pragma("unroll") for(int t=0;t<2;t++){ \
        const int ch=8*(w&1)+4*t+2*blk+(p4>>1); \
        lds8* a0=L+L_CKV+(16*ks+8*hh+q4)*1024+256*sgm+16*(ch^((q4<<2)|(2*hh)))+8*(p4&1); \
        lds8* a1=L+L_CKV+(16*ks+8*hh+4+q4)*1024+256*sgm+16*(ch^((q4<<2)|(2*hh+1)))+8*(p4&1); \
        vf[ks][t]=trd2(a0,a1); } SB(); }while(0)
#define GATHER_QA() do{ v4u qa[4]; const int qo=WS_QA+(xc&1)*131072+ab*32768+4*w*1024+16*lane; \
      _Pragma("unroll") for(int i=0;i<4;i++) qa[i]=ld128<AUX_SC1>(rws,qo+i*1024); \
      _Pragma("unroll 1") for(int sp=0;sp<SPIN_CAP;++sp){ u32 bad=0; \
        _Pragma("unroll") for(int i=0;i<4;i++) bad|=(__all(ok4(qa[i]))?0u:1u)<<i; \
        if(!bad) break; \
        __builtin_amdgcn_s_sleep(SLP_S); FENCE(); \
        _Pragma("unroll") for(int i=0;i<4;i++) if((bad>>i)&1) qa[i]=ld128<AUX_SC1>(rws,qo+i*1024); } \
      _Pragma("unroll") for(int i=0;i<4;i++) *(LAS v4u*)(L+L_QAS+(4*w+i)*1024+((lane*16)^(16*((i<<2)|(w&3)))))=qa[i]; }while(0)
    float mx, lse;
    if(w<4){
      #pragma unroll
      for(int r=INFL0;r<32;r++){
        if(r>=CKV_INFL) VMW(CKV_INFL-1);
        DMA_CKV(r);
      }
      BAR();
      GATHER_QA();
      VMW(0);
      ESTAMP(0);
      v4u af[32];
      #pragma unroll
      for(int k=0;k<32;k++) af[k]=lrd128(L+L_CKV+(32*w+l31)*1024+256*(k>>3)+16*((2*(k&7)+hh)^fl));
      BAR();
      ESTAMP(1);
      f32x16 sacc;
      #pragma unroll
      for(int i=0;i<16;i++) sacc[i]=0.f;
      v4u bf[12];
      #pragma unroll
      for(int k=0;k<12;k++) bf[k]=lrd128(L+L_QAS+l31*1024+256*(k>>3)+16*((2*(k&7)+hh)^fl));
      SB();
      #pragma unroll
      for(int k=0;k<32;k++){
        sacc=mfma32(af[k],bf[k%12],sacc);
        if(k+12<32) bf[k%12]=lrd128(L+L_QAS+l31*1024+256*((k+12)>>3)+16*((2*((k+12)&7)+hh)^fl));
        SB();
      }
      float mloc=vmax3(vmax3(vmax3(sacc[0],sacc[1],sacc[2]),vmax3(sacc[3],sacc[4],sacc[5]),vmax3(sacc[6],sacc[7],sacc[8])),
                       vmax3(vmax3(sacc[9],sacc[10],sacc[11]),vmax3(sacc[12],sacc[13],sacc[14]),sacc[15]),sacc[15]);
      mloc=sw32max(mloc);
      BAR();
      STAMP(4);
      if(lane<32) *(LAS float*)(L+L_MX+w*128+lane*4)=mloc;
      BAR();
      mx=vmax(vmax3(*(LAS float*)(L+L_MX+l31*4),*(LAS float*)(L+L_MX+128+l31*4),*(LAS float*)(L+L_MX+256+l31*4)),*(LAS float*)(L+L_MX+384+l31*4));
      float ls=0.f;
      #pragma unroll
      for(int a=0;a<4;a++){
        float p0=__builtin_amdgcn_exp2f(sacc[4*a]-mx), p1=__builtin_amdgcn_exp2f(sacc[4*a+1]-mx), p2=__builtin_amdgcn_exp2f(sacc[4*a+2]-mx), p3=__builtin_amdgcn_exp2f(sacc[4*a+3]-mx);
        const u32 u0=pk2(p0,p1), u1=pk2(p2,p3);
        ls+=(p0+p1)+(p2+p3);
        *(LAS v2u*)(L+L_P+l31*256+16*((4*w+a)^fl)+8*hh)=v2u{u0,u1};
      }
      ls=sw32add(ls,ls);
      if(lane<32) *(LAS float*)(L+L_LS+w*128+lane*4)=ls;
      BAR();
      STAMP(5);
      LOAD_VF();
    } else {
      const int wv=w-4;
      const u64 t_q=rt();
      wait_t(t_start,T_FLAG1);
      _Pragma("unroll 1") for(int sp=0;sp<SPIN_CAP;++sp){ FENCE();
        if(__all(flag_ok(ld128<AUX_SC1>(rws,WS_FLAG1+xc*1024+wv*256+(lane&15)*16),want))) break;
        __builtin_amdgcn_s_sleep(SLP);
      }
      ESTAMP(0);
      wait_t(t_q,D_G);
      BAR();
      GATHER_QA();
      ESTAMP(1);
      BAR();
      LOAD_VF();
      BAR();
      BAR();
      mx=0.f;
      BAR();
    }
    {
      v4u pb[8];
      #pragma unroll
      for(int ks=0;ks<8;ks++) pb[ks]=lrd128(L+L_P+l31*256+16*((2*ks+hh)^fl));
      const float lsum=(*(LAS float*)(L+L_LS+l31*4)+*(LAS float*)(L+L_LS+128+l31*4))+(*(LAS float*)(L+L_LS+256+l31*4)+*(LAS float*)(L+L_LS+384+l31*4));
      SB();
      f32x16 o[2];
      #pragma unroll
      for(int i=0;i<16;i++){ o[0][i]=0.f; o[1][i]=0.f; }
      #pragma unroll
      for(int t=0;t<2;t++)
        #pragma unroll
        for(int ks=0;ks<8;ks++) o[t]=mfma32(vf[ks][t],pb[ks],o[t]);
      const float inv=__builtin_amdgcn_rcpf(lsum);
      lse=mx+__builtin_amdgcn_logf(lsum);
      STAMP(6);
      #pragma unroll
      for(int t=0;t<2;t++)
        #pragma unroll
        for(int a=0;a<4;a++)
          *(LAS v2u*)(L+L_CKV+l31*1024+256*sgm+16*((8*(w&1)+4*t+a)^fl)+8*hh)=v2u{pk2(o[t][4*a]*inv,o[t][4*a+1]*inv),pk2(o[t][4*a+2]*inv,o[t][4*a+3]*inv)};
      v4u ov[4];
      #pragma unroll
      for(int i=0;i<4;i++){ const int head=8*i+(lane>>3); ov[i]=lrd128(L+L_CKV+head*1024+256*sgm+16*((8*(w&1)+(lane&7))^fsw(head))); }
      SB();
      #pragma unroll
      for(int i=0;i<4;i++){ const int head=8*i+(lane>>3); st128(rws,WS_PO+(head*4+sgm)*PO_STRIDE+(ab*32+as)*256+(w&1)*128+(lane&7)*16,ov[i]); }
    }
    STAMP(7);
    VMW(0);
    BAR();
    if(w==0 && lane<32) st128(rws,WS_LSE+(lane>>2)*8192+fi*64+(lane&3)*16,v4u{__float_as_uint(lse),tag,__float_as_uint(lse),(u32)(want>>32)^0x5A5A5A5Au});
    STAMP(11);
  }
  STAMP(15);
}

extern "C" void kernel_launch(void* const* d_in,const int* in_sizes,int n_in,void* d_out,int out_size,void* d_ws,size_t ws_size,hipStream_t stream){
  (void)hipFuncSetAttribute((const void*)mla_mlp_kernel,hipFuncAttributeMaxDynamicSharedMemorySize,LDS_BYTES);
  mla_mlp_kernel<<<256,512,LDS_BYTES,stream>>>((const u16*)d_in[0],(const u16*)d_in[1],(const u16*)d_in[2],(const u16*)d_in[3],(const u16*)d_in[4],(const u16*)d_in[5],
     (const u16*)d_in[7],(const u16*)d_in[8],(const u16*)d_in[9],(const u16*)d_in[10],(u16*)d_out,(unsigned char*)d_ws);
}
```

```cpp
#include <hip/hip_runtime.h>
#include <cstring>
typedef unsigned short u16; typedef unsigned u32; typedef unsigned long long u64;
#define GAS __attribute__((address_space(1)))
#define LAS __attribute__((address_space(3)))
typedef u32 v4u __attribute__((ext_vector_type(4)));
typedef u32 v2u __attribute__((ext_vector_type(2)));
typedef float f32x2 __attribute__((ext_vector_type(2)));
typedef float f32x4 __attribute__((ext_vector_type(4)));
typedef float f32x16 __attribute__((ext_vector_type(16)));
typedef __bf16 bf16x8 __attribute__((ext_vector_type(8)));
typedef __bf16 bf16x2 __attribute__((ext_vector_type(2)));
typedef short s16x4 __attribute__((ext_vector_type(4)));
typedef GAS u64 gu64;
typedef LAS unsigned char lds8;
#define RLX_AGENT __ATOMIC_RELAXED, __HIP_MEMORY_SCOPE_AGENT
#define DI __device__ __forceinline__

constexpr int NB=4, NH=32, SK=4096, HD=128, DM=512, DC=512, FF=1792;
constexpr int WS_EPOCH=0, WS_FLAG1=8192, WS_Q1=32768, WS_HM=WS_Q1+262144, WS_QA=WS_HM+262144, WS_LSE=WS_QA+262144, WS_OUT=WS_LSE+65536,
              WS_PO=WS_OUT+(2<<20), WS_END=WS_PO+(5<<20), WS_STAMP=WS_END;
constexpr int L_CKV=0, L_QAS=131072, L_P=L_QAS, L_MX=L_QAS+8192, L_LS=L_QAS+8704, L_AWQ=L_QAS, L_AXN=126*1024, L_AEP=L_AXN+1536;
constexpr int L_WUK=0, L_WO=0, L_W2=81920, L_WUV=98304, L_W1=131072, L_XN=147456, L_QS=148736, L_CL=149760, L_H2=151040, L_RED=151552, L_EP=152064, L_QL=152576, L_WG=156672, L_CX=157184, L_STF=158720;
constexpr int LDS_BYTES=163840;
constexpr u32 SENT=0xFFFFFFFFu;
constexpr int SPIN_CAP=1<<20;
constexpr int AUX_NT=2, AUX_SC1=16;
#define PO_STRIDE 32768
#define OUT_STRIDE 8192
#define CKV_INFL 12
#define INFL0 12
#define T_CKV0 120
#define T_E1 150
#define T_FLAG1 240
#define T_GATHER 400
#define WO_1 4
#define T_WO 0
#define T_TAIL 460
#define T_GUN 620
#define D_E4 50
#define LSE_OFF (lane*2048+fi*16)
#define D_G 200
#define SLP 4
#define SLP_S 1
static_assert(CKV_INFL<=30,"ring");
#define STAMP(k) do{}while(0)
#define ESTAMP(i) do{}while(0)

DI u64 rt(){ return __builtin_amdgcn_s_memrealtime(); }
DI u64 ct(){ u64 t; asm volatile("s_memtime %0\n\ts_waitcnt lgkmcnt(0)" : "=s"(t) :: "memory"); return t; }
DI __amdgpu_buffer_rsrc_t mkrs(const void* p, int bytes){ return __builtin_amdgcn_make_buffer_rsrc((void*)p,0,bytes,0x00020000); }
template<int AUX> DI v4u ld128(__amdgpu_buffer_rsrc_t rs,int off){ return __builtin_amdgcn_raw_buffer_load_b128(rs,off,0,AUX); }
template<int AUX> DI u32 ld32(__amdgpu_buffer_rsrc_t rs,int off){ return __builtin_amdgcn_raw_buffer_load_b32(rs,off,0,AUX); }
DI void st128(__amdgpu_buffer_rsrc_t rs,int off,v4u v){ __builtin_amdgcn_raw_buffer_store_b128(v,rs,off,0,AUX_SC1); }
DI void st32(__amdgpu_buffer_rsrc_t rs,int off,u32 v){ __builtin_amdgcn_raw_buffer_store_b32(v,rs,off,0,AUX_SC1); }
DI f32x4 mfma16(v4u a,v4u b,f32x4 c){ return __builtin_amdgcn_mfma_f32_16x16x32_bf16(__builtin_bit_cast(bf16x8,a),__builtin_bit_cast(bf16x8,b),c,0,0,0); }
DI f32x16 mfma32(v4u a,v4u b,f32x16 c){ return __builtin_amdgcn_mfma_f32_32x32x16_bf16(__builtin_bit_cast(bf16x8,a),__builtin_bit_cast(bf16x8,b),c,0,0,0); }
DI v2u trd(lds8* p){ return __builtin_bit_cast(v2u,__builtin_amdgcn_ds_read_tr16_b64_v4i16((LAS s16x4*)p)); }
DI v4u trd2(lds8* p0,lds8* p1){ v2u a=trd(p0), b=trd(p1); return v4u{a.x,a.y,b.x,b.y}; }
DI v4u lrd128(lds8* p){ return *(LAS v4u*)p; }
DI u32 pk2(float lo,float hi){ f32x2 f={lo,hi}; return __builtin_bit_cast(u32,__builtin_convertvector(f,bf16x2)); }
DI float rbf(float x){ return __uint_as_float(pk2(x,0.f)<<16); }
DI float vmax(float a,float b){ return fmaxf(a,b); }
DI float vmax3(float a,float b,float c){ return fmaxf(fmaxf(a,b),c); }
DI float blo(u32 w){ return __uint_as_float(w<<16); }
DI float bhi(u32 w){ return __uint_as_float(w&0xFFFF0000u); }
DI u32 nos(u32 w){ return w<0xFFFFFFFEu? w:0xFFFFFFFEu; }
DI v4u nos4(v4u v){ return v4u{nos(v.x),nos(v.y),nos(v.z),nos(v.w)}; }
DI bool ok4(v4u v){ return (v.x!=SENT)&(v.y!=SENT)&(v.z!=SENT)&(v.w!=SENT); }
#define BAR() asm volatile("s_waitcnt lgkmcnt(0)\n\ts_barrier" ::: "memory")
#define VMW(n) asm volatile("s_waitcnt vmcnt(%0)" :: "i"(n) : "memory")
#define FENCE() asm volatile("" ::: "memory")
#define SB() __builtin_amdgcn_sched_barrier(0)
template<int CTRL> DI float dpp(float x){ return __builtin_bit_cast(float,__builtin_amdgcn_mov_dpp(__builtin_bit_cast(int,x),CTRL,0xf,0xf,true)); }
constexpr int DXOR1=0xB1, DXOR2=0x4E, DHMIR=0x141, DROR8=0x128, DROR4=0x124;
DI float sw32add(float a,float b){ auto s=__builtin_amdgcn_permlane32_swap(__float_as_uint(a),__float_as_uint(b),false,false); return __uint_as_float(s[0])+__uint_as_float(s[1]); }
DI float sw16add(float a,float b){ auto s=__builtin_amdgcn_permlane16_swap(__float_as_uint(a),__float_as_uint(b),false,false); return __uint_as_float(s[0])+__uint_as_float(s[1]); }
DI float sw32max(float a){ auto s=__builtin_amdgcn_permlane32_swap(__float_as_uint(a),__float_as_uint(a),false,false); return vmax(__uint_as_float(s[0]),__uint_as_float(s[1])); }
DI float sw16max(float a){ auto s=__builtin_amdgcn_permlane16_swap(__float_as_uint(a),__float_as_uint(a),false,false); return vmax(__uint_as_float(s[0]),__uint_as_float(s[1])); }
DI float wave_sum(float v){ v+=dpp<DXOR1>(v); v+=dpp<DXOR2>(v); v+=dpp<DHMIR>(v); v+=dpp<DROR8>(v); v=sw16add(v,v); v=sw32add(v,v); return v; }
DI void dma16(const void* gbase,u32 voff,u32 ldsb){
  u32 keep;
  asm volatile("s_mov_b32 %0, m0\n\ts_mov_b32 m0, %3\n\ts_nop 0\n\tglobal_load_lds_dwordx4 %1, %2 nt\n\ts_mov_b32 m0, %0" : "=&s"(keep) : "v"(voff),"s"(gbase),"s"(ldsb) : "memory");
}
DI void dma16c(const void* gbase,u32 voff,u32 ldsb){
  u32 keep;
  asm volatile("s_mov_b32 %0, m0\n\ts_mov_b32 m0, %3\n\ts_nop 0\n\tglobal_load_lds_dwordx4 %1, %2\n\ts_mov_b32 m0, %0" : "=&s"(keep) : "v"(voff),"s"(gbase),"s"(ldsb) : "memory");
}
DI constexpr int fsw(int row){ return ((row&3)<<2)|((row>>2)&3); }
DI bool flag_ok(v4u f,u64 want){ return (f.x==(u32)want)&(f.y==(u32)(want>>32))&(f.z==(u32)want)&(f.w==(u32)(want>>32)); }
DI void wait_t(u64 t0,int ticks){ while(rt()-t0<(u64)ticks) __builtin_amdgcn_s_sleep(1); }
DI void ln_row(v4u xr,int lane,int tq,lds8* dst){
  float v[8]={blo(xr.x),bhi(xr.x),blo(xr.y),bhi(xr.y),blo(xr.z),bhi(xr.z),blo(xr.w),bhi(xr.w)};
  const float mean=wave_sum(((v[0]+v[1])+(v[2]+v[3]))+((v[4]+v[5])+(v[6]+v[7])))*(1.f/DM);
  float qq=0.f;
  #pragma unroll
  for(int i=0;i<8;i++){ const float d=v[i]-mean; qq+=d*d; }
  const float var=wave_sum(qq)*(1.f/DM);
  const float mb=rbf(mean), isd=__builtin_amdgcn_rcpf(rbf(__builtin_amdgcn_sqrtf(rbf(rbf(var)+1e-5f))));
  float y[8];
  #pragma unroll
  for(int i=0;i<8;i++) y[i]=rbf(v[i]-mb)*isd;
  if((lane>>4)==tq) *(LAS v4u*)(dst+16*(lane&15))=v4u{pk2(y[0],y[1]),pk2(y[2],y[3]),pk2(y[4],y[5]),pk2(y[6],y[7])};
}

extern "C" __global__ void __launch_bounds__(512,2) mla_mlp_kernel(const u16* __restrict__ x,const u16* __restrict__ ckv,const u16* __restrict__ wq,const u16* __restrict__ wuk,
    const u16* __restrict__ wuv,const u16* __restrict__ wo,const u16* __restrict__ w1,const u16* __restrict__ b1,const u16* __restrict__ w2,const u16* __restrict__ b2,
    u16* __restrict__ out,unsigned char* __restrict__ ws){
  extern __shared__ __attribute__((aligned(16))) unsigned char smem[];
  lds8* L=(lds8*)smem;
  const u32 Lb=(u32)(size_t)L;
  const int tid=threadIdx.x, lane=tid&63, w=__builtin_amdgcn_readfirstlane(tid>>6);
  const int wg=blockIdx.x, xc=wg&7, j=wg>>3, jj=j&15;
  const bool isF=j<16;
  const int fi=xc*16+jj, th=fi>>2, tq=fi&3;
  const int g=lane>>4, q4=(lane&15)>>2, p4=lane&3, hh=lane>>5, l31=lane&31;
  if(isF && (w==5 || w==6)){
    u64 pc; u32 back, size;
    asm volatile("s_getpc_b64 %0\n.Lmla_pc_anchor:\n\ts_mov_b32 %1, .Lmla_pc_anchor-mla_mlp_kernel\n\ts_mov_b32 %2, .Lmla_code_end-mla_mlp_kernel" : "=s"(pc),"=s"(back),"=s"(size));
    const u32 off=(u32)((j+16*(6-w))*1024+16*lane);
    dma16c((const void*)(pc-back),off+16<=size?off:0u,Lb+L_W1+(w-5)*1024);
  }
  const __amdgpu_buffer_rsrc_t rws=mkrs(ws,WS_END);
  asm volatile("" :: "s"(x),"s"(ckv),"s"(wq),"s"(wuk),"s"(ws) : "memory");
  const u64 t_start=rt();
  const u32 rowoff=(u32)((128*tq+16*w+g)*8192+th*256);
#define DMA_T(p,dst,i) dma16(p,rowoff+(u32)(4*(i)*8192)+16*((lane&15)^(2*(g|(((i)>>1)<<2)))),Lb+(dst)+(16*w+4*(i))*256)
  const int troff=(8*g+q4)*256+(w^(q4|((g&1)<<2)))*32+p4*8;
  const int r0=4*g+q4;
  if(isF){
    const bool mlp = fi<FF/16;
    if(w==7){
      v2u e2;
      st128(rws,WS_QA+g*32768+th*1024+tq*256+(lane&15)*16,v4u{SENT,SENT,SENT,SENT});
      st128(rws,WS_QA+131072+g*32768+th*1024+tq*256+(lane&15)*16,v4u{SENT,SENT,SENT,SENT});
      asm volatile("global_load_dwordx2 %0, %1, %2" : "=v"(e2) : "v"(0), "s"(ws) : "memory");
      #pragma unroll
      for(int i=0;i<4;i++) dma16(wuk,rowoff+(u32)(4*i*8192)+16*((lane&15)^(4*i+g)),Lb+L_WUK+(16*w+4*i)*256);
      asm volatile("s_waitcnt vmcnt(4)" : "+v"(e2) :: "memory");
      const u64 wn=(((u64)e2.y<<32)|e2.x)+1; if(lane<8) __hip_atomic_store((gu64*)(ws+WS_FLAG1)+lane*128+fi,wn,RLX_AGENT);
      if(lane==0) *(LAS u64*)(L+L_EP)=wn;
      VMW(1);
    } else {
      #pragma unroll
      for(int i=0;i<4;i++) dma16(wuk,rowoff+(u32)(4*i*8192)+16*((lane&15)^(4*i+g)),Lb+L_WUK+(16*w+4*i)*256);
      VMW(0);
    }
    v4u auk[4];
    #pragma unroll
    for(int k=0;k<4;k++){
      lds8* a=L+L_WUK+(16*w+(lane&15))*256+8*(g&1);
      const v2u lo=*(LAS v2u*)(a+16*((4*k+(g>>1))^(lane&15))), hi=*(LAS v2u*)(a+16*((4*k+2+(g>>1))^(lane&15)));
      auk[k]=v4u{lo.x,lo.y,hi.x,hi.y};
    }
    BAR();
    STAMP(1);
    const v2u ep=*(LAS v2u*)(L+L_EP);
    const u32 tag=__builtin_amdgcn_readfirstlane(ep.x);
    const u64 want=((u64)__builtin_amdgcn_readfirstlane(ep.y)<<32)|tag;
    wait_t(t_start,T_E1);
    {
      v4u qf;
      _Pragma("unroll 1") for(int sp=0;sp<SPIN_CAP;++sp){ FENCE();
        qf=ld128<AUX_SC1>(rws,WS_Q1+th*8192+w*1024+lane*16);
        if(__all((qf.y==tag)&(qf.w==tag))) break;
        __builtin_amdgcn_s_sleep(SLP_S);
      }
      const int t=4*(w&1)+g;
      *(LAS v2u*)(L+L_QL+(w>>1)*1024+(t>>1)*256+(lane&15)*16+(t&1)*8)=v2u{qf.x,qf.z};
    }
    BAR();
    STAMP(2);
    {
      v4u qb[4];
      #pragma unroll
      for(int k=0;k<4;k++) qb[k]=lrd128(L+L_QL+q4*1024+k*256+g*64+p4*16);
      SB();
      f32x4 acc={0,0,0,0};
      #pragma unroll
      for(int k=0;k<4;k++) acc=mfma16(auk[k],qb[k],acc);
      #pragma unroll
      for(int r=0;r<4;r++){ acc[r]+=dpp<DROR4>(acc[r]); acc[r]+=dpp<DROR8>(acc[r]); }
      const float SC=0.08838834764831845f*1.4426950408889634f;
      if((lane&15)<4) *(LAS v2u*)(L+L_QS+(lane&15)*256+(16*w+4*g)*2)=v2u{nos(pk2(acc[0]*SC,acc[1]*SC)),nos(pk2(acc[2]*SC,acc[3]*SC))};
    }
    BAR();
    if(w<2){ v4u v=lrd128(L+L_QS+lane*16); st128(rws,WS_QA+w*131072+g*32768+th*1024+tq*256+(lane&15)*16,v); }
    STAMP(3);
    u32 b1raw[4];
    #pragma unroll
    for(int r=0;r<4;r++) b1raw[r]=__builtin_amdgcn_raw_buffer_load_b16(mkrs(b1,FF*2),(16*fi+4*g+r)*2,0,0);
    u32 xraw=__builtin_amdgcn_raw_buffer_load_b16(mkrs(x,NB*DM*2),(p4*DM+4*fi+q4)*2,0,0);
    u32 b2raw=__builtin_amdgcn_raw_buffer_load_b16(mkrs(b2,DM*2),(4*fi+q4)*2,0,0);
    SB();
#define dma_wo dma16
#define WO_NT " nt"
#define DMA_WO(i) dma_wo(wo,(u32)((th*128+8*(i)+w)*1024+16*(lane^(2*((w&3)|(((i)&1)<<2))))),Lb+L_WO+(8*(i)+w)*1024)
    wait_t(t_start,T_WO);
    #pragma unroll
    for(int i=0;i<10;i++){ if(i>=WO_1) VMW(WO_1-1); DMA_WO(i); }
    wait_t(t_start,T_TAIL);
    v4u xr;
    asm volatile("global_load_dwordx4 %0, %1, %2" : "=v"(xr) : "v"((w&3)*1024+16*lane), "s"(x) : "memory");
    const int thm=mlp?th:0, fim=mlp?fi:0;
    #pragma unroll
    for(int i=0;i<2;i++){ const int r=8*i+(lane>>3); dma16(w1,(u32)((128*tq+16*w+r)*(FF*2)+thm*128+16*((lane&7)^(2*(((r>>1)&1)|(i<<1))))),Lb+L_W1+(16*w+8*i)*128); }
    #pragma unroll
    for(int i=0;i<4;i++) DMA_T(wuv,L_WUV,i);
    #pragma unroll
    for(int i=0;i<2;i++){ const int r=8*i+w; dma16(w2,(u32)((16*fim+r)*1024+16*(lane^(2*(r&7)))),Lb+L_W2+r*1024); }
    v4u rwo[6];
    #pragma unroll
    for(int i=0;i<6;i++) asm volatile("global_load_dwordx4 %0, %1, %2" WO_NT : "=v"(rwo[i]) : "v"((th*128+80+8*i+w)*1024+16*lane), "s"(wo) : "memory");
    asm volatile("s_waitcnt vmcnt(12)" : "+v"(xr) :: "memory");
    STAMP(4);
    if(w<4) ln_row(xr,lane,tq,L+L_XN+w*320);
    BAR();
    if(mlp && w<4){
      v4u a1[4], bq[4];
      #pragma unroll
      for(int ks=0;ks<4;ks++){ lds8* a=L+L_W1+(32*ks+8*g+q4)*128+32*(w^((q4>>1)|((g&1)<<1)))+p4*8; a1[ks]=trd2(a,a+512); bq[ks]=lrd128(L+L_XN+p4*320+(32*ks+8*g)*2); }
      SB();
      f32x4 acc={0,0,0,0};
      #pragma unroll
      for(int ks=0;ks<4;ks++) acc=mfma16(a1[ks],bq[ks],acc);
      if((lane&15)<4){
        st128(rws,WS_HM+fi*2048+w*512+g*128+p4*16,v4u{__float_as_uint(acc[0]),tag,__float_as_uint(acc[1]),tag});
        st128(rws,WS_HM+fi*2048+w*512+g*128+64+p4*16,v4u{__float_as_uint(acc[2]),tag,__float_as_uint(acc[3]),tag}); }
    }
    VMW(6);
    BAR();
    v4u auv[4]; v2u aw2[4];
    #pragma unroll
    for(int k=0;k<4;k++){ lds8* a=L+L_WUV+k*8192+troff; auv[k]=trd2(a,a+1024); }
    #pragma unroll
    for(int t=0;t<4;t++) aw2[t]=trd(L+L_W2+r0*1024+(w>>1)*256+32*((4*(w&1)+t)^(r0&7))+p4*8);
    BAR();
    if(mlp && w==4){
      v4u h0,h1;
      _Pragma("unroll 1") for(int sp=0;sp<SPIN_CAP;++sp){ FENCE();
        h0=ld128<AUX_SC1>(rws,WS_HM+(th*4+q4)*2048+tq*512+g*128+p4*16);
        h1=ld128<AUX_SC1>(rws,WS_HM+(th*4+q4)*2048+tq*512+g*128+64+p4*16);
        if(__all((h0.y==tag)&(h0.w==tag)&(h1.y==tag)&(h1.w==tag))) break;
        __builtin_amdgcn_s_sleep(SLP);
      }
      float hv[4]={__uint_as_float(h0.x),__uint_as_float(h0.z),__uint_as_float(h1.x),__uint_as_float(h1.z)};
      #pragma unroll
      for(int r=0;r<4;r++){ float sv=hv[r]; sv+=dpp<DROR4>(sv); sv+=dpp<DROR8>(sv); float h=rbf(rbf(sv)+blo(b1raw[r])); h=h>0.f?h:0.f; hv[r]=h*h; }
      *(LAS v2u*)(L+L_H2+lane*8)=v2u{pk2(hv[0],hv[1]),pk2(hv[2],hv[3])};
    }
    asm volatile("s_waitcnt vmcnt(0)" : "+v"(rwo[0]),"+v"(rwo[1]),"+v"(rwo[2]),"+v"(rwo[3]),"+v"(rwo[4]),"+v"(rwo[5]) :: "memory");
    #pragma unroll
    for(int i=0;i<6;i++) *(LAS v4u*)(L+L_WO+(80+8*i+w)*1024+16*(lane^(2*((w&3)|((i&1)<<2)))))=rwo[i];
    v4u awo[4][4];
#define LOAD_AWO(ks) _Pragma("unroll") for(int t=0;t<4;t++){ lds8* a=L+L_WO+(32*(ks)+8*g+q4)*1024+(w>>1)*256+p4*8+32*((4*(w&1)+t)^(q4|((g&1)<<2))); awo[ks][t]=trd2(a,a+4096); }
    LOAD_AWO(0); LOAD_AWO(1);
    SB();
    STAMP(5);
    STAMP(6);
    asm volatile("" : "+v"(xraw), "+v"(b2raw));
    const float xb_res=blo(xraw)+blo(b2raw);
    f32x4 oacc[4];
    if(w==0){
      wait_t(t_start,T_GUN);
      v4u v0,v1;
      const u32 tagh=(u32)(want>>32)^0x5A5A5A5Au;
      _Pragma("unroll 1") for(int sp=0;sp<SPIN_CAP;++sp){ FENCE();
        v0=ld128<AUX_SC1>(rws,WS_LSE+th*2048+lane*16);
        v1=ld128<AUX_SC1>(rws,WS_LSE+th*2048+1024+lane*16);
        if(__all((v0.y==tag)&(v1.y==tag)&(v0.w==tagh)&(v1.w==tagh)&(v0.x==v0.z)&(v1.x==v1.z))) break; __builtin_amdgcn_s_sleep(SLP_S);
      }
      STAMP(7);
      *(LAS u32*)(L+L_WG+hh*128+(l31&7)*16+(l31>>3)*4)=v0.x;
      *(LAS u32*)(L+L_WG+256+hh*128+(l31&7)*16+(l31>>3)*4)=v1.x;
    }
    BAR();
    {
      const int b=w>>1, half=w&1, slo=lane>>3, q=lane&7;
      const int po_off=WS_PO+fi*PO_STRIDE+(b*32+slo)*256+half*128+q*16;
      v4u po[4];
      #pragma unroll
      for(int i=0;i<4;i++) po[i]=ld128<AUX_SC1>(rws,po_off+i*2048);
      SB();
      LOAD_AWO(2); LOAD_AWO(3);
      #pragma unroll
      for(int t=0;t<4;t++) oacc[t]=f32x4{0,0,0,0};
      if(mlp){
        const v2u h2=*(LAS v2u*)(L+L_H2+lane*8);
        #pragma unroll
        for(int t=0;t<4;t++) oacc[t]=mfma16(v4u{aw2[t].x,aw2[t].y,0,0},v4u{h2.x,h2.y,0,0},oacc[t]);
      }
      const f32x4 lf=*(LAS f32x4*)(L+L_WG+b*128+slo*16);
      float M=vmax(vmax3(lf[0],lf[1],lf[2]),lf[3]);
      M=vmax(M,dpp<DROR8>(M)); M=sw16max(M); M=sw32max(M);
      f32x4 e;
      #pragma unroll
      for(int i=0;i<4;i++) e[i]=__builtin_amdgcn_exp2f(lf[i]-M);
      float Ls=(e[0]+e[1])+(e[2]+e[3]);
      Ls+=dpp<DROR8>(Ls); Ls=sw16add(Ls,Ls); Ls=sw32add(Ls,Ls);
      e*=__builtin_amdgcn_rcpf(Ls);
      SB();
      STAMP(8);
      float a[8];
      #pragma unroll
      for(int k=0;k<8;k++) a[k]=0.f;
      #pragma unroll
      for(int i=0;i<4;i++){
        a[0]+=e[i]*blo(po[i].x); a[1]+=e[i]*bhi(po[i].x); a[2]+=e[i]*blo(po[i].y); a[3]+=e[i]*bhi(po[i].y);
        a[4]+=e[i]*blo(po[i].z); a[5]+=e[i]*bhi(po[i].z); a[6]+=e[i]*blo(po[i].w); a[7]+=e[i]*bhi(po[i].w);
      }
      float c4[4], c2[2];
      #pragma unroll
      for(int k=0;k<4;k++) c4[k]=sw32add(a[k],a[k+4]);
      c2[0]=sw16add(c4[0],c4[2]); c2[1]=sw16add(c4[1],c4[3]);
      const bool b3=(lane>>3)&1;
      const float keep=b3?c2[1]:c2[0], send=b3?c2[0]:c2[1];
      const float r=keep+dpp<DROR8>(send);
      const int col=8*q+4*hh+2*((lane>>4)&1)+(b3?1:0);
      *(LAS u16*)(L+L_CL+b*320+(half*64+col)*2)=(u16)pk2(r,0.f);
    }
    BAR();
    STAMP(9);
    {
      v4u bc[4];
      #pragma unroll
      for(int k=0;k<4;k++) bc[k]=lrd128(L+L_CL+p4*320+(32*k+8*g)*2);
      SB();
      f32x4 acc={0,0,0,0};
      #pragma unroll
      for(int k=0;k<4;k++) acc=mfma16(auv[k],bc[k],acc);
      if((lane&15)<4) *(LAS v2u*)(L+L_CX+p4*320+(16*w+4*g)*2)=v2u{pk2(acc[0],acc[1]),pk2(acc[2],acc[3])};
    }
    BAR();
    {
      v4u bc[4];
      #pragma unroll
      for(int ks=0;ks<4;ks++) bc[ks]=lrd128(L+L_CX+p4*320+(32*ks+8*g)*2);
      SB();
      #pragma unroll
      for(int ks=0;ks<4;ks++)
        #pragma unroll
        for(int t=0;t<4;t++) oacc[t]=mfma16(awo[ks][t],bc[ks],oacc[t]);
      f32x4 v;
      #pragma unroll
      for(int r=0;r<4;r++){ const float lo=(q4&1)?oacc[1][r]:oacc[0][r], hi=(q4&1)?oacc[3][r]:oacc[2][r]; v[r]=(q4&2)?hi:lo; }
      st128(rws,WS_OUT+(16*w+4*q4+g)*OUT_STRIDE+fi*64+p4*16,v4u{pk2(v[0],v[1]),tag,pk2(v[2],v[3]),tag});
    }
    STAMP(10);
    { const u64 t1=rt(); wait_t(t1,D_E4); }
    v4u pv;
    _Pragma("unroll 1") for(int sp=0;sp<SPIN_CAP;++sp){ FENCE();
      pv=ld128<AUX_SC1>(rws,WS_OUT+fi*OUT_STRIDE+tid*16);
      if(__all((pv.y==tag)&(pv.w==tag))) break;
      __builtin_amdgcn_s_sleep(SLP_S);
    }
    STAMP(11);
    const float u0=sw32add(blo(pv.x),blo(pv.z)), u1=sw32add(bhi(pv.x),bhi(pv.z));
    float v=sw16add(u0,u1);
    v+=dpp<DROR4>(v); v+=dpp<DROR8>(v);
    if((lane&12)==0) *(LAS float*)(L+L_RED+w*64+(g*4+p4)*4)=v;
    BAR();
    if(tid<16){
      float a=0.f;
      #pragma unroll
      for(int ww=0;ww<8;ww++) a+=*(LAS float*)(L+L_RED+ww*64+tid*4);
      out[(tid&3)*DM+4*fi+(tid>>2)]=(u16)pk2(xb_res+a,0.f);
    }
    if(wg==0 && tid==0) __hip_atomic_store((gu64*)(ws+WS_EPOCH),want,RLX_AGENT);
    asm volatile(".Lmla_code_end:");
  } else {
    const int ab=fi>>5, as=fi&31;
    const int fl=fsw(l31);
    const int blk=(lane>>4)&1, sgm=w>>1;
    const u16* gb=ckv+((size_t)ab*SK+as*128+32*(w&3))*DC;
#define DMA_CKV(r) dma16(gb+(r)*DC,(u32)((lane*16)^(16*fsw(r))),Lb+L_CKV+(32*w+(r))*1024)
    #pragma unroll
    for(int i=0;i<4;i++) DMA_T(wq,L_AWQ,i);
    if(w<4){
      v2u e2;
      asm volatile("global_load_dwordx2 %0, %1, %2" : "=v"(e2) : "v"(0), "s"(ws) : "memory");
      wait_t(t_start,T_CKV0);
      #pragma unroll
      for(int r=0;r<INFL0;r++) DMA_CKV(r);
      asm volatile("s_waitcnt vmcnt(%1)" : "+v"(e2) : "i"(INFL0) : "memory");
      if(w==0 && lane==0) *(LAS u64*)(L+L_AEP)=(((u64)e2.y<<32)|e2.x)+1;
    } else {
      v4u xr;
      asm volatile("global_load_dwordx4 %0, %1, %2\n\ts_waitcnt vmcnt(0)" : "=v"(xr) : "v"((w&3)*1024+16*lane), "s"(x) : "memory");
      ln_row(xr,lane,tq,L+L_AXN+(w-4)*320);
    }
    BAR();
    ESTAMP(2);
    u32 tag; u64 want;
    {
      v4u aq[4], bq[4];
      #pragma unroll
      for(int ks=0;ks<4;ks++){ lds8* a=L+L_AWQ+ks*8192+troff; aq[ks]=trd2(a,a+1024); bq[ks]=lrd128(L+L_AXN+p4*320+(32*ks+8*g)*2); }
      const v2u e=*(LAS v2u*)(L+L_AEP);
      SB();
      f32x4 acc={0,0,0,0};
      #pragma unroll
      for(int ks=0;ks<4;ks++) acc=mfma16(aq[ks],bq[ks],acc);
      tag=__builtin_amdgcn_readfirstlane(e.x);
      if((lane&15)<4) st128(rws,WS_Q1+fi*2048+w*256+g*64+p4*16,v4u{pk2(acc[0],acc[1]),tag,pk2(acc[2],acc[3]),tag});
      want=((u64)__builtin_amdgcn_readfirstlane(e.y)<<32)|tag;
    }
    ESTAMP(3);
    BAR();
    v4u vf[8][2];
#define LOAD_VF() do{ _Pragma("unroll") for(int ks=0;ks<8;ks++) _Pragma("unroll") for(int t=0;t<2;t++){ \
        const int ch=8*(w&1)+4*t+2*blk+(p4>>1); \
        lds8* a0=L+L_CKV+(16*ks+8*hh+q4)*1024+256*sgm+16*(ch^((q4<<2)|(2*hh)))+8*(p4&1); \
        lds8* a1=L+L_CKV+(16*ks+8*hh+4+q4)*1024+256*sgm+16*(ch^((q4<<2)|(2*hh+1)))+8*(p4&1); \
        vf[ks][t]=trd2(a0,a1); } SB(); }while(0)
#define GATHER_QA() do{ v4u qa[4]; const int qo=WS_QA+(xc&1)*131072+ab*32768+4*w*1024+16*lane; \
      _Pragma("unroll") for(int i=0;i<4;i++) qa[i]=ld128<AUX_SC1>(rws,qo+i*1024); \
      _Pragma("unroll 1") for(int sp=0;sp<SPIN_CAP;++sp){ u32 bad=0; \
        _Pragma("unroll") for(int i=0;i<4;i++) bad|=(__all(ok4(qa[i]))?0u:1u)<<i; \
        if(!bad) break; \
        __builtin_amdgcn_s_sleep(SLP_S); FENCE(); \
        _Pragma("unroll") for(int i=0;i<4;i++) if((bad>>i)&1) qa[i]=ld128<AUX_SC1>(rws,qo+i*1024); } \
      _Pragma("unroll") for(int i=0;i<4;i++) *(LAS v4u*)(L+L_QAS+(4*w+i)*1024+((lane*16)^(16*((i<<2)|(w&3)))))=qa[i]; }while(0)
    float mx, lse;
    if(w<4){
      #pragma unroll
      for(int r=INFL0;r<32;r++){
        if(r>=CKV_INFL) VMW(CKV_INFL-1);
        DMA_CKV(r);
      }
      BAR();
      GATHER_QA();
      VMW(0);
      ESTAMP(0);
      v4u af[32];
      #pragma unroll
      for(int k=0;k<32;k++) af[k]=lrd128(L+L_CKV+(32*w+l31)*1024+256*(k>>3)+16*((2*(k&7)+hh)^fl));
      BAR();
      ESTAMP(1);
      f32x16 sacc;
      #pragma unroll
      for(int i=0;i<16;i++) sacc[i]=0.f;
      v4u bf[12];
      #pragma unroll
      for(int k=0;k<12;k++) bf[k]=lrd128(L+L_QAS+l31*1024+256*(k>>3)+16*((2*(k&7)+hh)^fl));
      SB();
      #pragma unroll
      for(int k=0;k<32;k++){
        sacc=mfma32(af[k],bf[k%12],sacc);
        if(k+12<32) bf[k%12]=lrd128(L+L_QAS+l31*1024+256*((k+12)>>3)+16*((2*((k+12)&7)+hh)^fl));
        SB();
      }
      float mloc=vmax3(vmax3(vmax3(sacc[0],sacc[1],sacc[2]),vmax3(sacc[3],sacc[4],sacc[5]),vmax3(sacc[6],sacc[7],sacc[8])),
                       vmax3(vmax3(sacc[9],sacc[10],sacc[11]),vmax3(sacc[12],sacc[13],sacc[14]),sacc[15]),sacc[15]);
      mloc=sw32max(mloc);
      BAR();
      STAMP(4);
      if(lane<32) *(LAS float*)(L+L_MX+w*128+lane*4)=mloc;
      BAR();
      mx=vmax(vmax3(*(LAS float*)(L+L_MX+l31*4),*(LAS float*)(L+L_MX+128+l31*4),*(LAS float*)(L+L_MX+256+l31*4)),*(LAS float*)(L+L_MX+384+l31*4));
      float ls=0.f;
      #pragma unroll
      for(int a=0;a<4;a++){
        float p0=__builtin_amdgcn_exp2f(sacc[4*a]-mx), p1=__builtin_amdgcn_exp2f(sacc[4*a+1]-mx), p2=__builtin_amdgcn_exp2f(sacc[4*a+2]-mx), p3=__builtin_amdgcn_exp2f(sacc[4*a+3]-mx);
        const u32 u0=pk2(p0,p1), u1=pk2(p2,p3);
        ls+=(p0+p1)+(p2+p3);
        *(LAS v2u*)(L+L_P+l31*256+16*((4*w+a)^fl)+8*hh)=v2u{u0,u1};
      }
      ls=sw32add(ls,ls);
      if(lane<32) *(LAS float*)(L+L_LS+w*128+lane*4)=ls;
      BAR();
      STAMP(5);
      LOAD_VF();
    } else {
      const int wv=w-4;
      const u64 t_q=rt();
      wait_t(t_start,T_FLAG1);
      _Pragma("unroll 1") for(int sp=0;sp<SPIN_CAP;++sp){ FENCE();
        if(__all(flag_ok(ld128<AUX_SC1>(rws,WS_FLAG1+xc*1024+wv*256+(lane&15)*16),want))) break;
        __builtin_amdgcn_s_sleep(SLP);
      }
      ESTAMP(0);
      wait_t(t_q,D_G);
      BAR();
      GATHER_QA();
      ESTAMP(1);
      BAR();
      LOAD_VF();
      BAR();
      BAR();
      mx=0.f;
      BAR();
    }
    {
      v4u pb[8];
      #pragma unroll
      for(int ks=0;ks<8;ks++) pb[ks]=lrd128(L+L_P+l31*256+16*((2*ks+hh)^fl));
      const float lsum=(*(LAS float*)(L+L_LS+l31*4)+*(LAS float*)(L+L_LS+128+l31*4))+(*(LAS float*)(L+L_LS+256+l31*4)+*(LAS float*)(L+L_LS+384+l31*4));
      SB();
      f32x16 o[2];
      #pragma unroll
      for(int i=0;i<16;i++){ o[0][i]=0.f; o[1][i]=0.f; }
      #pragma unroll
      for(int t=0;t<2;t++)
        #pragma unroll
        for(int ks=0;ks<8;ks++) o[t]=mfma32(vf[ks][t],pb[ks],o[t]);
      const float inv=__builtin_amdgcn_rcpf(lsum);
      lse=mx+__builtin_amdgcn_logf(lsum);
      STAMP(6);
      #pragma unroll
      for(int t=0;t<2;t++)
        #pragma unroll
        for(int a=0;a<4;a++)
          *(LAS v2u*)(L+L_CKV+l31*1024+256*sgm+16*((8*(w&1)+4*t+a)^fl)+8*hh)=v2u{pk2(o[t][4*a]*inv,o[t][4*a+1]*inv),pk2(o[t][4*a+2]*inv,o[t][4*a+3]*inv)};
      v4u ov[4];
      #pragma unroll
      for(int i=0;i<4;i++){ const int head=8*i+(lane>>3); ov[i]=lrd128(L+L_CKV+head*1024+256*sgm+16*((8*(w&1)+(lane&7))^fsw(head))); }
      SB();
      #pragma unroll
      for(int i=0;i<4;i++){ const int head=8*i+(lane>>3); st128(rws,WS_PO+(head*4+sgm)*PO_STRIDE+(ab*32+as)*256+(w&1)*128+(lane&7)*16,ov[i]); }
    }
    STAMP(7);
    VMW(0);
    BAR();
    if(w==0 && lane<32) st128(rws,WS_LSE+LSE_OFF,v4u{__float_as_uint(lse),tag,__float_as_uint(lse),(u32)(want>>32)^0x5A5A5A5Au});
    STAMP(11);
  }
  STAMP(15);
}

extern "C" void kernel_launch(void* const* d_in,const int* in_sizes,int n_in,void* d_out,int out_size,void* d_ws,size_t ws_size,hipStream_t stream){
  (void)hipFuncSetAttribute((const void*)mla_mlp_kernel,hipFuncAttributeMaxDynamicSharedMemorySize,LDS_BYTES);
  mla_mlp_kernel<<<256,512,LDS_BYTES,stream>>>((const u16*)d_in[0],(const u16*)d_in[1],(const u16*)d_in[2],(const u16*)d_in[3],(const u16*)d_in[4],(const u16*)d_in[5],
     (const u16*)d_in[7],(const u16*)d_in[8],(const u16*)d_in[9],(const u16*)d_in[10],(u16*)d_out,(unsigned char*)d_ws);
}
```

```cpp
#include <hip/hip_runtime.h>
#include <cstring>
typedef unsigned short u16; typedef unsigned u32; typedef unsigned long long u64;
#define GAS __attribute__((address_space(1)))
#define LAS __attribute__((address_space(3)))
typedef u32 v4u __attribute__((ext_vector_type(4)));
typedef u32 v2u __attribute__((ext_vector_type(2)));
typedef float f32x2 __attribute__((ext_vector_type(2)));
typedef float f32x4 __attribute__((ext_vector_type(4)));
typedef float f32x16 __attribute__((ext_vector_type(16)));
typedef __bf16 bf16x8 __attribute__((ext_vector_type(8)));
typedef __bf16 bf16x2 __attribute__((ext_vector_type(2)));
typedef short s16x4 __attribute__((ext_vector_type(4)));
typedef GAS u64 gu64;
typedef LAS unsigned char lds8;
#define RLX_AGENT __ATOMIC_RELAXED, __HIP_MEMORY_SCOPE_AGENT
#define DI __device__ __forceinline__

constexpr int NB=4, NH=32, SK=4096, HD=128, DM=512, DC=512, FF=1792;
constexpr int WS_EPOCH=0, WS_FLAG1=8192, WS_Q1=32768, WS_HM=WS_Q1+524288, WS_QA=WS_HM+262144, WS_LSE=WS_QA+262144, WS_OUT=WS_LSE+65536,
              WS_PO=WS_OUT+(2<<20), WS_END=WS_PO+(5<<20), WS_STAMP=WS_END;
constexpr int L_CKV=0, L_QAS=131072, L_P=L_QAS, L_MX=L_QAS+8192, L_LS=L_QAS+8704, L_AWQ=L_QAS, L_AXN=126*1024, L_AEP=L_AXN+1536;
constexpr int L_WUK=0, L_WO=0, L_W2=81920, L_WUV=98304, L_W1=131072, L_XN=147456, L_QS=148736, L_CL=149760, L_H2=151040, L_RED=151552, L_EP=152064, L_QL=L_WUV, L_WG=156672, L_CX=157184, L_STF=158720;
constexpr int LDS_BYTES=163840;
constexpr u32 SENT=0xFFFFFFFFu;
constexpr int SPIN_CAP=1<<20;
constexpr int AUX_NT=2, AUX_SC1=16;
#define PO_STRIDE 32768
#define OUT_STRIDE 8192
#define CKV_INFL 12
#define INFL0 12
#define T_CKV0 120
#define T_E1 150
#define T_FLAG1 240
#define T_GATHER 400
#define WO_1 4
#define T_WO 0
#define T_TAIL 460
#define T_GUN 620
#define D_E4 50
#define LSE_OFF (lane*2048+fi*16)
#define D_G 200
#define SLP_G 1
#define D_E1 110
#define SLP 4
#define SLP_S 1
static_assert(CKV_INFL<=30,"ring");
#define STAMP(k) do{}while(0)
#define ESTAMP(i) do{}while(0)

DI u64 rt(){ return __builtin_amdgcn_s_memrealtime(); }
DI u64 ct(){ u64 t; asm volatile("s_memtime %0\n\ts_waitcnt lgkmcnt(0)" : "=s"(t) :: "memory"); return t; }
DI __amdgpu_buffer_rsrc_t mkrs(const void* p, int bytes){ return __builtin_amdgcn_make_buffer_rsrc((void*)p,0,bytes,0x00020000); }
template<int AUX> DI v4u ld128(__amdgpu_buffer_rsrc_t rs,int off){ return __builtin_amdgcn_raw_buffer_load_b128(rs,off,0,AUX); }
template<int AUX> DI u32 ld32(__amdgpu_buffer_rsrc_t rs,int off){ return __builtin_amdgcn_raw_buffer_load_b32(rs,off,0,AUX); }
DI void st128(__amdgpu_buffer_rsrc_t rs,int off,v4u v){ __builtin_amdgcn_raw_buffer_store_b128(v,rs,off,0,AUX_SC1); }
DI void st32(__amdgpu_buffer_rsrc_t rs,int off,u32 v){ __builtin_amdgcn_raw_buffer_store_b32(v,rs,off,0,AUX_SC1); }
DI f32x4 mfma16(v4u a,v4u b,f32x4 c){ return __builtin_amdgcn_mfma_f32_16x16x32_bf16(__builtin_bit_cast(bf16x8,a),__builtin_bit_cast(bf16x8,b),c,0,0,0); }
DI f32x16 mfma32(v4u a,v4u b,f32x16 c){ return __builtin_amdgcn_mfma_f32_32x32x16_bf16(__builtin_bit_cast(bf16x8,a),__builtin_bit_cast(bf16x8,b),c,0,0,0); }
DI v2u trd(lds8* p){ return __builtin_bit_cast(v2u,__builtin_amdgcn_ds_read_tr16_b64_v4i16((LAS s16x4*)p)); }
DI v4u trd2(lds8* p0,lds8* p1){ v2u a=trd(p0), b=trd(p1); return v4u{a.x,a.y,b.x,b.y}; }
DI v4u lrd128(lds8* p){ return *(LAS v4u*)p; }
DI u32 pk2(float lo,float hi){ f32x2 f={lo,hi}; return __builtin_bit_cast(u32,__builtin_convertvector(f,bf16x2)); }
DI float rbf(float x){ return __uint_as_float(pk2(x,0.f)<<16); }
DI float vmax(float a,float b){ return fmaxf(a,b); }
DI float vmax3(float a,float b,float c){ return fmaxf(fmaxf(a,b),c); }
DI float blo(u32 w){ return __uint_as_float(w<<16); }
DI float bhi(u32 w){ return __uint_as_float(w&0xFFFF0000u); }
DI u32 nos(u32 w){ return w<0xFFFFFFFEu? w:0xFFFFFFFEu; }
DI v4u nos4(v4u v){ return v4u{nos(v.x),nos(v.y),nos(v.z),nos(v.w)}; }
DI bool ok4(v4u v){ return (v.x!=SENT)&(v.y!=SENT)&(v.z!=SENT)&(v.w!=SENT); }
#define BAR() asm volatile("s_waitcnt lgkmcnt(0)\n\ts_barrier" ::: "memory")
#define VMW(n) asm volatile("s_waitcnt vmcnt(%0)" :: "i"(n) : "memory")
#define FENCE() asm volatile("" ::: "memory")
#define SB() __builtin_amdgcn_sched_barrier(0)
template<int CTRL> DI float dpp(float x){ return __builtin_bit_cast(float,__builtin_amdgcn_mov_dpp(__builtin_bit_cast(int,x),CTRL,0xf,0xf,true)); }
constexpr int DXOR1=0xB1, DXOR2=0x4E, DHMIR=0x141, DROR8=0x128, DROR4=0x124;
DI float sw32add(float a,float b){ auto s=__builtin_amdgcn_permlane32_swap(__float_as_uint(a),__float_as_uint(b),false,false); return __uint_as_float(s[0])+__uint_as_float(s[1]); }
DI float sw16add(float a,float b){ auto s=__builtin_amdgcn_permlane16_swap(__float_as_uint(a),__float_as_uint(b),false,false); return __uint_as_float(s[0])+__uint_as_float(s[1]); }
DI float sw32max(float a){ auto s=__builtin_amdgcn_permlane32_swap(__float_as_uint(a),__float_as_uint(a),false,false); return vmax(__uint_as_float(s[0]),__uint_as_float(s[1])); }
DI float sw16max(float a){ auto s=__builtin_amdgcn_permlane16_swap(__float_as_uint(a),__float_as_uint(a),false,false); return vmax(__uint_as_float(s[0]),__uint_as_float(s[1])); }
DI float wave_sum(float v){ v+=dpp<DXOR1>(v); v+=dpp<DXOR2>(v); v+=dpp<DHMIR>(v); v+=dpp<DROR8>(v); v=sw16add(v,v); v=sw32add(v,v); return v; }
DI void dma16(const void* gbase,u32 voff,u32 ldsb){
  u32 keep;
  asm volatile("s_mov_b32 %0, m0\n\ts_mov_b32 m0, %3\n\ts_nop 0\n\tglobal_load_lds_dwordx4 %1, %2 nt\n\ts_mov_b32 m0, %0" : "=&s"(keep) : "v"(voff),"s"(gbase),"s"(ldsb) : "memory");
}
DI void dma16c(const void* gbase,u32 voff,u32 ldsb){
  u32 keep;
  asm volatile("s_mov_b32 %0, m0\n\ts_mov_b32 m0, %3\n\ts_nop 0\n\tglobal_load_lds_dwordx4 %1, %2\n\ts_mov_b32 m0, %0" : "=&s"(keep) : "v"(voff),"s"(gbase),"s"(ldsb) : "memory");
}
DI constexpr int fsw(int row){ return ((row&3)<<2)|((row>>2)&3); }
DI bool flag_ok(v4u f,u64 want){ return (f.x==(u32)want)&(f.y==(u32)(want>>32))&(f.z==(u32)want)&(f.w==(u32)(want>>32)); }
DI void wait_t(u64 t0,int ticks){ while(rt()-t0<(u64)ticks) __builtin_amdgcn_s_sleep(1); }
DI void ln_row(v4u xr,int lane,int tq,lds8* dst){
  float v[8]={blo(xr.x),bhi(xr.x),blo(xr.y),bhi(xr.y),blo(xr.z),bhi(xr.z),blo(xr.w),bhi(xr.w)};
  const float mean=wave_sum(((v[0]+v[1])+(v[2]+v[3]))+((v[4]+v[5])+(v[6]+v[7])))*(1.f/DM);
  float qq=0.f;
  #pragma unroll
  for(int i=0;i<8;i++){ const float d=v[i]-mean; qq+=d*d; }
  const float var=wave_sum(qq)*(1.f/DM);
  const float mb=rbf(mean), isd=__builtin_amdgcn_rcpf(rbf(__builtin_amdgcn_sqrtf(rbf(rbf(var)+1e-5f))));
  float y[8];
  #pragma unroll
  for(int i=0;i<8;i++) y[i]=rbf(v[i]-mb)*isd;
  if((lane>>4)==tq) *(LAS v4u*)(dst+16*(lane&15))=v4u{pk2(y[0],y[1]),pk2(y[2],y[3]),pk2(y[4],y[5]),pk2(y[6],y[7])};
}

extern "C" __global__ void __launch_bounds__(512,2) mla_mlp_kernel(const u16* __restrict__ x,const u16* __restrict__ ckv,const u16* __restrict__ wq,const u16* __restrict__ wuk,
    const u16* __restrict__ wuv,const u16* __restrict__ wo,const u16* __restrict__ w1,const u16* __restrict__ b1,const u16* __restrict__ w2,const u16* __restrict__ b2,
    u16* __restrict__ out,unsigned char* __restrict__ ws){
  extern __shared__ __attribute__((aligned(16))) unsigned char smem[];
  lds8* L=(lds8*)smem;
  const u32 Lb=(u32)(size_t)L;
  const int tid=threadIdx.x, lane=tid&63, w=__builtin_amdgcn_readfirstlane(tid>>6);
  const int wg=blockIdx.x, xc=wg&7, j=wg>>3, jj=j&15;
  const bool isF=j<16;
  const int fi=xc*16+jj, th=fi>>2, tq=fi&3;
  const int g=lane>>4, q4=(lane&15)>>2, p4=lane&3, hh=lane>>5, l31=lane&31;
  if(isF && (w==5 || w==6)){
    u64 pc; u32 back, size;
    asm volatile("s_getpc_b64 %0\n.Lmla_pc_anchor:\n\ts_mov_b32 %1, .Lmla_pc_anchor-mla_mlp_kernel\n\ts_mov_b32 %2, .Lmla_code_end-mla_mlp_kernel" : "=s"(pc),"=s"(back),"=s"(size));
    const u32 off=(u32)((j+16*(6-w))*1024+16*lane);
    dma16c((const void*)(pc-back),off+16<=size?off:0u,Lb+L_W1+(w-5)*1024);
  }
  const __amdgpu_buffer_rsrc_t rws=mkrs(ws,WS_END);
  asm volatile("" :: "s"(x),"s"(ckv),"s"(wq),"s"(wuk),"s"(ws) : "memory");
  const u64 t_start=rt();
  const u32 rowoff=(u32)((128*tq+16*w+g)*8192+th*256);
#define DMA_T(p,dst,i) dma16(p,rowoff+(u32)(4*(i)*8192)+16*((lane&15)^(2*(g|(((i)>>1)<<2)))),Lb+(dst)+(16*w+4*(i))*256)
  const int troff=(8*g+q4)*256+(w^(q4|((g&1)<<2)))*32+p4*8;
  const int r0=4*g+q4;
  if(isF){
    const bool mlp = fi<FF/16;
    if(w==7){
      v2u e2;
      st128(rws,WS_QA+g*32768+th*1024+tq*256+(lane&15)*16,v4u{SENT,SENT,SENT,SENT});
      st128(rws,WS_QA+131072+g*32768+th*1024+tq*256+(lane&15)*16,v4u{SENT,SENT,SENT,SENT});
      asm volatile("global_load_dwordx2 %0, %1, %2" : "=v"(e2) : "v"(0), "s"(ws) : "memory");
      #pragma unroll
      for(int i=0;i<4;i++) dma16(wuk,rowoff+(u32)(4*i*8192)+16*((lane&15)^(4*i+g)),Lb+L_WUK+(16*w+4*i)*256);
      asm volatile("s_waitcnt vmcnt(4)" : "+v"(e2) :: "memory");
      const u64 wn=(((u64)e2.y<<32)|e2.x)+1; if(lane<8) __hip_atomic_store((gu64*)(ws+WS_FLAG1)+lane*128+fi,wn,RLX_AGENT);
      if(lane==0) *(LAS u64*)(L+L_EP)=wn;
      VMW(1);
    } else {
      #pragma unroll
      for(int i=0;i<4;i++) dma16(wuk,rowoff+(u32)(4*i*8192)+16*((lane&15)^(4*i+g)),Lb+L_WUK+(16*w+4*i)*256);
      VMW(0);
    }
    v4u auk[4];
    #pragma unroll
    for(int k=0;k<4;k++){
      lds8* a=L+L_WUK+(16*w+(lane&15))*256+8*(g&1);
      const v2u lo=*(LAS v2u*)(a+16*((4*k+(g>>1))^(lane&15))), hi=*(LAS v2u*)(a+16*((4*k+2+(g>>1))^(lane&15)));
      auk[k]=v4u{lo.x,lo.y,hi.x,hi.y};
    }
    BAR();
    STAMP(1);
    const v2u ep=*(LAS v2u*)(L+L_EP);
    const u32 tag=__builtin_amdgcn_readfirstlane(ep.x);
    const u64 want=((u64)__builtin_amdgcn_readfirstlane(ep.y)<<32)|tag;
    { const u64 t1=rt(); wait_t(t1,D_E1); }
    {
      v4u qf[2];
      const int qo=WS_Q1+(th*4+hh)*4096+w*512+l31*16;
      _Pragma("unroll 1") for(int sp=0;sp<SPIN_CAP;++sp){ FENCE();
        qf[0]=ld128<AUX_SC1>(rws,qo);
        qf[1]=ld128<AUX_SC1>(rws,qo+8192);
        if(__all((qf[0].y==tag)&(qf[0].w==tag)&(qf[1].y==tag)&(qf[1].w==tag))) break;
        __builtin_amdgcn_s_sleep(SLP_S);
      }
      float s0=__uint_as_float(qf[0].x)+__uint_as_float(qf[1].x), s1=__uint_as_float(qf[0].z)+__uint_as_float(qf[1].z);
      s0=sw32add(s0,s0); s1=sw32add(s1,s1);
      if(lane<32) *(LAS u32*)(L+L_QL+(w>>1)*256+(lane>>3)*64+p4*16+(w&1)*8+((lane>>2)&1)*4)=pk2(s0,s1);
    }
    BAR();
    STAMP(2);
    {
      v4u qb[4];
      #pragma unroll
      for(int k=0;k<4;k++) qb[k]=lrd128(L+L_QL+k*256+g*64+p4*16);
      SB();
      f32x4 acc={0,0,0,0};
      #pragma unroll
      for(int k=0;k<4;k++) acc=mfma16(auk[k],qb[k],acc);
      const float SC=0.08838834764831845f*1.4426950408889634f;
      if((lane&15)<4) *(LAS v2u*)(L+L_QS+(lane&15)*256+(16*w+4*g)*2)=v2u{nos(pk2(acc[0]*SC,acc[1]*SC)),nos(pk2(acc[2]*SC,acc[3]*SC))};
    }
    BAR();
    if(w<2){ v4u v=lrd128(L+L_QS+lane*16); st128(rws,WS_QA+w*131072+g*32768+th*1024+tq*256+(lane&15)*16,v); }
    STAMP(3);
    u32 b1raw[4];
    #pragma unroll
    for(int r=0;r<4;r++) b1raw[r]=__builtin_amdgcn_raw_buffer_load_b16(mkrs(b1,FF*2),(16*fi+4*g+r)*2,0,0);
    u32 xraw=__builtin_amdgcn_raw_buffer_load_b16(mkrs(x,NB*DM*2),(p4*DM+4*fi+q4)*2,0,0);
    u32 b2raw=__builtin_amdgcn_raw_buffer_load_b16(mkrs(b2,DM*2),(4*fi+q4)*2,0,0);
    SB();
#define dma_wo dma16
#define WO_NT " nt"
#define DMA_WO(i) dma_wo(wo,(u32)((th*128+8*(i)+w)*1024+16*(lane^(2*((w&3)|(((i)&1)<<2))))),Lb+L_WO+(8*(i)+w)*1024)
    wait_t(t_start,T_WO);
    #pragma unroll
    for(int i=0;i<10;i++){ if(i>=WO_1) VMW(WO_1-1); DMA_WO(i); }
    wait_t(t_start,T_TAIL);
    v4u xr;
    asm volatile("global_load_dwordx4 %0, %1, %2" : "=v"(xr) : "v"((w&3)*1024+16*lane), "s"(x) : "memory");
    const int thm=mlp?th:0, fim=mlp?fi:0;
    #pragma unroll
    for(int i=0;i<2;i++){ const int r=8*i+(lane>>3); dma16(w1,(u32)((128*tq+16*w+r)*(FF*2)+thm*128+16*((lane&7)^(2*(((r>>1)&1)|(i<<1))))),Lb+L_W1+(16*w+8*i)*128); }
    #pragma unroll
    for(int i=0;i<4;i++) DMA_T(wuv,L_WUV,i);
    #pragma unroll
    for(int i=0;i<2;i++){ const int r=8*i+w; dma16(w2,(u32)((16*fim+r)*1024+16*(lane^(2*(r&7)))),Lb+L_W2+r*1024); }
    v4u rwo[6];
    #pragma unroll
    for(int i=0;i<6;i++) asm volatile("global_load_dwordx4 %0, %1, %2" WO_NT : "=v"(rwo[i]) : "v"((th*128+80+8*i+w)*1024+16*lane), "s"(wo) : "memory");
    asm volatile("s_waitcnt vmcnt(12)" : "+v"(xr) :: "memory");
    STAMP(4);
    if(w<4) ln_row(xr,lane,tq,L+L_XN+w*320);
    BAR();
    if(mlp && w<4){
      v4u a1[4], bq[4];
      #pragma unroll
      for(int ks=0;ks<4;ks++){ lds8* a=L+L_W1+(32*ks+8*g+q4)*128+32*(w^((q4>>1)|((g&1)<<1)))+p4*8; a1[ks]=trd2(a,a+512); bq[ks]=lrd128(L+L_XN+p4*320+(32*ks+8*g)*2); }
      SB();
      f32x4 acc={0,0,0,0};
      #pragma unroll
      for(int ks=0;ks<4;ks++) acc=mfma16(a1[ks],bq[ks],acc);
      if((lane&15)<4){
        st128(rws,WS_HM+fi*2048+w*512+g*128+p4*16,v4u{__float_as_uint(acc[0]),tag,__float_as_uint(acc[1]),tag});
        st128(rws,WS_HM+fi*2048+w*512+g*128+64+p4*16,v4u{__float_as_uint(acc[2]),tag,__float_as_uint(acc[3]),tag}); }
    }
    VMW(6);
    BAR();
    v4u auv[4]; v2u aw2[4];
    #pragma unroll
    for(int k=0;k<4;k++){ lds8* a=L+L_WUV+k*8192+troff; auv[k]=trd2(a,a+1024); }
    #pragma unroll
    for(int t=0;t<4;t++) aw2[t]=trd(L+L_W2+r0*1024+(w>>1)*256+32*((4*(w&1)+t)^(r0&7))+p4*8);
    BAR();
    if(mlp && w==4){
      v4u h0,h1;
      _Pragma("unroll 1") for(int sp=0;sp<SPIN_CAP;++sp){ FENCE();
        h0=ld128<AUX_SC1>(rws,WS_HM+(th*4+q4)*2048+tq*512+g*128+p4*16);
        h1=ld128<AUX_SC1>(rws,WS_HM+(th*4+q4)*2048+tq*512+g*128+64+p4*16);
        if(__all((h0.y==tag)&(h0.w==tag)&(h1.y==tag)&(h1.w==tag))) break;
        __builtin_amdgcn_s_sleep(SLP);
      }
      float hv[4]={__uint_as_float(h0.x),__uint_as_float(h0.z),__uint_as_float(h1.x),__uint_as_float(h1.z)};
      #pragma unroll
      for(int r=0;r<4;r++){ float sv=hv[r]; sv+=dpp<DROR4>(sv); sv+=dpp<DROR8>(sv); float h=rbf(rbf(sv)+blo(b1raw[r])); h=h>0.f?h:0.f; hv[r]=h*h; }
      *(LAS v2u*)(L+L_H2+lane*8)=v2u{pk2(hv[0],hv[1]),pk2(hv[2],hv[3])};
    }
    asm volatile("s_waitcnt vmcnt(0)" : "+v"(rwo[0]),"+v"(rwo[1]),"+v"(rwo[2]),"+v"(rwo[3]),"+v"(rwo[4]),"+v"(rwo[5]) :: "memory");
    #pragma unroll
    for(int i=0;i<6;i++) *(LAS v4u*)(L+L_WO+(80+8*i+w)*1024+16*(lane^(2*((w&3)|((i&1)<<2)))))=rwo[i];
    v4u awo[4][4];
#define LOAD_AWO(ks) _Pragma("unroll") for(int t=0;t<4;t++){ lds8* a=L+L_WO+(32*(ks)+8*g+q4)*1024+(w>>1)*256+p4*8+32*((4*(w&1)+t)^(q4|((g&1)<<2))); awo[ks][t]=trd2(a,a+4096); }
    LOAD_AWO(0); LOAD_AWO(1);
    SB();
    STAMP(5);
    STAMP(6);
    asm volatile("" : "+v"(xraw), "+v"(b2raw));
    const float xb_res=blo(xraw)+blo(b2raw);
    f32x4 oacc[4];
    if(w==0){
      wait_t(t_start,T_GUN);
      v4u v0,v1;
      const u32 tagh=(u32)(want>>32)^0x5A5A5A5Au;
      _Pragma("unroll 1") for(int sp=0;sp<SPIN_CAP;++sp){ FENCE();
        v0=ld128<AUX_SC1>(rws,WS_LSE+th*2048+lane*16);
        v1=ld128<AUX_SC1>(rws,WS_LSE+th*2048+1024+lane*16);
        if(__all((v0.y==tag)&(v1.y==tag)&(v0.w==tagh)&(v1.w==tagh)&(v0.x==v0.z)&(v1.x==v1.z))) break; __builtin_amdgcn_s_sleep(SLP_G);
      }
      STAMP(7);
      *(LAS u32*)(L+L_WG+hh*128+(l31&7)*16+(l31>>3)*4)=v0.x;
      *(LAS u32*)(L+L_WG+256+hh*128+(l31&7)*16+(l31>>3)*4)=v1.x;
    }
    BAR();
    {
      const int b=w>>1, half=w&1, slo=lane>>3, q=lane&7;
      const int po_off=WS_PO+fi*PO_STRIDE+(b*32+slo)*256+half*128+q*16;
      v4u po[4];
      #pragma unroll
      for(int i=0;i<4;i++) po[i]=ld128<AUX_SC1>(rws,po_off+i*2048);
      SB();
      LOAD_AWO(2); LOAD_AWO(3);
      #pragma unroll
      for(int t=0;t<4;t++) oacc[t]=f32x4{0,0,0,0};
      if(mlp){
        const v2u h2=*(LAS v2u*)(L+L_H2+lane*8);
        #pragma unroll
        for(int t=0;t<4;t++) oacc[t]=mfma16(v4u{aw2[t].x,aw2[t].y,0,0},v4u{h2.x,h2.y,0,0},oacc[t]);
      }
      const f32x4 lf=*(LAS f32x4*)(L+L_WG+b*128+slo*16);
      float M=vmax(vmax3(lf[0],lf[1],lf[2]),lf[3]);
      M=vmax(M,dpp<DROR8>(M)); M=sw16max(M); M=sw32max(M);
      f32x4 e;
      #pragma unroll
      for(int i=0;i<4;i++) e[i]=__builtin_amdgcn_exp2f(lf[i]-M);
      float Ls=(e[0]+e[1])+(e[2]+e[3]);
      Ls+=dpp<DROR8>(Ls); Ls=sw16add(Ls,Ls); Ls=sw32add(Ls,Ls);
      e*=__builtin_amdgcn_rcpf(Ls);
      SB();
      STAMP(8);
      float a[8];
      #pragma unroll
      for(int k=0;k<8;k++) a[k]=0.f;
      #pragma unroll
      for(int i=0;i<4;i++){
        a[0]+=e[i]*blo(po[i].x); a[1]+=e[i]*bhi(po[i].x); a[2]+=e[i]*blo(po[i].y); a[3]+=e[i]*bhi(po[i].y);
        a[4]+=e[i]*blo(po[i].z); a[5]+=e[i]*bhi(po[i].z); a[6]+=e[i]*blo(po[i].w); a[7]+=e[i]*bhi(po[i].w);
      }
      float c4[4], c2[2];
      #pragma unroll
      for(int k=0;k<4;k++) c4[k]=sw32add(a[k],a[k+4]);
      c2[0]=sw16add(c4[0],c4[2]); c2[1]=sw16add(c4[1],c4[3]);
      const bool b3=(lane>>3)&1;
      const float keep=b3?c2[1]:c2[0], send=b3?c2[0]:c2[1];
      const float r=keep+dpp<DROR8>(send);
      const int col=8*q+4*hh+2*((lane>>4)&1)+(b3?1:0);
      *(LAS u16*)(L+L_CL+b*320+(half*64+col)*2)=(u16)pk2(r,0.f);
    }
    BAR();
    STAMP(9);
    {
      v4u bc[4];
      #pragma unroll
      for(int k=0;k<4;k++) bc[k]=lrd128(L+L_CL+p4*320+(32*k+8*g)*2);
      SB();
      f32x4 acc={0,0,0,0};
      #pragma unroll
      for(int k=0;k<4;k++) acc=mfma16(auv[k],bc[k],acc);
      if((lane&15)<4) *(LAS v2u*)(L+L_CX+p4*320+(16*w+4*g)*2)=v2u{pk2(acc[0],acc[1]),pk2(acc[2],acc[3])};
    }
    BAR();
    {
      v4u bc[4];
      #pragma unroll
      for(int ks=0;ks<4;ks++) bc[ks]=lrd128(L+L_CX+p4*320+(32*ks+8*g)*2);
      SB();
      #pragma unroll
      for(int ks=0;ks<4;ks++)
        #pragma unroll
        for(int t=0;t<4;t++) oacc[t]=mfma16(awo[ks][t],bc[ks],oacc[t]);
      f32x4 v;
      #pragma unroll
      for(int r=0;r<4;r++){ const float lo=(q4&1)?oacc[1][r]:oacc[0][r], hi=(q4&1)?oacc[3][r]:oacc[2][r]; v[r]=(q4&2)?hi:lo; }
      st128(rws,WS_OUT+(16*w+4*q4+g)*OUT_STRIDE+fi*64+p4*16,v4u{pk2(v[0],v[1]),tag,pk2(v[2],v[3]),tag});
    }
    STAMP(10);
    { const u64 t1=rt(); wait_t(t1,D_E4); }
    v4u pv;
    _Pragma("unroll 1") for(int sp=0;sp<SPIN_CAP;++sp){ FENCE();
      pv=ld128<AUX_SC1>(rws,WS_OUT+fi*OUT_STRIDE+tid*16);
      if(__all((pv.y==tag)&(pv.w==tag))) break;
      __builtin_amdgcn_s_sleep(SLP_S);
    }
    STAMP(11);
    const float u0=sw32add(blo(pv.x),blo(pv.z)), u1=sw32add(bhi(pv.x),bhi(pv.z));
    float v=sw16add(u0,u1);
    v+=dpp<DROR4>(v); v+=dpp<DROR8>(v);
    if((lane&12)==0) *(LAS float*)(L+L_RED+w*64+(g*4+p4)*4)=v;
    BAR();
    if(tid<16){
      float a=0.f;
      #pragma unroll
      for(int ww=0;ww<8;ww++) a+=*(LAS float*)(L+L_RED+ww*64+tid*4);
      out[(tid&3)*DM+4*fi+(tid>>2)]=(u16)pk2(xb_res+a,0.f);
    }
    if(wg==0 && tid==0) __hip_atomic_store((gu64*)(ws+WS_EPOCH),want,RLX_AGENT);
    asm volatile(".Lmla_code_end:");
  } else {
    const int ab=fi>>5, as=fi&31;
    const int fl=fsw(l31);
    const int blk=(lane>>4)&1, sgm=w>>1;
    const u16* gb=ckv+((size_t)ab*SK+as*128+32*(w&3))*DC;
#define DMA_CKV(r) dma16(gb+(r)*DC,(u32)((lane*16)^(16*fsw(r))),Lb+L_CKV+(32*w+(r))*1024)
    #pragma unroll
    for(int i=0;i<4;i++) DMA_T(wq,L_AWQ,i);
    if(w<4){
      v2u e2;
      asm volatile("global_load_dwordx2 %0, %1, %2" : "=v"(e2) : "v"(0), "s"(ws) : "memory");
      wait_t(t_start,T_CKV0);
      #pragma unroll
      for(int r=0;r<INFL0;r++) DMA_CKV(r);
      asm volatile("s_waitcnt vmcnt(%1)" : "+v"(e2) : "i"(INFL0) : "memory");
      if(w==0 && lane==0) *(LAS u64*)(L+L_AEP)=(((u64)e2.y<<32)|e2.x)+1;
    } else {
      v4u xr;
      asm volatile("global_load_dwordx4 %0, %1, %2\n\ts_waitcnt vmcnt(0)" : "=v"(xr) : "v"((w&3)*1024+16*lane), "s"(x) : "memory");
      ln_row(xr,lane,tq,L+L_AXN+(w-4)*320);
    }
    BAR();
    ESTAMP(2);
    u32 tag; u64 want;
    {
      v4u aq[4], bq[4];
      #pragma unroll
      for(int ks=0;ks<4;ks++){ lds8* a=L+L_AWQ+ks*8192+troff; aq[ks]=trd2(a,a+1024); bq[ks]=lrd128(L+L_AXN+p4*320+(32*ks+8*g)*2); }
      const v2u e=*(LAS v2u*)(L+L_AEP);
      SB();
      f32x4 acc={0,0,0,0};
      #pragma unroll
      for(int ks=0;ks<4;ks++) acc=mfma16(aq[ks],bq[ks],acc);
      tag=__builtin_amdgcn_readfirstlane(e.x);
      if((lane&15)<8) st128(rws,WS_Q1+fi*4096+w*512+g*128+(lane&7)*16,v4u{__float_as_uint(q4?acc[2]:acc[0]),tag,__float_as_uint(q4?acc[3]:acc[1]),tag});
      want=((u64)__builtin_amdgcn_readfirstlane(e.y)<<32)|tag;
    }
    ESTAMP(3);
    BAR();
    v4u vf[8][2];
#define LOAD_VF() do{ _Pragma("unroll") for(int ks=0;ks<8;ks++) _Pragma("unroll") for(int t=0;t<2;t++){ \
        const int ch=8*(w&1)+4*t+2*blk+(p4>>1); \
        lds8* a0=L+L_CKV+(16*ks+8*hh+q4)*1024+256*sgm+16*(ch^((q4<<2)|(2*hh)))+8*(p4&1); \
        lds8* a1=L+L_CKV+(16*ks+8*hh+4+q4)*1024+256*sgm+16*(ch^((q4<<2)|(2*hh+1)))+8*(p4&1); \
        vf[ks][t]=trd2(a0,a1); } SB(); }while(0)
#define GATHER_QA() do{ v4u qa[4]; const int qo=WS_QA+(xc&1)*131072+ab*32768+4*w*1024+16*lane; \
      _Pragma("unroll") for(int i=0;i<4;i++) qa[i]=ld128<AUX_SC1>(rws,qo+i*1024); \
      _Pragma("unroll 1") for(int sp=0;sp<SPIN_CAP;++sp){ u32 bad=0; \
        _Pragma("unroll") for(int i=0;i<4;i++) bad|=(__all(ok4(qa[i]))?0u:1u)<<i; \
        if(!bad) break; \
        __builtin_amdgcn_s_sleep(SLP_S); FENCE(); \
        _Pragma("unroll") for(int i=0;i<4;i++) if((bad>>i)&1) qa[i]=ld128<AUX_SC1>(rws,qo+i*1024); } \
      _Pragma("unroll") for(int i=0;i<4;i++) *(LAS v4u*)(L+L_QAS+(4*w+i)*1024+((lane*16)^(16*((i<<2)|(w&3)))))=qa[i]; }while(0)
    float mx, lse;
    if(w<4){
      #pragma unroll
      for(int r=INFL0;r<32;r++){
        if(r>=CKV_INFL) VMW(CKV_INFL-1);
        DMA_CKV(r);
      }
      BAR();
      GATHER_QA();
      VMW(0);
      ESTAMP(0);
      v4u af[32];
      #pragma unroll
      for(int k=0;k<32;k++) af[k]=lrd128(L+L_CKV+(32*w+l31)*1024+256*(k>>3)+16*((2*(k&7)+hh)^fl));
      BAR();
      ESTAMP(1);
      f32x16 sacc;
      #pragma unroll
      for(int i=0;i<16;i++) sacc[i]=0.f;
      v4u bf[12];
      #pragma unroll
      for(int k=0;k<12;k++) bf[k]=lrd128(L+L_QAS+l31*1024+256*(k>>3)+16*((2*(k&7)+hh)^fl));
      SB();
      #pragma unroll
      for(int k=0;k<32;k++){
        sacc=mfma32(af[k],bf[k%12],sacc);
        if(k+12<32) bf[k%12]=lrd128(L+L_QAS+l31*1024+256*((k+12)>>3)+16*((2*((k+12)&7)+hh)^fl));
        SB();
      }
      float mloc=vmax3(vmax3(vmax3(sacc[0],sacc[1],sacc[2]),vmax3(sacc[3],sacc[4],sacc[5]),vmax3(sacc[6],sacc[7],sacc[8])),
                       vmax3(vmax3(sacc[9],sacc[10],sacc[11]),vmax3(sacc[12],sacc[13],sacc[14]),sacc[15]),sacc[15]);
      mloc=sw32max(mloc);
      BAR();
      STAMP(4);
      if(lane<32) *(LAS float*)(L+L_MX+w*128+lane*4)=mloc;
      BAR();
      mx=vmax(vmax3(*(LAS float*)(L+L_MX+l31*4),*(LAS float*)(L+L_MX+128+l31*4),*(LAS float*)(L+L_MX+256+l31*4)),*(LAS float*)(L+L_MX+384+l31*4));
      float ls=0.f;
      #pragma unroll
      for(int a=0;a<4;a++){
        float p0=__builtin_amdgcn_exp2f(sacc[4*a]-mx), p1=__builtin_amdgcn_exp2f(sacc[4*a+1]-mx), p2=__builtin_amdgcn_exp2f(sacc[4*a+2]-mx), p3=__builtin_amdgcn_exp2f(sacc[4*a+3]-mx);
        const u32 u0=pk2(p0,p1), u1=pk2(p2,p3);
        ls+=(p0+p1)+(p2+p3);
        *(LAS v2u*)(L+L_P+l31*256+16*((4*w+a)^fl)+8*hh)=v2u{u0,u1};
      }
      ls=sw32add(ls,ls);
      if(lane<32) *(LAS float*)(L+L_LS+w*128+lane*4)=ls;
      BAR();
      STAMP(5);
      LOAD_VF();
    } else {
      const int wv=w-4;
      const u64 t_q=rt();
      wait_t(t_start,T_FLAG1);
      _Pragma("unroll 1") for(int sp=0;sp<SPIN_CAP;++sp){ FENCE();
        if(__all(flag_ok(ld128<AUX_SC1>(rws,WS_FLAG1+xc*1024+wv*256+(lane&15)*16),want))) break;
        __builtin_amdgcn_s_sleep(SLP);
      }
      ESTAMP(0);
      wait_t(t_q,D_G);
      BAR();
      GATHER_QA();
      ESTAMP(1);
      BAR();
      LOAD_VF();
      BAR();
      BAR();
      mx=0.f;
      BAR();
    }
    {
      v4u pb[8];
      #pragma unroll
      for(int ks=0;ks<8;ks++) pb[ks]=lrd128(L+L_P+l31*256+16*((2*ks+hh)^fl));
      const float lsum=(*(LAS float*)(L+L_LS+l31*4)+*(LAS float*)(L+L_LS+128+l31*4))+(*(LAS float*)(L+L_LS+256+l31*4)+*(LAS float*)(L+L_LS+384+l31*4));
      SB();
      f32x16 o[2];
      #pragma unroll
      for(int i=0;i<16;i++){ o[0][i]=0.f; o[1][i]=0.f; }
      #pragma unroll
      for(int t=0;t<2;t++)
        #pragma unroll
        for(int ks=0;ks<8;ks++) o[t]=mfma32(vf[ks][t],pb[ks],o[t]);
      const float inv=__builtin_amdgcn_rcpf(lsum);
      lse=mx+__builtin_amdgcn_logf(lsum);
      STAMP(6);
      #pragma unroll
      for(int t=0;t<2;t++)
        #pragma unroll
        for(int a=0;a<4;a++)
          *(LAS v2u*)(L+L_CKV+l31*1024+256*sgm+16*((8*(w&1)+4*t+a)^fl)+8*hh)=v2u{pk2(o[t][4*a]*inv,o[t][4*a+1]*inv),pk2(o[t][4*a+2]*inv,o[t][4*a+3]*inv)};
      v4u ov[4];
      #pragma unroll
      for(int i=0;i<4;i++){ const int head=8*i+(lane>>3); ov[i]=lrd128(L+L_CKV+head*1024+256*sgm+16*((8*(w&1)+(lane&7))^fsw(head))); }
      SB();
      #pragma unroll
      for(int i=0;i<4;i++){ const int head=8*i+(lane>>3); st128(rws,WS_PO+(head*4+sgm)*PO_STRIDE+(ab*32+as)*256+(w&1)*128+(lane&7)*16,ov[i]); }
    }
    STAMP(7);
    VMW(0);
    BAR();
    if(w==0 && lane<32) st128(rws,WS_LSE+LSE_OFF,v4u{__float_as_uint(lse),tag,__float_as_uint(lse),(u32)(want>>32)^0x5A5A5A5Au});
    STAMP(11);
  }
  STAMP(15);
}

extern "C" void kernel_launch(void* const* d_in,const int* in_sizes,int n_in,void* d_out,int out_size,void* d_ws,size_t ws_size,hipStream_t stream){
  (void)hipFuncSetAttribute((const void*)mla_mlp_kernel,hipFuncAttributeMaxDynamicSharedMemorySize,LDS_BYTES);
  mla_mlp_kernel<<<256,512,LDS_BYTES,stream>>>((const u16*)d_in[0],(const u16*)d_in[1],(const u16*)d_in[2],(const u16*)d_in[3],(const u16*)d_in[4],(const u16*)d_in[5],
     (const u16*)d_in[7],(const u16*)d_in[8],(const u16*)d_in[9],(const u16*)d_in[10],(u16*)d_out,(unsigned char*)d_ws);
}
```

```cpp
#include <hip/hip_runtime.h>
typedef unsigned short u16; typedef unsigned u32; typedef unsigned long long u64;
#define GAS __attribute__((address_space(1)))
#define LAS __attribute__((address_space(3)))
typedef u32 v4u __attribute__((ext_vector_type(4)));
typedef u32 v2u __attribute__((ext_vector_type(2)));
typedef float f32x2 __attribute__((ext_vector_type(2)));
typedef float f32x4 __attribute__((ext_vector_type(4)));
typedef float f32x16 __attribute__((ext_vector_type(16)));
typedef __bf16 bf16x8 __attribute__((ext_vector_type(8)));
typedef __bf16 bf16x2 __attribute__((ext_vector_type(2)));
typedef short s16x4 __attribute__((ext_vector_type(4)));
typedef GAS u64 gu64;
typedef LAS unsigned char lds8;
#define RLX_AGENT __ATOMIC_RELAXED, __HIP_MEMORY_SCOPE_AGENT
#define DI __device__ __forceinline__

constexpr int NB=4, NH=32, SK=4096, HD=128, DM=512, DC=512, FF=1792;
constexpr int WS_EPOCH=0, WS_FLAG1=8192, WS_Q1=32768, WS_HM=WS_Q1+524288, WS_QA=WS_HM+262144, WS_LSE=WS_QA+262144, WS_OUT=WS_LSE+65536,
              WS_PO=WS_OUT+(1<<20), WS_END=WS_PO+(4<<20);
constexpr int L_CKV=0, L_QAS=131072, L_P=L_QAS, L_MX=L_QAS+8192, L_LS=L_QAS+8704, L_AWQ=L_QAS, L_AXN=126*1024, L_AEP=L_AXN+1536;
constexpr int L_WUK=0, L_WO=0, L_W2=81920, L_WUV=98304, L_W1=131072, L_XN=147456, L_QS=148736, L_CL=149760, L_H2=151040, L_RED=151552, L_EP=152064, L_QL=L_WUV, L_WG=156672, L_CX=157184;
constexpr int LDS_BYTES=163840;
constexpr u32 SENT=0xFFFFFFFFu;
constexpr int SPIN_CAP=1<<20;
constexpr int AUX_NT=2, AUX_SC1=16;
constexpr int CKV_INFL=12, INFL0=12, T_CKV0=120, T_FLAG1=240, D_G=200, D_E1=110, WO_1=4, T_TAIL=460, T_GUN=620, D_E4=50;

DI u64 rt(){ return __builtin_amdgcn_s_memrealtime(); }
DI __amdgpu_buffer_rsrc_t mkrs(const void* p, int bytes){ return __builtin_amdgcn_make_buffer_rsrc((void*)p,0,bytes,0x00020000); }
template<int AUX> DI v4u ld128(__amdgpu_buffer_rsrc_t rs,int off){ return __builtin_amdgcn_raw_buffer_load_b128(rs,off,0,AUX); }
DI void st128(__amdgpu_buffer_rsrc_t rs,int off,v4u v){ __builtin_amdgcn_raw_buffer_store_b128(v,rs,off,0,AUX_SC1); }
DI f32x4 mfma16(v4u a,v4u b,f32x4 c){ return __builtin_amdgcn_mfma_f32_16x16x32_bf16(__builtin_bit_cast(bf16x8,a),__builtin_bit_cast(bf16x8,b),c,0,0,0); }
DI f32x16 mfma32(v4u a,v4u b,f32x16 c){ return __builtin_amdgcn_mfma_f32_32x32x16_bf16(__builtin_bit_cast(bf16x8,a),__builtin_bit_cast(bf16x8,b),c,0,0,0); }
DI v2u trd(lds8* p){ return __builtin_bit_cast(v2u,__builtin_amdgcn_ds_read_tr16_b64_v4i16((LAS s16x4*)p)); }
DI v4u trd2(lds8* p0,lds8* p1){ v2u a=trd(p0), b=trd(p1); return v4u{a.x,a.y,b.x,b.y}; }
DI v4u lrd128(lds8* p){ return *(LAS v4u*)p; }
DI u32 pk2(float lo,float hi){ f32x2 f={lo,hi}; return __builtin_bit_cast(u32,__builtin_convertvector(f,bf16x2)); }
DI float rbf(float x){ return __uint_as_float(pk2(x,0.f)<<16); }
DI float vmax(float a,float b){ return fmaxf(a,b); }
DI float vmax3(float a,float b,float c){ return fmaxf(fmaxf(a,b),c); }
DI float blo(u32 w){ return __uint_as_float(w<<16); }
DI float bhi(u32 w){ return __uint_as_float(w&0xFFFF0000u); }
DI u32 nos(u32 w){ return w<0xFFFFFFFEu? w:0xFFFFFFFEu; }
DI bool ok4(v4u v){ return (v.x!=SENT)&(v.y!=SENT)&(v.z!=SENT)&(v.w!=SENT); }
#define BAR() asm volatile("s_waitcnt lgkmcnt(0)\n\ts_barrier" ::: "memory")
#define VMW(n) asm volatile("s_waitcnt vmcnt(%0)" :: "i"(n) : "memory")
#define FENCE() asm volatile("" ::: "memory")
#define SB() __builtin_amdgcn_sched_barrier(0)
template<int CTRL> DI float dpp(float x){ return __builtin_bit_cast(float,__builtin_amdgcn_mov_dpp(__builtin_bit_cast(int,x),CTRL,0xf,0xf,true)); }
constexpr int DXOR1=0xB1, DXOR2=0x4E, DHMIR=0x141, DROR8=0x128, DROR4=0x124;
DI float sw32add(float a,float b){ auto s=__builtin_amdgcn_permlane32_swap(__float_as_uint(a),__float_as_uint(b),false,false); return __uint_as_float(s[0])+__uint_as_float(s[1]); }
DI float sw16add(float a,float b){ auto s=__builtin_amdgcn_permlane16_swap(__float_as_uint(a),__float_as_uint(b),false,false); return __uint_as_float(s[0])+__uint_as_float(s[1]); }
DI float sw32max(float a){ auto s=__builtin_amdgcn_permlane32_swap(__float_as_uint(a),__float_as_uint(a),false,false); return vmax(__uint_as_float(s[0]),__uint_as_float(s[1])); }
DI float sw16max(float a){ auto s=__builtin_amdgcn_permlane16_swap(__float_as_uint(a),__float_as_uint(a),false,false); return vmax(__uint_as_float(s[0]),__uint_as_float(s[1])); }
DI float wave_sum(float v){ v+=dpp<DXOR1>(v); v+=dpp<DXOR2>(v); v+=dpp<DHMIR>(v); v+=dpp<DROR8>(v); v=sw16add(v,v); v=sw32add(v,v); return v; }
DI void dma16(const void* gbase,u32 voff,u32 ldsb){
  u32 keep;
  asm volatile("s_mov_b32 %0, m0\n\ts_mov_b32 m0, %3\n\ts_nop 0\n\tglobal_load_lds_dwordx4 %1, %2 nt\n\ts_mov_b32 m0, %0" : "=&s"(keep) : "v"(voff),"s"(gbase),"s"(ldsb) : "memory");
}
DI void dma16c(const void* gbase,u32 voff,u32 ldsb){
  u32 keep;
  asm volatile("s_mov_b32 %0, m0\n\ts_mov_b32 m0, %3\n\ts_nop 0\n\tglobal_load_lds_dwordx4 %1, %2\n\ts_mov_b32 m0, %0" : "=&s"(keep) : "v"(voff),"s"(gbase),"s"(ldsb) : "memory");
}
DI constexpr int fsw(int row){ return ((row&3)<<2)|((row>>2)&3); }
DI bool flag_ok(v4u f,u64 want){ return (f.x==(u32)want)&(f.y==(u32)(want>>32))&(f.z==(u32)want)&(f.w==(u32)(want>>32)); }
DI void wait_t(u64 t0,int ticks){ while(rt()-t0<(u64)ticks) __builtin_amdgcn_s_sleep(1); }
DI void ln_row(v4u xr,int lane,int tq,lds8* dst){
  float v[8]={blo(xr.x),bhi(xr.x),blo(xr.y),bhi(xr.y),blo(xr.z),bhi(xr.z),blo(xr.w),bhi(xr.w)};
  const float mean=wave_sum(((v[0]+v[1])+(v[2]+v[3]))+((v[4]+v[5])+(v[6]+v[7])))*(1.f/DM);
  float qq=0.f;
  #pragma unroll
  for(int i=0;i<8;i++){ const float d=v[i]-mean; qq+=d*d; }
  const float var=wave_sum(qq)*(1.f/DM);
  const float mb=rbf(mean), isd=__builtin_amdgcn_rcpf(rbf(__builtin_amdgcn_sqrtf(rbf(rbf(var)+1e-5f))));
  float y[8];
  #pragma unroll
  for(int i=0;i<8;i++) y[i]=rbf(v[i]-mb)*isd;
  if((lane>>4)==tq) *(LAS v4u*)(dst+16*(lane&15))=v4u{pk2(y[0],y[1]),pk2(y[2],y[3]),pk2(y[4],y[5]),pk2(y[6],y[7])};
}

extern "C" __global__ void __launch_bounds__(512,2) mla_mlp_kernel(const u16* __restrict__ x,const u16* __restrict__ ckv,const u16* __restrict__ wq,const u16* __restrict__ wuk,
    const u16* __restrict__ wuv,const u16* __restrict__ wo,const u16* __restrict__ w1,const u16* __restrict__ b1,const u16* __restrict__ w2,const u16* __restrict__ b2,
    u16* __restrict__ out,unsigned char* __restrict__ ws){
  extern __shared__ __attribute__((aligned(16))) unsigned char smem[];
  lds8* L=(lds8*)smem;
  const u32 Lb=(u32)(size_t)L;
  const int tid=threadIdx.x, lane=tid&63, w=__builtin_amdgcn_readfirstlane(tid>>6);
  const int wg=blockIdx.x, xc=wg&7, j=wg>>3, jj=j&15;
  const bool isF=j<16;
  const int fi=xc*16+jj, th=fi>>2, tq=fi&3;
  const int g=lane>>4, q4=(lane&15)>>2, p4=lane&3, hh=lane>>5, l31=lane&31;
  if(isF && (w==5 || w==6)){
    u64 pc; u32 back, size;
    asm volatile("s_getpc_b64 %0\n.Lmla_pc_anchor:\n\ts_mov_b32 %1, .Lmla_pc_anchor-mla_mlp_kernel\n\ts_mov_b32 %2, .Lmla_code_end-mla_mlp_kernel" : "=s"(pc),"=s"(back),"=s"(size));
    const u32 off=(u32)((j+16*(6-w))*1024+16*lane);
    dma16c((const void*)(pc-back),off+16<=size?off:0u,Lb+L_W1+(w-5)*1024);
  }
  const __amdgpu_buffer_rsrc_t rws=mkrs(ws,WS_END);
  asm volatile("" :: "s"(x),"s"(ckv),"s"(wq),"s"(wuk),"s"(ws) : "memory");
  const u64 t_start=rt();
  const u32 rowoff=(u32)((128*tq+16*w+g)*8192+th*256);
#define DMA_T(p,dst,i) dma16(p,rowoff+(u32)(4*(i)*8192)+16*((lane&15)^(2*(g|(((i)>>1)<<2)))),Lb+(dst)+(16*w+4*(i))*256)
  const int troff=(8*g+q4)*256+(w^(q4|((g&1)<<2)))*32+p4*8;
  const int r0=4*g+q4;
  if(isF){
    const bool mlp = fi<FF/16;
    if(w==7){
      v2u e2;
      st128(rws,WS_QA+g*32768+th*1024+tq*256+(lane&15)*16,v4u{SENT,SENT,SENT,SENT});
      st128(rws,WS_QA+131072+g*32768+th*1024+tq*256+(lane&15)*16,v4u{SENT,SENT,SENT,SENT});
      asm volatile("global_load_dwordx2 %0, %1, %2 sc1" : "=v"(e2) : "v"(0), "s"(ws) : "memory");
      #pragma unroll
      for(int i=0;i<4;i++) dma16(wuk,rowoff+(u32)(4*i*8192)+16*((lane&15)^(4*i+g)),Lb+L_WUK+(16*w+4*i)*256);
      asm volatile("s_waitcnt vmcnt(4)" : "+v"(e2) :: "memory");
      const u64 wn=(((u64)e2.y<<32)|e2.x)+1; if(lane<8) __hip_atomic_store((gu64*)(ws+WS_FLAG1)+lane*128+fi,wn,RLX_AGENT);
      if(lane==0) *(LAS u64*)(L+L_EP)=wn;
      VMW(1);
    } else {
      #pragma unroll
      for(int i=0;i<4;i++) dma16(wuk,rowoff+(u32)(4*i*8192)+16*((lane&15)^(4*i+g)),Lb+L_WUK+(16*w+4*i)*256);
      VMW(0);
    }
    v4u auk[4];
    #pragma unroll
    for(int k=0;k<4;k++){
      lds8* a=L+L_WUK+(16*w+(lane&15))*256+8*(g&1);
      const v2u lo=*(LAS v2u*)(a+16*((4*k+(g>>1))^(lane&15))), hi=*(LAS v2u*)(a+16*((4*k+2+(g>>1))^(lane&15)));
      auk[k]=v4u{lo.x,lo.y,hi.x,hi.y};
    }
    BAR();
    const v2u ep=*(LAS v2u*)(L+L_EP);
    const u32 tag=__builtin_amdgcn_readfirstlane(ep.x);
    const u64 want=((u64)__builtin_amdgcn_readfirstlane(ep.y)<<32)|tag;
    { const u64 t1=rt(); wait_t(t1,D_E1); }
    {
      v4u qf[2];
      const int qo=WS_Q1+(th*4+hh)*4096+w*512+l31*16;
      _Pragma("unroll 1") for(int sp=0;sp<SPIN_CAP;++sp){ FENCE();
        qf[0]=ld128<AUX_SC1>(rws,qo);
        qf[1]=ld128<AUX_SC1>(rws,qo+8192);
        if(__all((qf[0].y==tag)&(qf[0].w==tag)&(qf[1].y==tag)&(qf[1].w==tag))) break;
        __builtin_amdgcn_s_sleep(1);
      }
      float s0=__uint_as_float(qf[0].x)+__uint_as_float(qf[1].x), s1=__uint_as_float(qf[0].z)+__uint_as_float(qf[1].z);
      s0=sw32add(s0,s0); s1=sw32add(s1,s1);
      if(lane<32) *(LAS u32*)(L+L_QL+(w>>1)*256+(lane>>3)*64+p4*16+(w&1)*8+((lane>>2)&1)*4)=pk2(s0,s1);
    }
    BAR();
    {
      v4u qb[4];
      #pragma unroll
      for(int k=0;k<4;k++) qb[k]=lrd128(L+L_QL+k*256+g*64+p4*16);
      SB();
      f32x4 acc={0,0,0,0};
      #pragma unroll
      for(int k=0;k<4;k++) acc=mfma16(auk[k],qb[k],acc);
      const float SC=0.08838834764831845f*1.4426950408889634f;
      if((lane&15)<4) *(LAS v2u*)(L+L_QS+(lane&15)*256+(16*w+4*g)*2)=v2u{nos(pk2(acc[0]*SC,acc[1]*SC)),nos(pk2(acc[2]*SC,acc[3]*SC))};
    }
    BAR();
    if(w<2){ v4u v=lrd128(L+L_QS+lane*16); st128(rws,WS_QA+w*131072+g*32768+th*1024+tq*256+(lane&15)*16,v); }
    u32 b1raw[4];
    #pragma unroll
    for(int r=0;r<4;r++) b1raw[r]=__builtin_amdgcn_raw_buffer_load_b16(mkrs(b1,FF*2),(16*fi+4*g+r)*2,0,0);
    u32 xraw=__builtin_amdgcn_raw_buffer_load_b16(mkrs(x,NB*DM*2),(p4*DM+4*fi+q4)*2,0,0);
    u32 b2raw=__builtin_amdgcn_raw_buffer_load_b16(mkrs(b2,DM*2),(4*fi+q4)*2,0,0);
    SB();
#define DMA_WO(i) dma16(wo,(u32)((th*128+8*(i)+w)*1024+16*(lane^(2*((w&3)|(((i)&1)<<2))))),Lb+L_WO+(8*(i)+w)*1024)
    #pragma unroll
    for(int i=0;i<10;i++){ if(i>=WO_1) VMW(WO_1-1); DMA_WO(i); }
    wait_t(t_start,T_TAIL);
    v4u xr;
    asm volatile("global_load_dwordx4 %0, %1, %2" : "=v"(xr) : "v"((w&3)*1024+16*lane), "s"(x) : "memory");
    const int thm=mlp?th:0, fim=mlp?fi:0;
    #pragma unroll
    for(int i=0;i<2;i++){ const int r=8*i+(lane>>3); dma16(w1,(u32)((128*tq+16*w+r)*(FF*2)+thm*128+16*((lane&7)^(2*(((r>>1)&1)|(i<<1))))),Lb+L_W1+(16*w+8*i)*128); }
    #pragma unroll
    for(int i=0;i<4;i++) DMA_T(wuv,L_WUV,i);
    #pragma unroll
    for(int i=0;i<2;i++){ const int r=8*i+w; dma16(w2,(u32)((16*fim+r)*1024+16*(lane^(2*(r&7)))),Lb+L_W2+r*1024); }
    v4u rwo[6];
    #pragma unroll
    for(int i=0;i<6;i++) asm volatile("global_load_dwordx4 %0, %1, %2 nt" : "=v"(rwo[i]) : "v"((th*128+80+8*i+w)*1024+16*lane), "s"(wo) : "memory");
    asm volatile("s_waitcnt vmcnt(12)" : "+v"(xr) :: "memory");
    if(w<4) ln_row(xr,lane,tq,L+L_XN+w*320);
    BAR();
    if(mlp && w<4){
      v4u a1[4], bq[4];
      #pragma unroll
      for(int ks=0;ks<4;ks++){ lds8* a=L+L_W1+(32*ks+8*g+q4)*128+32*(w^((q4>>1)|((g&1)<<1)))+p4*8; a1[ks]=trd2(a,a+512); bq[ks]=lrd128(L+L_XN+p4*320+(32*ks+8*g)*2); }
      SB();
      f32x4 acc={0,0,0,0};
      #pragma unroll
      for(int ks=0;ks<4;ks++) acc=mfma16(a1[ks],bq[ks],acc);
      if((lane&15)<4){
        st128(rws,WS_HM+fi*2048+w*512+g*128+p4*16,v4u{__float_as_uint(acc[0]),tag,__float_as_uint(acc[1]),tag});
        st128(rws,WS_HM+fi*2048+w*512+g*128+64+p4*16,v4u{__float_as_uint(acc[2]),tag,__float_as_uint(acc[3]),tag}); }
    }
    VMW(6);
    BAR();
    v4u auv[4]; v2u aw2[4];
    #pragma unroll
    for(int k=0;k<4;k++){ lds8* a=L+L_WUV+k*8192+troff; auv[k]=trd2(a,a+1024); }
    #pragma unroll
    for(int t=0;t<4;t++) aw2[t]=trd(L+L_W2+r0*1024+(w>>1)*256+32*((4*(w&1)+t)^(r0&7))+p4*8);
    BAR();
    if(mlp && w==4){
      v4u h0,h1;
      _Pragma("unroll 1") for(int sp=0;sp<SPIN_CAP;++sp){ FENCE();
        h0=ld128<AUX_SC1>(rws,WS_HM+(th*4+q4)*2048+tq*512+g*128+p4*16);
        h1=ld128<AUX_SC1>(rws,WS_HM+(th*4+q4)*2048+tq*512+g*128+64+p4*16);
        if(__all((h0.y==tag)&(h0.w==tag)&(h1.y==tag)&(h1.w==tag))) break;
        __builtin_amdgcn_s_sleep(4);
      }
      float hv[4]={__uint_as_float(h0.x),__uint_as_float(h0.z),__uint_as_float(h1.x),__uint_as_float(h1.z)};
      #pragma unroll
      for(int r=0;r<4;r++){ float sv=hv[r]; sv+=dpp<DROR4>(sv); sv+=dpp<DROR8>(sv); float h=rbf(rbf(sv)+blo(b1raw[r])); h=h>0.f?h:0.f; hv[r]=h*h; }
      *(LAS v2u*)(L+L_H2+lane*8)=v2u{pk2(hv[0],hv[1]),pk2(hv[2],hv[3])};
    }
    asm volatile("s_waitcnt vmcnt(0)" : "+v"(rwo[0]),"+v"(rwo[1]),"+v"(rwo[2]),"+v"(rwo[3]),"+v"(rwo[4]),"+v"(rwo[5]) :: "memory");
    #pragma unroll
    for(int i=0;i<6;i++) *(LAS v4u*)(L+L_WO+(80+8*i+w)*1024+16*(lane^(2*((w&3)|((i&1)<<2)))))=rwo[i];
    v4u awo[4][4];
#define LOAD_AWO(ks) _Pragma("unroll") for(int t=0;t<4;t++){ lds8* a=L+L_WO+(32*(ks)+8*g+q4)*1024+(w>>1)*256+p4*8+32*((4*(w&1)+t)^(q4|((g&1)<<2))); awo[ks][t]=trd2(a,a+4096); }
    LOAD_AWO(0); LOAD_AWO(1);
    SB();
    asm volatile("" : "+v"(xraw), "+v"(b2raw));
    const float xb_res=blo(xraw)+blo(b2raw);
    f32x4 oacc[4];
    if(w==0){
      wait_t(t_start,T_GUN);
      v4u v0,v1;
      const u32 tagh=(u32)(want>>32)^0x5A5A5A5Au;
      _Pragma("unroll 1") for(int sp=0;sp<SPIN_CAP;++sp){ FENCE();
        v0=ld128<AUX_SC1>(rws,WS_LSE+th*2048+lane*16);
        v1=ld128<AUX_SC1>(rws,WS_LSE+th*2048+1024+lane*16);
        if(__all((v0.y==tag)&(v1.y==tag)&(v0.w==tagh)&(v1.w==tagh)&(v0.x==v0.z)&(v1.x==v1.z))) break; __builtin_amdgcn_s_sleep(1);
      }
      *(LAS u32*)(L+L_WG+hh*128+(l31&7)*16+(l31>>3)*4)=v0.x;
      *(LAS u32*)(L+L_WG+256+hh*128+(l31&7)*16+(l31>>3)*4)=v1.x;
    }
    BAR();
    {
      const int b=w>>1, half=w&1, slo=lane>>3, q=lane&7;
      const int po_off=WS_PO+fi*32768+(b*32+slo)*256+half*128+q*16;
      v4u po[4];
      #pragma unroll
      for(int i=0;i<4;i++) po[i]=ld128<AUX_SC1>(rws,po_off+i*2048);
      SB();
      LOAD_AWO(2); LOAD_AWO(3);
      #pragma unroll
      for(int t=0;t<4;t++) oacc[t]=f32x4{0,0,0,0};
      if(mlp){
        const v2u h2=*(LAS v2u*)(L+L_H2+lane*8);
        #pragma unroll
        for(int t=0;t<4;t++) oacc[t]=mfma16(v4u{aw2[t].x,aw2[t].y,0,0},v4u{h2.x,h2.y,0,0},oacc[t]);
      }
      const f32x4 lf=*(LAS f32x4*)(L+L_WG+b*128+slo*16);
      float M=vmax(vmax3(lf[0],lf[1],lf[2]),lf[3]);
      M=vmax(M,dpp<DROR8>(M)); M=sw16max(M); M=sw32max(M);
      f32x4 e;
      #pragma unroll
      for(int i=0;i<4;i++) e[i]=__builtin_amdgcn_exp2f(lf[i]-M);
      float Ls=(e[0]+e[1])+(e[2]+e[3]);
      Ls+=dpp<DROR8>(Ls); Ls=sw16add(Ls,Ls); Ls=sw32add(Ls,Ls);
      e*=__builtin_amdgcn_rcpf(Ls);
      SB();
      float a[8];
      #pragma unroll
      for(int k=0;k<8;k++) a[k]=0.f;
      #pragma unroll
      for(int i=0;i<4;i++){
        a[0]+=e[i]*blo(po[i].x); a[1]+=e[i]*bhi(po[i].x); a[2]+=e[i]*blo(po[i].y); a[3]+=e[i]*bhi(po[i].y);
        a[4]+=e[i]*blo(po[i].z); a[5]+=e[i]*bhi(po[i].z); a[6]+=e[i]*blo(po[i].w); a[7]+=e[i]*bhi(po[i].w);
      }
      float c4[4], c2[2];
      #pragma unroll
      for(int k=0;k<4;k++) c4[k]=sw32add(a[k],a[k+4]);
      c2[0]=sw16add(c4[0],c4[2]); c2[1]=sw16add(c4[1],c4[3]);
      const bool b3=(lane>>3)&1;
      const float keep=b3?c2[1]:c2[0], send=b3?c2[0]:c2[1];
      const float r=keep+dpp<DROR8>(send);
      const int col=8*q+4*hh+2*((lane>>4)&1)+(b3?1:0);
      *(LAS u16*)(L+L_CL+b*320+(half*64+col)*2)=(u16)pk2(r,0.f);
    }
    BAR();
    {
      v4u bc[4];
      #pragma unroll
      for(int k=0;k<4;k++) bc[k]=lrd128(L+L_CL+p4*320+(32*k+8*g)*2);
      SB();
      f32x4 acc={0,0,0,0};
      #pragma unroll
      for(int k=0;k<4;k++) acc=mfma16(auv[k],bc[k],acc);
      if((lane&15)<4) *(LAS v2u*)(L+L_CX+p4*320+(16*w+4*g)*2)=v2u{pk2(acc[0],acc[1]),pk2(acc[2],acc[3])};
    }
    BAR();
    {
      v4u bc[4];
      #pragma unroll
      for(int ks=0;ks<4;ks++) bc[ks]=lrd128(L+L_CX+p4*320+(32*ks+8*g)*2);
      SB();
      #pragma unroll
      for(int ks=0;ks<4;ks++)
        #pragma unroll
        for(int t=0;t<4;t++) oacc[t]=mfma16(awo[ks][t],bc[ks],oacc[t]);
      f32x4 v;
      #pragma unroll
      for(int r=0;r<4;r++){ const float lo=(q4&1)?oacc[1][r]:oacc[0][r], hi=(q4&1)?oacc[3][r]:oacc[2][r]; v[r]=(q4&2)?hi:lo; }
      st128(rws,WS_OUT+(16*w+4*q4+g)*8192+fi*64+p4*16,v4u{pk2(v[0],v[1]),tag,pk2(v[2],v[3]),tag});
    }
    { const u64 t1=rt(); wait_t(t1,D_E4); }
    v4u pv;
    _Pragma("unroll 1") for(int sp=0;sp<SPIN_CAP;++sp){ FENCE();
      pv=ld128<AUX_SC1>(rws,WS_OUT+fi*8192+tid*16);
      if(__all((pv.y==tag)&(pv.w==tag))) break;
      __builtin_amdgcn_s_sleep(1);
    }
    const float u0=sw32add(blo(pv.x),blo(pv.z)), u1=sw32add(bhi(pv.x),bhi(pv.z));
    float v=sw16add(u0,u1);
    v+=dpp<DROR4>(v); v+=dpp<DROR8>(v);
    if((lane&12)==0) *(LAS float*)(L+L_RED+w*64+(g*4+p4)*4)=v;
    BAR();
    if(tid<16){
      float a=0.f;
      #pragma unroll
      for(int ww=0;ww<8;ww++) a+=*(LAS float*)(L+L_RED+ww*64+tid*4);
      out[(tid&3)*DM+4*fi+(tid>>2)]=(u16)pk2(xb_res+a,0.f);
    }
    if(wg==0 && tid==0) __hip_atomic_store((gu64*)(ws+WS_EPOCH),want,RLX_AGENT);
    asm volatile(".Lmla_code_end:");
  } else {
    const int ab=fi>>5, as=fi&31;
    const int fl=fsw(l31);
    const int blk=(lane>>4)&1, sgm=w>>1;
    const u16* gb=ckv+((size_t)ab*SK+as*128+32*(w&3))*DC;
#define DMA_CKV(r) dma16(gb+(r)*DC,(u32)((lane*16)^(16*fsw(r))),Lb+L_CKV+(32*w+(r))*1024)
    #pragma unroll
    for(int i=0;i<4;i++) DMA_T(wq,L_AWQ,i);
    if(w<4){
      v2u e2;
      asm volatile("global_load_dwordx2 %0, %1, %2 sc1" : "=v"(e2) : "v"(0), "s"(ws) : "memory");
      wait_t(t_start,T_CKV0);
      #pragma unroll
      for(int r=0;r<INFL0;r++) DMA_CKV(r);
      asm volatile("s_waitcnt vmcnt(%1)" : "+v"(e2) : "i"(INFL0) : "memory");
      if(w==0 && lane==0) *(LAS u64*)(L+L_AEP)=(((u64)e2.y<<32)|e2.x)+1;
    } else {
      v4u xr;
      asm volatile("global_load_dwordx4 %0, %1, %2\n\ts_waitcnt vmcnt(0)" : "=v"(xr) : "v"((w&3)*1024+16*lane), "s"(x) : "memory");
      ln_row(xr,lane,tq,L+L_AXN+(w-4)*320);
    }
    BAR();
    u32 tag; u64 want;
    {
      v4u aq[4], bq[4];
      #pragma unroll
      for(int ks=0;ks<4;ks++){ lds8* a=L+L_AWQ+ks*8192+troff; aq[ks]=trd2(a,a+1024); bq[ks]=lrd128(L+L_AXN+p4*320+(32*ks+8*g)*2); }
      const v2u e=*(LAS v2u*)(L+L_AEP);
      SB();
      f32x4 acc={0,0,0,0};
      #pragma unroll
      for(int ks=0;ks<4;ks++) acc=mfma16(aq[ks],bq[ks],acc);
      tag=__builtin_amdgcn_readfirstlane(e.x);
      if((lane&15)<8) st128(rws,WS_Q1+fi*4096+w*512+g*128+(lane&7)*16,v4u{__float_as_uint(q4?acc[2]:acc[0]),tag,__float_as_uint(q4?acc[3]:acc[1]),tag});
      want=((u64)__builtin_amdgcn_readfirstlane(e.y)<<32)|tag;
    }
    BAR();
    v4u vf[8][2];
#define LOAD_VF() do{ _Pragma("unroll") for(int ks=0;ks<8;ks++) _Pragma("unroll") for(int t=0;t<2;t++){ \
        const int ch=8*(w&1)+4*t+2*blk+(p4>>1); \
        lds8* a0=L+L_CKV+(16*ks+8*hh+q4)*1024+256*sgm+16*(ch^((q4<<2)|(2*hh)))+8*(p4&1); \
        lds8* a1=L+L_CKV+(16*ks+8*hh+4+q4)*1024+256*sgm+16*(ch^((q4<<2)|(2*hh+1)))+8*(p4&1); \
        vf[ks][t]=trd2(a0,a1); } SB(); }while(0)
#define GATHER_ISSUE() v4u qa[4]; const int qo=WS_QA+(xc&1)*131072+ab*32768+4*w*1024+16*lane; \
      _Pragma("unroll") for(int i=0;i<4;i++) qa[i]=ld128<AUX_SC1>(rws,qo+i*1024)
#define GATHER_QA() do{ \
      _Pragma("unroll 1") for(int sp=0;sp<SPIN_CAP;++sp){ u32 bad=0; \
        _Pragma("unroll") for(int i=0;i<4;i++) bad|=(__all(ok4(qa[i]))?0u:1u)<<i; \
        if(!bad) break; \
        __builtin_amdgcn_s_sleep(1); FENCE(); \
        _Pragma("unroll") for(int i=0;i<4;i++) if((bad>>i)&1) qa[i]=ld128<AUX_SC1>(rws,qo+i*1024); } \
      _Pragma("unroll") for(int i=0;i<4;i++) *(LAS v4u*)(L+L_QAS+(4*w+i)*1024+((lane*16)^(16*((i<<2)|(w&3)))))=qa[i]; }while(0)
    float mx, lse;
    if(w<4){
      #pragma unroll
      for(int r=INFL0;r<32;r++){
        if(r>=CKV_INFL) VMW(CKV_INFL-1);
        DMA_CKV(r);
      }
      BAR();
      GATHER_ISSUE();
      VMW(4);
      v4u af[32];
      #pragma unroll
      for(int k=0;k<32;k++) af[k]=lrd128(L+L_CKV+(32*w+l31)*1024+256*(k>>3)+16*((2*(k&7)+hh)^fl));
      SB();
      GATHER_QA();
      BAR();
      f32x16 sacc;
      #pragma unroll
      for(int i=0;i<16;i++) sacc[i]=0.f;
      v4u bf[12];
      #pragma unroll
      for(int k=0;k<12;k++) bf[k]=lrd128(L+L_QAS+l31*1024+256*(k>>3)+16*((2*(k&7)+hh)^fl));
      SB();
      #pragma unroll
      for(int k=0;k<32;k++){
        sacc=mfma32(af[k],bf[k%12],sacc);
        if(k+12<32) bf[k%12]=lrd128(L+L_QAS+l31*1024+256*((k+12)>>3)+16*((2*((k+12)&7)+hh)^fl));
        SB();
      }
      float mloc=vmax3(vmax3(vmax3(sacc[0],sacc[1],sacc[2]),vmax3(sacc[3],sacc[4],sacc[5]),vmax3(sacc[6],sacc[7],sacc[8])),
                       vmax3(vmax3(sacc[9],sacc[10],sacc[11]),vmax3(sacc[12],sacc[13],sacc[14]),sacc[15]),sacc[15]);
      mloc=sw32max(mloc);
      BAR();
      if(lane<32) *(LAS float*)(L+L_MX+w*128+lane*4)=mloc;
      BAR();
      mx=vmax(vmax3(*(LAS float*)(L+L_MX+l31*4),*(LAS float*)(L+L_MX+128+l31*4),*(LAS float*)(L+L_MX+256+l31*4)),*(LAS float*)(L+L_MX+384+l31*4));
      float ls=0.f;
      #pragma unroll
      for(int a=0;a<4;a++){
        float p0=__builtin_amdgcn_exp2f(sacc[4*a]-mx), p1=__builtin_amdgcn_exp2f(sacc[4*a+1]-mx), p2=__builtin_amdgcn_exp2f(sacc[4*a+2]-mx), p3=__builtin_amdgcn_exp2f(sacc[4*a+3]-mx);
        const u32 u0=pk2(p0,p1), u1=pk2(p2,p3);
        ls+=(p0+p1)+(p2+p3);
        *(LAS v2u*)(L+L_P+l31*256+16*((4*w+a)^fl)+8*hh)=v2u{u0,u1};
      }
      ls=sw32add(ls,ls);
      if(lane<32) *(LAS float*)(L+L_LS+w*128+lane*4)=ls;
      BAR();
      LOAD_VF();
    } else {
      const int wv=w-4;
      const u64 t_q=rt();
      wait_t(t_start,T_FLAG1);
      _Pragma("unroll 1") for(int sp=0;sp<SPIN_CAP;++sp){ FENCE();
        if(__all(flag_ok(ld128<AUX_SC1>(rws,WS_FLAG1+xc*1024+wv*256+(lane&15)*16),want))) break;
        __builtin_amdgcn_s_sleep(4);
      }
      wait_t(t_q,D_G);
      BAR();
      GATHER_ISSUE();
      GATHER_QA();
      BAR();
      LOAD_VF();
      BAR();
      BAR();
      mx=0.f;
      BAR();
    }
    {
      v4u pb[8];
      #pragma unroll
      for(int ks=0;ks<8;ks++) pb[ks]=lrd128(L+L_P+l31*256+16*((2*ks+hh)^fl));
      const float lsum=(*(LAS float*)(L+L_LS+l31*4)+*(LAS float*)(L+L_LS+128+l31*4))+(*(LAS float*)(L+L_LS+256+l31*4)+*(LAS float*)(L+L_LS+384+l31*4));
      SB();
      f32x16 o[2];
      #pragma unroll
      for(int i=0;i<16;i++){ o[0][i]=0.f; o[1][i]=0.f; }
      #pragma unroll
      for(int t=0;t<2;t++)
        #pragma unroll
        for(int ks=0;ks<8;ks++) o[t]=mfma32(vf[ks][t],pb[ks],o[t]);
      const float inv=__builtin_amdgcn_rcpf(lsum);
      lse=mx+__builtin_amdgcn_logf(lsum);
      #pragma unroll
      for(int t=0;t<2;t++)
        #pragma unroll
        for(int a=0;a<4;a++)
          *(LAS v2u*)(L+L_CKV+l31*1024+256*sgm+16*((8*(w&1)+4*t+a)^fl)+8*hh)=v2u{pk2(o[t][4*a]*inv,o[t][4*a+1]*inv),pk2(o[t][4*a+2]*inv,o[t][4*a+3]*inv)};
      v4u ov[4];
      #pragma unroll
      for(int i=0;i<4;i++){ const int head=8*i+(lane>>3); ov[i]=lrd128(L+L_CKV+head*1024+256*sgm+16*((8*(w&1)+(lane&7))^fsw(head))); }
      SB();
      #pragma unroll
      for(int i=0;i<4;i++){ const int head=8*i+(lane>>3); st128(rws,WS_PO+(head*4+sgm)*32768+(ab*32+as)*256+(w&1)*128+(lane&7)*16,ov[i]); }
    }
    VMW(0);
    BAR();
    if(w==0 && lane<32) st128(rws,WS_LSE+lane*2048+fi*16,v4u{__float_as_uint(lse),tag,__float_as_uint(lse),(u32)(want>>32)^0x5A5A5A5Au});
  }
}

extern "C" void kernel_launch(void* const* d_in,const int* in_sizes,int n_in,void* d_out,int out_size,void* d_ws,size_t ws_size,hipStream_t stream){
  (void)hipFuncSetAttribute((const void*)mla_mlp_kernel,hipFuncAttributeMaxDynamicSharedMemorySize,LDS_BYTES);
  mla_mlp_kernel<<<256,512,LDS_BYTES,stream>>>((const u16*)d_in[0],(const u16*)d_in[1],(const u16*)d_in[2],(const u16*)d_in[3],(const u16*)d_in[4],(const u16*)d_in[5],
     (const u16*)d_in[7],(const u16*)d_in[8],(const u16*)d_in[9],(const u16*)d_in[10],(u16*)d_out,(unsigned char*)d_ws);
}
```
